```python
import torch

def _softmax(x, axis=-1):
    return torch.softmax(x, dim=axis)
def _gelu(x):
    return torch.nn.functional.gelu(x, approximate="tanh")
def _silu(x):
    return torch.nn.functional.silu(x)
def _sigmoid(x):
    return torch.sigmoid(x)
def _layernorm(x, axis=-1, eps=1e-5):
    m = x.mean(dim=axis, keepdim=True)
    v = x.var(dim=axis, keepdim=True, unbiased=False)
    return (x - m) / torch.sqrt(v + eps)
def _scatter_add(tgt, idx, src, axis=0):
    return tgt.clone().index_add_(axis, idx, src)
def _segment_sum(src, seg, n):
    out = torch.zeros((n,) + src.shape[1:], dtype=src.dtype)
    return out.index_add_(0, seg, src)
def _pad(x, pw):
    flat = tuple(v for pair in reversed(pw) for v in pair)
    return torch.nn.functional.pad(x, flat)

kernel_name = 'gated_mix__ssm_conv_h3__sliding_block__prompt'

def setup_inputs():

    _w = (lambda a: (lambda t: t.to(torch.bfloat16) if t.is_floating_point() else t)(torch.as_tensor(a)))
    def _A_setup():
        B, S, D = 1, 2048, 512
        ks = 4
        return {
            "q": _w((lambda *s: torch.randn(*s, dtype=torch.bfloat16) * 0.05)(B, S, D)),
            "k": _w((lambda *s: torch.randn(*s, dtype=torch.bfloat16) * 0.8)(B, S, D)),
            "v": _w((lambda *s: torch.randn(*s, dtype=torch.bfloat16) * 0.8)(B, S, D)),
            "shift_k": _w((lambda *s: torch.randn(*s, dtype=torch.bfloat16) * 0.8)(D, ks) * 0.5),
            "ssm_k": _w((lambda *s: torch.randn(*s, dtype=torch.bfloat16) * 1.6)(D, S) * (1.0 / S ** 0.5)),
            "ssm_D": _w((lambda *s: torch.randn(*s, dtype=torch.bfloat16) * 0.8)(D)),
        }
    _A_in = _A_setup()

    def _B_setup():
        B, S, D = 1, 2048, 512
        return {
            "x": _w((lambda *s: torch.randn(*s, dtype=torch.bfloat16) * 0.05)(B, S, D)),
            "wqkv": _w((lambda *s: torch.randn(*s, dtype=torch.bfloat16) * 0.8)(D, 3 * D)),
            "wo": _w((lambda *s: torch.randn(*s, dtype=torch.bfloat16) * 0.05)(D, D)),
        }
    _B_in = _B_setup()

    return {
        "A_q": _A_in["q"],
        "A_k": _A_in["k"],
        "A_v": _A_in["v"],
        "A_shift_k": _A_in["shift_k"],
        "A_ssm_k": _A_in["ssm_k"],
        "A_ssm_D": _A_in["ssm_D"],
        "B_x": _B_in["x"],
        "B_wqkv": _B_in["wqkv"],
        "B_wo": _B_in["wo"],
        "gate_w": _w((lambda *s: torch.randn(*s, dtype=torch.bfloat16) * 0.05)(512) * 16.0),
    }

def reference(A_q, A_k, A_v, A_shift_k, A_ssm_k, A_ssm_D, B_x, B_wqkv, B_wo, gate_w):
    def _A_ref(q, k, v, shift_k, ssm_k, ssm_D):
        B, S, D = q.shape
        ks = shift_k.shape[-1]
        k_pad = _pad(k, ((0, 0), (ks - 1, 0), (0, 0)))
        k_shift = torch.zeros_like(k)
        for t in range(ks):
            k_shift = k_shift + k_pad[:, t:t + S, :] * shift_k[:, ks - 1 - t][None, None, :]
        kv = k_shift * v
        L = 2 * S
        kv_pad = _pad(kv, ((0, 0), (0, S), (0, 0)))
        kf_pad = _pad(ssm_k, ((0, 0), (0, S)))
        kv_f = (lambda x, axes=(-2,-1): torch.fft.rfft2(x.float(), dim=axes))(kv_pad[:, None, :, :], axes=(1, 2))
        kf_f = (lambda x, axes=(-2,-1): torch.fft.rfft2(x.float(), dim=axes))(kf_pad[:, None, :], axes=(1, 2))
        kf_f = (lambda x, *d: x.permute(*d))(kf_f, 1, 2, 0)
        y_f = kv_f * kf_f[None, :, :, :]
        y = (lambda x, s, axes=(-2,-1): torch.fft.irfft2(x, s=s, dim=axes).to(torch.bfloat16))(y_f, (1, L), axes=(1, 2))[:, 0, :S, :]
        y = y + kv * ssm_D[None, None, :]
        return q * y

    def _B_ref(x, wqkv, wo):
        B, S, D = x.shape
        H = 32; Dh = D // H; W = 256
        qkv = (lambda x, *d: x.permute(*d))((x @ wqkv).reshape(B, S, 3, H, Dh), 2, 0, 3, 1, 4)
        q, k, v = qkv[0], qkv[1], qkv[2]
        scores = torch.einsum("bhqd,bhkd->bhqk", q, k) * (Dh ** -0.5)
        idx = torch.arange(S)
        mask = (idx[:, None] - idx[None, :] >= 0) & (idx[:, None] - idx[None, :] < W)
        scores = torch.where(mask[None, None], scores, -1e9)
        p = _softmax(scores, axis=-1)
        o = torch.einsum("bhqk,bhkd->bhqd", p, v)
        return (lambda x, *d: x.permute(*d))(o, 0, 2, 1, 3).reshape(B, S, D) @ wo

    x = A_q
    g = _sigmoid((x * gate_w).sum(-1, keepdims=True))
    a = _A_ref(x, A_k, A_v, A_shift_k, A_ssm_k, A_ssm_D)
    b = _B_ref(x, B_wqkv, B_wo)
    return g * a + (1 - g) * b
```

```cpp
#include <hip/hip_runtime.h>
#include <hip/hip_bf16.h>
#include <stdint.h>


#define DI __device__ __forceinline__
typedef __attribute__((ext_vector_type(8))) __bf16 bf16x8;
typedef __attribute__((ext_vector_type(4))) __bf16 bf16x4;
typedef __attribute__((ext_vector_type(2))) __bf16 bf16x2;
typedef __attribute__((ext_vector_type(4))) short s16x4;
typedef __attribute__((ext_vector_type(16))) float f32x16;
typedef __attribute__((ext_vector_type(4))) float f32x4;
typedef __attribute__((ext_vector_type(2))) float f32x2;
typedef __attribute__((ext_vector_type(4))) unsigned u32x4;
typedef __attribute__((ext_vector_type(2))) unsigned u32x2;
typedef unsigned short u16;

#define SEQ 2048
#define DM 512

#define MFMA32(a, b, c) __builtin_amdgcn_mfma_f32_32x32x16_bf16((a), (b), (c), 0, 0, 0)

DI float bf2f(u16 v) { return __uint_as_float(((unsigned)v) << 16); }
DI float bflo(unsigned v) { return __uint_as_float(v << 16); }
DI float bfhi(unsigned v) { return __uint_as_float(v & 0xffff0000u); }
DI unsigned pack2(float a, float b) {
  f32x2 f = {a, b};
  bf16x2 r = __builtin_convertvector(f, bf16x2);
  return __builtin_bit_cast(unsigned, r);
}
DI float rbf(float a) {
  return (float)((__bf16)a);
}
DI s16x4 tr_read(const char* p) {
  typedef __attribute__((address_space(3))) s16x4* lp;
  return __builtin_amdgcn_ds_read_tr16_b64_v4i16((lp)(p));
}
DI bf16x8 cat_frag(s16x4 lo, s16x4 hi) {
  typedef __attribute__((ext_vector_type(8))) short s16x8;
  s16x8 v = __builtin_shufflevector(lo, hi, 0, 1, 2, 3, 4, 5, 6, 7);
  return __builtin_bit_cast(bf16x8, v);
}

#define WS_Q 0
#define WS_K (2u << 20)
#define WS_VT (4u << 20)
#define WS_KVT (6u << 20)
#define WS_Y2T (8u << 20)
#define WS_O (10u << 20)
#define WS_G (12u << 20)

#define K1_ASTR 144
#define K1_BSTR 320
#define K1_ABYTES (128 * K1_ASTR)
#define K1_BBYTES (64 * K1_BSTR)
#define K1_STAGE (K1_ABYTES + K1_BBYTES)

template <bool ISV>
DI void k1_gemm(const u16* __restrict__ x, const u16* __restrict__ w, char* __restrict__ ws, char* smem, int tm,
                int tn) {
  const int t = threadIdx.x;
  const int l = t & 63, wv = t >> 6;
  const int r = l & 31, hh = l >> 5;
  const int wm = wv & 1, wn = wv >> 1;
  const int m0 = tm * 128, n0 = tn * 128;

  const int arow = t >> 3, ach = t & 7;
  const int brow = t >> 4, bch = t & 15;
  const u16* xg = x + (size_t)(m0 + arow) * DM + ach * 8;
  const u16* wg = w + (size_t)brow * 1536 + n0 + bch * 8;

  u32x4 ra[4], rb[4];
#pragma unroll
  for (int i = 0; i < 4; ++i) {
    ra[i] = *(const u32x4*)(xg + (size_t)(32 * i) * DM);
    rb[i] = *(const u32x4*)(wg + (size_t)(16 * i) * 1536);
  }
  f32x16 acc[2][2];
#pragma unroll
  for (int i = 0; i < 2; ++i)
#pragma unroll
    for (int j = 0; j < 2; ++j)
#pragma unroll
      for (int e = 0; e < 16; ++e) acc[i][j][e] = 0.f;

  const int blk = (l >> 4) & 1, q4 = (l & 15) >> 2, p4 = l & 3;
  const int a_off = (wm * 64 + r) * K1_ASTR + hh * 16;
  const int b_off = (8 * hh + q4) * K1_BSTR + (wn * 64 + 16 * blk + 4 * p4) * 2;

#pragma unroll 1
  for (int kt = 0; kt < 8; ++kt) {
    char* As = smem + (kt & 1) * K1_STAGE;
    char* Bs = As + K1_ABYTES;
#pragma unroll
    for (int i = 0; i < 4; ++i) {
      *(u32x4*)(As + (arow + 32 * i) * K1_ASTR + ach * 16) = ra[i];
      *(u32x4*)(Bs + (brow + 16 * i) * K1_BSTR + bch * 16) = rb[i];
    }
    __syncthreads();
    if (kt < 7) {
#pragma unroll
      for (int i = 0; i < 4; ++i) {
        ra[i] = *(const u32x4*)(xg + (size_t)(32 * i) * DM + (kt + 1) * 64);
        rb[i] = *(const u32x4*)(wg + (size_t)(16 * i + (kt + 1) * 64) * 1536);
      }
    }
#pragma unroll
    for (int ks = 0; ks < 4; ++ks) {
      bf16x8 af[2], bfr[2];
#pragma unroll
      for (int i = 0; i < 2; ++i) af[i] = *(const bf16x8*)(As + a_off + i * 32 * K1_ASTR + ks * 32);
#pragma unroll
      for (int j = 0; j < 2; ++j) {
        s16x4 lo = tr_read(Bs + b_off + (16 * ks) * K1_BSTR + j * 64);
        s16x4 hi = tr_read(Bs + b_off + (16 * ks + 4) * K1_BSTR + j * 64);
        bfr[j] = cat_frag(lo, hi);
      }
#pragma unroll
      for (int i = 0; i < 2; ++i)
#pragma unroll
        for (int j = 0; j < 2; ++j) {
          if (ISV)
            acc[i][j] = MFMA32(af[i], bfr[j], acc[i][j]);
          else
            acc[i][j] = MFMA32(bfr[j], af[i], acc[i][j]);
        }
    }
  }

  if (ISV) {
    u16* Vt = (u16*)(ws + WS_VT);
#pragma unroll
    for (int i = 0; i < 2; ++i)
#pragma unroll
      for (int j = 0; j < 2; ++j) {
        const int n = (n0 - 1024) + wn * 64 + j * 32 + r;
        const int tok0 = m0 + wm * 64 + i * 32;
        u16* dst = Vt + (size_t)n * SEQ + tok0 + 8 * hh;
#pragma unroll
        for (int bb = 0; bb < 2; ++bb) {
          u32x4 v;
          v[0] = pack2(acc[i][j][8 * bb + 0], acc[i][j][8 * bb + 1]);
          v[1] = pack2(acc[i][j][8 * bb + 2], acc[i][j][8 * bb + 3]);
          v[2] = pack2(acc[i][j][8 * bb + 4], acc[i][j][8 * bb + 5]);
          v[3] = pack2(acc[i][j][8 * bb + 6], acc[i][j][8 * bb + 7]);
          *(u32x4*)(dst + 16 * bb) = v;
        }
      }
  } else {
    u16* base = (u16*)(ws + (n0 >= 512 ? WS_K : WS_Q));
    const int nn0 = n0 & 511;
#pragma unroll
    for (int i = 0; i < 2; ++i)
#pragma unroll
      for (int j = 0; j < 2; ++j) {
        const int tok = m0 + wm * 64 + i * 32 + r;
        const int nb = nn0 + wn * 64 + j * 32;
        const int head = nb >> 4;
#pragma unroll
        for (int g = 0; g < 4; ++g) {
          u32x2 v;
          v[0] = pack2(acc[i][j][4 * g + 0], acc[i][j][4 * g + 1]);
          v[1] = pack2(acc[i][j][4 * g + 2], acc[i][j][4 * g + 3]);
          *(u32x2*)(base + ((size_t)(head + (g >> 1)) * SEQ + tok) * 16 + 8 * (g & 1) + 4 * hh) = v;
        }
      }
  }
}

__global__ __launch_bounds__(256) void k1_kernel(const u16* __restrict__ x, const u16* __restrict__ kin,
                                                 const u16* __restrict__ vin, const u16* __restrict__ shiftk,
                                                 const u16* __restrict__ wqkv, const u16* __restrict__ gatew,
                                                 char* __restrict__ ws) {
  __shared__ __attribute__((aligned(16))) char smem[2 * K1_STAGE];
  const int bid = blockIdx.x;
  if (bid < 192) {
    const int tm = bid & 15, tn = bid >> 4;
    if (tn >= 8)
      k1_gemm<true>(x, wqkv, ws, smem, tm, tn);
    else
      k1_gemm<false>(x, wqkv, ws, smem, tm, tn);
    return;
  }
  const int pb = bid - 192;
  const int l = threadIdx.x & 63, wv = threadIdx.x >> 6;
  u16* kvT = (u16*)(ws + WS_KVT);
  const int wid = pb * 4 + wv;
  {
    const int dg = wid & 7;
    const int d = dg * 64 + l;
    const float sk0 = bf2f(shiftk[d * 4 + 0]), sk1 = bf2f(shiftk[d * 4 + 1]), sk2 = bf2f(shiftk[d * 4 + 2]),
                sk3 = bf2f(shiftk[d * 4 + 3]);
    const int sg0 = (wid >> 3) * 8;
#pragma unroll 1
    for (int it = 0; it < 8; ++it) {
      const int s0 = (sg0 + it) * 8;
      float kk[11], vv[8];
#pragma unroll
      for (int i = 0; i < 11; ++i) {
        const int s = s0 - 3 + i;
        kk[i] = (s >= 0) ? bf2f(kin[(size_t)s * DM + d]) : 0.f;
      }
#pragma unroll
      for (int i = 0; i < 8; ++i) vv[i] = bf2f(vin[(size_t)(s0 + i) * DM + d]);
      float res[8];
#pragma unroll
      for (int i = 0; i < 8; ++i) {
        float a = rbf(kk[i] * sk3);
        a = rbf(a + rbf(kk[i + 1] * sk2));
        a = rbf(a + rbf(kk[i + 2] * sk1));
        a = rbf(a + rbf(kk[i + 3] * sk0));
        res[i] = a * vv[i];
      }
      u32x4 o;
      o[0] = pack2(res[0], res[1]);
      o[1] = pack2(res[2], res[3]);
      o[2] = pack2(res[4], res[5]);
      o[3] = pack2(res[6], res[7]);
      *(u32x4*)(kvT + (size_t)d * SEQ + s0) = o;
    }
  }
  {
    float* g = (float*)(ws + WS_G);
    const u32x4 gw = *(const u32x4*)(gatew + l * 8);
#pragma unroll 1
    for (int it = 0; it < 8; ++it) {
      const int s = wid * 8 + it;
      const u32x4 xv = *(const u32x4*)(x + (size_t)s * DM + l * 8);
      float sum = 0.f;
#pragma unroll
      for (int i = 0; i < 4; ++i) {
        sum += rbf(bflo(xv[i]) * bflo(gw[i]));
        sum += rbf(bfhi(xv[i]) * bfhi(gw[i]));
      }
#pragma unroll
      for (int o = 32; o >= 1; o >>= 1) sum += __shfl_xor(sum, o);
      if (l == 0) {
        const float z = rbf(sum);
        g[s] = rbf(1.f / (1.f + __expf(-z)));
      }
    }
  }
}

#define CV_KVSTR 80
#define CV_KVBYTES (96 * CV_KVSTR)
#define CV_HSTR 4160
#define CV_REDSTR 36
#define CV_SMEM (CV_KVBYTES + 4 * 64 * CV_REDSTR * 4)

DI void attn_wave(const char* __restrict__ ws, int head, int qt) {
  const int l = threadIdx.x & 63;
  const int r = l & 31, hh = l >> 5;
  const u16* Qh = (const u16*)(ws + WS_Q) + (size_t)head * SEQ * 16;
  const u16* Kh = (const u16*)(ws + WS_K) + (size_t)head * SEQ * 16;
  const u16* Vt = (const u16*)(ws + WS_VT) + ((size_t)head * 16 + (r & 15)) * SEQ;
  u16* O = (u16*)(ws + WS_O);
  const int q0 = qt * 32;
  const int kt0 = q0 >= 256 ? 0 : ((256 - q0) >> 5);
  const bf16x8 qf = *(const bf16x8*)(Qh + (size_t)(q0 + r) * 16 + 8 * hh);

  unsigned sp[9][8];
  float mx = -3.0e38f;
  f32x16 zero;
#pragma unroll
  for (int e = 0; e < 16; ++e) zero[e] = 0.f;
#pragma unroll
  for (int kt = 0; kt < 9; ++kt) {
    if (kt >= kt0) {
      const int key0 = q0 - 256 + 32 * kt;
      const bf16x8 kf = *(const bf16x8*)(Kh + (size_t)(key0 + r) * 16 + 8 * hh);
      f32x16 s = MFMA32(kf, qf, zero);
      if (kt == 0) {
#pragma unroll
        for (int e = 0; e < 16; ++e) {
          const int rr = (e & 3) + 8 * (e >> 2) + 4 * hh;
          s[e] = (rr > r) ? s[e] : -3.0e38f;
        }
      }
      if (kt == 8) {
#pragma unroll
        for (int e = 0; e < 16; ++e) {
          const int rr = (e & 3) + 8 * (e >> 2) + 4 * hh;
          s[e] = (rr <= r) ? s[e] : -3.0e38f;
        }
      }
#pragma unroll
      for (int e = 0; e < 8; ++e) {
        mx = fmaxf(mx, fmaxf(s[2 * e], s[2 * e + 1]));
        sp[kt][e] = pack2(s[2 * e], s[2 * e + 1]);
      }
    } else {
#pragma unroll
      for (int e = 0; e < 8; ++e) sp[kt][e] = 0xff80ff80u;
    }
  }
  mx = fmaxf(mx, __shfl_xor(mx, 32));
  mx = rbf(mx);
  const float c1 = 0.25f * 1.44269504088896340736f;
  const float mc = mx * c1;
  float sum = 0.f;
  f32x16 oacc = zero;
#pragma unroll
  for (int kt = 0; kt < 9; ++kt) {
    if (kt >= kt0) {
      const int key0 = q0 - 256 + 32 * kt;
#pragma unroll
      for (int e = 0; e < 8; ++e) {
        const float e0 = __builtin_amdgcn_exp2f(bflo(sp[kt][e]) * c1 - mc);
        const float e1 = __builtin_amdgcn_exp2f(bfhi(sp[kt][e]) * c1 - mc);
        sum += e0 + e1;
        sp[kt][e] = pack2(e0, e1);
      }
#pragma unroll
      for (int s = 0; s < 2; ++s) {
        const bf16x8 vf = *(const bf16x8*)(Vt + key0 + 16 * s + 8 * hh);
        u32x4 pv = {sp[kt][4 * s], sp[kt][4 * s + 1], sp[kt][4 * s + 2], sp[kt][4 * s + 3]};
        oacc = MFMA32(vf, __builtin_bit_cast(bf16x8, pv), oacc);
      }
    }
  }
  sum += __shfl_xor(sum, 32);
  const float inv = 1.f / sum;
  u16* dst = O + (size_t)(q0 + r) * DM + head * 16 + 4 * hh;
  u32x2 v0, v1;
  v0[0] = pack2(oacc[0] * inv, oacc[1] * inv);
  v0[1] = pack2(oacc[2] * inv, oacc[3] * inv);
  v1[0] = pack2(oacc[4] * inv, oacc[5] * inv);
  v1[1] = pack2(oacc[6] * inv, oacc[7] * inv);
  *(u32x2*)(dst) = v0;
  *(u32x2*)(dst + 8) = v1;
}

DI void conv_block(const u16* __restrict__ ssmk, const u16* __restrict__ ssmD, char* __restrict__ ws, char* smem,
                   int d) {
  const int t = threadIdx.x;
  const int l = t & 63, wv = t >> 6;
  const int r = l & 31, hh = l >> 5;
  char* kvs = smem;
  char* hs = smem + CV_KVBYTES;
  const u16* kvT = (const u16*)(ws + WS_KVT) + (size_t)d * SEQ;
  const u16* hrow = ssmk + (size_t)d * SEQ;
  {
    const u32x4 kvv = *(const u32x4*)(kvT + 8 * t);
    *(u32x4*)(kvs + ((t >> 2) + 32) * CV_KVSTR + (t & 3) * 16) = kvv;
    if (t < 128) {
      u32x4 z = {0u, 0u, 0u, 0u};
      *(u32x4*)(kvs + (t >> 2) * CV_KVSTR + (t & 3) * 16) = z;
    }
  }
  {
    const int E = t;
    const u32x4 whi = *(const u32x4*)(hrow + 8 * E);
    u32x4 wlo = {0u, 0u, 0u, 0u};
    if (E > 0) wlo = *(const u32x4*)(hrow + 8 * E - 8);
    unsigned z[8];
#pragma unroll
    for (int i = 0; i < 4; ++i) {
      const unsigned a = whi[3 - i];
      z[i] = (a >> 16) | (a << 16);
      const unsigned b = wlo[3 - i];
      z[4 + i] = (b >> 16) | (b << 16);
    }
    const int M = 255 - E;
#pragma unroll
    for (int p = 0; p < 8; ++p) {
      const int o = 7 - p;
      u32x4 v;
      if ((o & 1) == 0) {
        v[0] = z[o / 2];
        v[1] = z[o / 2 + 1];
        v[2] = z[o / 2 + 2];
        v[3] = z[o / 2 + 3];
      } else {
        v[0] = (z[o / 2] >> 16) | (z[o / 2 + 1] << 16);
        v[1] = (z[o / 2 + 1] >> 16) | (z[o / 2 + 2] << 16);
        v[2] = (z[o / 2 + 2] >> 16) | (z[o / 2 + 3] << 16);
        v[3] = (z[o / 2 + 3] >> 16) | (z[o / 2 + 4] << 16);
      }
      *(u32x4*)(hs + p * CV_HSTR + 16 * M) = v;
    }
    if (t < 24) {
      u32x4 zz = {0u, 0u, 0u, 0u};
      *(u32x4*)(hs + (t & 7) * CV_HSTR + 16 * (256 + (t >> 3))) = zz;
    }
  }
  __syncthreads();
  f32x16 acc0, acc1;
#pragma unroll
  for (int e = 0; e < 16; ++e) {
    acc0[e] = 0.f;
    acc1[e] = 0.f;
  }
  const char* abase = hs + (r & 7) * CV_HSTR + 16 * (255 - (r >> 3) + hh);
  const char* bbase = kvs + (r + 32) * CV_KVSTR + 16 * hh;
#pragma unroll 1
  for (int i = 0; i < 8; ++i) {
    const int delta = wv + 4 * i;
#pragma unroll
    for (int ks = 0; ks < 2; ++ks) {
      const bf16x8 a_lo = *(const bf16x8*)(abase - 64 * delta + 32 * ks);
      const bf16x8 a_hi = *(const bf16x8*)(abase - 64 * (delta + 32) + 32 * ks);
      const bf16x8 b1 = *(const bf16x8*)(bbase + (32 - delta) * CV_KVSTR + 32 * ks);
      const bf16x8 b0 = *(const bf16x8*)(bbase - delta * CV_KVSTR + 32 * ks);
      acc1 = MFMA32(a_lo, b1, acc1);
      acc0 = MFMA32(a_lo, b0, acc0);
      acc1 = MFMA32(a_hi, b0, acc1);
    }
  }
  __syncthreads();
  float* red = (float*)hs;
  {
    float* rw = red + wv * (64 * CV_REDSTR);
#pragma unroll
    for (int g = 0; g < 4; ++g) {
      f32x4 v0 = {acc0[4 * g], acc0[4 * g + 1], acc0[4 * g + 2], acc0[4 * g + 3]};
      f32x4 v1 = {acc1[4 * g], acc1[4 * g + 1], acc1[4 * g + 2], acc1[4 * g + 3]};
      *(f32x4*)(rw + r * CV_REDSTR + 8 * g + 4 * hh) = v0;
      *(f32x4*)(rw + (32 + r) * CV_REDSTR + 8 * g + 4 * hh) = v1;
    }
  }
  __syncthreads();
  {
    const int a = t >> 2, b0 = 8 * (t & 3);
    f32x4 s0 = {0.f, 0.f, 0.f, 0.f}, s1 = {0.f, 0.f, 0.f, 0.f};
#pragma unroll
    for (int w = 0; w < 4; ++w) {
      const float* rp = red + w * (64 * CV_REDSTR) + a * CV_REDSTR + b0;
      s0 += *(const f32x4*)(rp);
      s1 += *(const f32x4*)(rp + 4);
    }
    const u32x4 kvv = *(const u32x4*)(kvs + (a + 32) * CV_KVSTR + (t & 3) * 16);
    const float Dd = bf2f(ssmD[d]);
    float yy[8] = {s0[0], s0[1], s0[2], s0[3], s1[0], s1[1], s1[2], s1[3]};
    u32x4 o;
#pragma unroll
    for (int i = 0; i < 4; ++i) {
      const float y0 = rbf(yy[2 * i]) + rbf(bflo(kvv[i]) * Dd);
      const float y1 = rbf(yy[2 * i + 1]) + rbf(bfhi(kvv[i]) * Dd);
      o[i] = pack2(y0, y1);
    }
    u16* y2T = (u16*)(ws + WS_Y2T) + (size_t)d * SEQ;
    *(u32x4*)(y2T + 8 * t) = o;
  }
}

__global__ __launch_bounds__(256) void k2_kernel(const u16* __restrict__ ssmk, const u16* __restrict__ ssmD,
                                                 char* __restrict__ ws) {
  __shared__ __attribute__((aligned(16))) char smem[CV_SMEM];
  const int bid = blockIdx.x;
  if (bid & 1) {
    conv_block(ssmk, ssmD, ws, smem, bid >> 1);
  } else {
    const int ab = bid >> 1;
    const int wv = threadIdx.x >> 6;
    const int qt = ab >> 3;
    const int head = (ab & 7) * 4 + wv;
    attn_wave(ws, head, qt);
  }
}

#define K3_ASTR 272
#define K3_BSTR 192
#define K3_ABYTES (64 * K3_ASTR)
#define K3_BBYTES (128 * K3_BSTR)
#define K3_STAGE (K3_ABYTES + K3_BBYTES)
#define K3_CSTR 68
#define K3_YSTR 136

__global__ __launch_bounds__(256) void k3_kernel(const u16* __restrict__ x, const u16* __restrict__ wo,
                                                 const char* __restrict__ ws, u16* __restrict__ out) {
  __shared__ __attribute__((aligned(16))) char smem[2 * K3_STAGE];
  const int t = threadIdx.x;
  const int l = t & 63, wv = t >> 6;
  const int r = l & 31, hh = l >> 5;
  const int wm = wv & 1, wn = wv >> 1;
  const int tm = blockIdx.x & 31, tn = blockIdx.x >> 5;
  const int m0 = tm * 64, n0 = tn * 64;
  const u16* o = (const u16*)(ws + WS_O);

  const int arow = t >> 4, ach = t & 15;
  const int brow = t >> 3, bch = t & 7;
  const u16* ag = o + (size_t)(m0 + arow) * DM + ach * 8;
  const u16* bg = wo + (size_t)brow * DM + n0 + bch * 8;
  u32x4 ra[4], rb[4];
#pragma unroll
  for (int i = 0; i < 4; ++i) {
    ra[i] = *(const u32x4*)(ag + (size_t)(16 * i) * DM);
    rb[i] = *(const u32x4*)(bg + (size_t)(32 * i) * DM);
  }
  f32x16 acc;
#pragma unroll
  for (int e = 0; e < 16; ++e) acc[e] = 0.f;
  const int blk = (l >> 4) & 1, q4 = (l & 15) >> 2, p4 = l & 3;
  const int a_off = (wm * 32 + r) * K3_ASTR + hh * 16;
  const int b_off = (8 * hh + q4) * K3_BSTR + (wn * 32 + 16 * blk + 4 * p4) * 2;
#pragma unroll 1
  for (int kt = 0; kt < 4; ++kt) {
    char* As = smem + (kt & 1) * K3_STAGE;
    char* Bs = As + K3_ABYTES;
#pragma unroll
    for (int i = 0; i < 4; ++i) {
      *(u32x4*)(As + (arow + 16 * i) * K3_ASTR + ach * 16) = ra[i];
      *(u32x4*)(Bs + (brow + 32 * i) * K3_BSTR + bch * 16) = rb[i];
    }
    __syncthreads();
    if (kt < 3) {
#pragma unroll
      for (int i = 0; i < 4; ++i) {
        ra[i] = *(const u32x4*)(ag + (size_t)(16 * i) * DM + (kt + 1) * 128);
        rb[i] = *(const u32x4*)(bg + (size_t)(32 * i + (kt + 1) * 128) * DM);
      }
    }
#pragma unroll
    for (int ks = 0; ks < 8; ++ks) {
      const bf16x8 af = *(const bf16x8*)(As + a_off + ks * 32);
      s16x4 lo = tr_read(Bs + b_off + (16 * ks) * K3_BSTR);
      s16x4 hi = tr_read(Bs + b_off + (16 * ks + 4) * K3_BSTR);
      acc = MFMA32(af, cat_frag(lo, hi), acc);
    }
  }
  __syncthreads();
  float* Cs = (float*)smem;
  char* Ys = smem + 64 * K3_CSTR * 4;
#pragma unroll
  for (int e = 0; e < 16; ++e) {
    const int row = wm * 32 + (e & 3) + 8 * (e >> 2) + 4 * hh;
    Cs[row * K3_CSTR + wn * 32 + r] = acc[e];
  }
  {
    const u16* y2T = (const u16*)(ws + WS_Y2T);
#pragma unroll
    for (int i = 0; i < 2; ++i) {
      const int idx = t + 256 * i;
      const int dr = idx >> 3, ch = idx & 7;
      const u32x4 v = *(const u32x4*)(y2T + (size_t)(n0 + dr) * SEQ + m0 + ch * 8);
      u32x2 a = {v[0], v[1]}, b = {v[2], v[3]};
      *(u32x2*)(Ys + dr * K3_YSTR + ch * 16) = a;
      *(u32x2*)(Ys + dr * K3_YSTR + ch * 16 + 8) = b;
    }
  }
  __syncthreads();
  const float* gp = (const float*)(ws + WS_G);
#pragma unroll
  for (int i = 0; i < 2; ++i) {
    const int idx = t + 256 * i;
    const int row = idx >> 3, ch = idx & 7;
    const int s = m0 + row;
    const float g = gp[s];
    const float omg = rbf(1.f - g);
    const u32x4 xq = *(const u32x4*)(x + (size_t)s * DM + n0 + ch * 8);
    const f32x4 c0 = *(const f32x4*)(Cs + row * K3_CSTR + ch * 8);
    const f32x4 c1 = *(const f32x4*)(Cs + row * K3_CSTR + ch * 8 + 4);
    float bb[8] = {c0[0], c0[1], c0[2], c0[3], c1[0], c1[1], c1[2], c1[3]};
    float res[8];
#pragma unroll
    for (int j = 0; j < 8; ++j) {
      const float y2 = bf2f(*(const u16*)(Ys + (ch * 8 + j) * K3_YSTR + row * 2));
      const float qv = (j & 1) ? bfhi(xq[j >> 1]) : bflo(xq[j >> 1]);
      const float a = rbf(qv * y2);
      const float ga = rbf(g * a);
      const float gb = rbf(omg * rbf(bb[j]));
      res[j] = ga + gb;
    }
    u32x4 ov;
    ov[0] = pack2(res[0], res[1]);
    ov[1] = pack2(res[2], res[3]);
    ov[2] = pack2(res[4], res[5]);
    ov[3] = pack2(res[6], res[7]);
    *(u32x4*)(out + (size_t)s * DM + n0 + ch * 8) = ov;
  }
}

extern "C" void kernel_launch(void* const* d_in, const int* in_sizes, int n_in, void* d_out, int out_size,
                              void* d_ws, size_t ws_size, hipStream_t stream) {
  const u16* A_q = (const u16*)d_in[0];
  const u16* A_k = (const u16*)d_in[1];
  const u16* A_v = (const u16*)d_in[2];
  const u16* A_shift = (const u16*)d_in[3];
  const u16* A_ssmk = (const u16*)d_in[4];
  const u16* A_ssmD = (const u16*)d_in[5];
  const u16* B_wqkv = (const u16*)d_in[7];
  const u16* B_wo = (const u16*)d_in[8];
  const u16* gate_w = (const u16*)d_in[9];
  char* ws = (char*)d_ws;
  k1_kernel<<<256, 256, 0, stream>>>(A_q, A_k, A_v, A_shift, B_wqkv, gate_w, ws);
  k2_kernel<<<1024, 256, 0, stream>>>(A_ssmk, A_ssmD, ws);
  k3_kernel<<<256, 256, 0, stream>>>(A_q, B_wo, ws, (u16*)d_out);
}
```

```cpp
#include <hip/hip_runtime.h>
#include <hip/hip_bf16.h>
#include <stdint.h>

#define DI __device__ __forceinline__
typedef __attribute__((ext_vector_type(8))) __bf16 bf16x8;
typedef __attribute__((ext_vector_type(4))) __bf16 bf16x4;
typedef __attribute__((ext_vector_type(2))) __bf16 bf16x2;
typedef __attribute__((ext_vector_type(4))) short s16x4;
typedef __attribute__((ext_vector_type(16))) float f32x16;
typedef __attribute__((ext_vector_type(4))) float f32x4;
typedef __attribute__((ext_vector_type(2))) float f32x2;
typedef __attribute__((ext_vector_type(4))) unsigned u32x4;
typedef __attribute__((ext_vector_type(2))) unsigned u32x2;
typedef unsigned short u16;

#define SEQ 2048
#define DM 512

#define MFMA32(a, b, c) __builtin_amdgcn_mfma_f32_32x32x16_bf16((a), (b), (c), 0, 0, 0)

DI float bf2f(u16 v) { return __uint_as_float(((unsigned)v) << 16); }
DI float bflo(unsigned v) { return __uint_as_float(v << 16); }
DI float bfhi(unsigned v) { return __uint_as_float(v & 0xffff0000u); }
DI unsigned pack2(float a, float b) {
  f32x2 f = {a, b};
  bf16x2 r = __builtin_convertvector(f, bf16x2);
  return __builtin_bit_cast(unsigned, r);
}
DI float rbf(float a) {
  return (float)((__bf16)a);
}
DI s16x4 tr_read(const char* p) {
  typedef __attribute__((address_space(3))) s16x4* lp;
  return __builtin_amdgcn_ds_read_tr16_b64_v4i16((lp)(p));
}
DI bf16x8 cat_frag(s16x4 lo, s16x4 hi) {
  typedef __attribute__((ext_vector_type(8))) short s16x8;
  s16x8 v = __builtin_shufflevector(lo, hi, 0, 1, 2, 3, 4, 5, 6, 7);
  return __builtin_bit_cast(bf16x8, v);
}

#define WS_Q 0
#define WS_K (2u << 20)
#define WS_VT (4u << 20)
#define WS_KVT (6u << 20)
#define WS_Y2T (8u << 20)
#define WS_O (10u << 20)
#define WS_G (12u << 20)

#define GLDS16(g, l) __builtin_amdgcn_global_load_lds((const unsigned*)(g), (unsigned*)(l), 16, 0, 0)
#define WAIT_V(n) asm volatile("s_waitcnt vmcnt(" #n ")" ::: "memory")
#define RAWBAR() do { asm volatile("s_waitcnt lgkmcnt(0)" ::: "memory"); __builtin_amdgcn_s_barrier(); } while (0)
#define K1_STAGE 32768

template <int N>
DI void wait_vm() {
  if constexpr (N == 0) WAIT_V(0);
  else if constexpr (N == 8) WAIT_V(8);
  else if constexpr (N == 16) WAIT_V(16);
  else if constexpr (N == 24) WAIT_V(24);
}

template <bool ISV>
DI void k1_gemm(const u16* __restrict__ x, const u16* __restrict__ w, char* __restrict__ ws, char* smem, int tm,
                int tn) {
  const int t = threadIdx.x;
  const int l = t & 63, wv = t >> 6;
  const int r = l & 31, hh = l >> 5;
  const int wm = wv & 1, wn = wv >> 1;
  const int m0 = tm * 128, n0 = tn * 128;

  const int arow0 = 32 * wv + (l >> 3);
  const int brow0 = 16 * wv + (l >> 4);
  const u16* xg[4];
  const u16* wg[4];
#pragma unroll
  for (int i = 0; i < 4; ++i) {
    const int ar = arow0 + 8 * i;
    xg[i] = x + (size_t)(m0 + ar) * DM + (((l & 7) ^ ((ar >> 1) & 7)) * 8);
    const int br = brow0 + 4 * i;
    wg[i] = w + (size_t)br * 1536 + n0 + (((l & 15) ^ ((br & 3) << 2)) * 8);
  }
  char* ldsw = smem + wv * 4096 + l * 16;

  auto issue = [&](int kt) {
    char* st = ldsw + (kt & 3) * K1_STAGE;
#pragma unroll
    for (int i = 0; i < 4; ++i) GLDS16(xg[i] + kt * 64, st + i * 1024);
#pragma unroll
    for (int i = 0; i < 4; ++i) GLDS16(wg[i] + (size_t)kt * 64 * 1536, st + 16384 + i * 1024);
  };

  f32x16 acc[2][2];
#pragma unroll
  for (int i = 0; i < 2; ++i)
#pragma unroll
    for (int j = 0; j < 2; ++j)
#pragma unroll
      for (int e = 0; e < 16; ++e) acc[i][j][e] = 0.f;

  const int blk = (l >> 4) & 1, q4 = (l & 15) >> 2, p4 = l & 3;
  const int a_row_off = (wm * 64 + r) * 128;
  const int a_sw = (r >> 1) & 7;
  const int b_base = 16384 + (8 * hh + q4) * 256 + (p4 & 1) * 8;
  const int b_ch0 = (wn * 8 + 2 * blk + (p4 >> 1));

  issue(0);
  issue(1);
  issue(2);
#pragma unroll
  for (int kt = 0; kt < 8; ++kt) {
    if (kt <= 5) wait_vm<16>();
    else if (kt == 6) wait_vm<8>();
    else wait_vm<0>();
    RAWBAR();
    if (kt + 3 <= 7) issue(kt + 3);
    const char* st = smem + (kt & 3) * K1_STAGE;
#pragma unroll
    for (int ks = 0; ks < 4; ++ks) {
      bf16x8 af[2], bfr[2];
#pragma unroll
      for (int i = 0; i < 2; ++i)
        af[i] = *(const bf16x8*)(st + a_row_off + i * 32 * 128 + (((2 * ks + hh) ^ a_sw) * 16));
#pragma unroll
      for (int j = 0; j < 2; ++j) {
        const int ch = ((b_ch0 + 4 * j) ^ (q4 << 2)) * 16;
        s16x4 lo = tr_read(st + b_base + (16 * ks) * 256 + ch);
        s16x4 hi = tr_read(st + b_base + (16 * ks + 4) * 256 + ch);
        bfr[j] = cat_frag(lo, hi);
      }
#pragma unroll
      for (int i = 0; i < 2; ++i)
#pragma unroll
        for (int j = 0; j < 2; ++j) {
          if (ISV)
            acc[i][j] = MFMA32(af[i], bfr[j], acc[i][j]);
          else
            acc[i][j] = MFMA32(bfr[j], af[i], acc[i][j]);
        }
    }
  }

  if (ISV) {
    u16* Vt = (u16*)(ws + WS_VT);
#pragma unroll
    for (int i = 0; i < 2; ++i)
#pragma unroll
      for (int j = 0; j < 2; ++j) {
        const int n = (n0 - 1024) + wn * 64 + j * 32 + r;
        const int tok0 = m0 + wm * 64 + i * 32;
        u16* dst = Vt + (size_t)n * SEQ + tok0 + 8 * hh;
#pragma unroll
        for (int bb = 0; bb < 2; ++bb) {
          u32x4 v;
          v[0] = pack2(acc[i][j][8 * bb + 0], acc[i][j][8 * bb + 1]);
          v[1] = pack2(acc[i][j][8 * bb + 2], acc[i][j][8 * bb + 3]);
          v[2] = pack2(acc[i][j][8 * bb + 4], acc[i][j][8 * bb + 5]);
          v[3] = pack2(acc[i][j][8 * bb + 6], acc[i][j][8 * bb + 7]);
          *(u32x4*)(dst + 16 * bb) = v;
        }
      }
  } else {
    u16* base = (u16*)(ws + (n0 >= 512 ? WS_K : WS_Q));
    const int nn0 = n0 & 511;
#pragma unroll
    for (int i = 0; i < 2; ++i)
#pragma unroll
      for (int j = 0; j < 2; ++j) {
        const int tok = m0 + wm * 64 + i * 32 + r;
        const int nb = nn0 + wn * 64 + j * 32;
        const int head = nb >> 4;
#pragma unroll
        for (int g = 0; g < 4; ++g) {
          u32x2 v;
          v[0] = pack2(acc[i][j][4 * g + 0], acc[i][j][4 * g + 1]);
          v[1] = pack2(acc[i][j][4 * g + 2], acc[i][j][4 * g + 3]);
          *(u32x2*)(base + ((size_t)(head + (g >> 1)) * SEQ + tok) * 16 + 8 * (g & 1) + 4 * hh) = v;
        }
      }
  }
}

__global__ __launch_bounds__(256) void k1_kernel(const u16* __restrict__ x, const u16* __restrict__ kin,
                                                 const u16* __restrict__ vin, const u16* __restrict__ shiftk,
                                                 const u16* __restrict__ wqkv, const u16* __restrict__ gatew,
                                                 char* __restrict__ ws) {
  __shared__ __attribute__((aligned(16))) char smem[4 * K1_STAGE];
  const int bid = blockIdx.x;
  if (bid < 192) {
    const int tm = bid & 15, tn = bid >> 4;
    if (tn >= 8)
      k1_gemm<true>(x, wqkv, ws, smem, tm, tn);
    else
      k1_gemm<false>(x, wqkv, ws, smem, tm, tn);
    return;
  }
  const int l = threadIdx.x & 63, wv = threadIdx.x >> 6;
  const int t = threadIdx.x;
  const int pb = bid - 192;
  {
    const int ch = t & 15, rg = t >> 4;
    u32x4 kr[2][7], vr[2][4], sk[2][4];
#pragma unroll
    for (int h2 = 0; h2 < 2; ++h2) {
      const int tile = pb * 2 + h2;
      const int s0 = (tile >> 2) * 64, d0 = (tile & 3) * 128;
      const int sb = s0 + rg * 4;
      const int dd = d0 + ch * 8;
#pragma unroll
      for (int i = 0; i < 7; ++i) {
        const int s = sb - 3 + i;
        u32x4 z = {0u, 0u, 0u, 0u};
        kr[h2][i] = (s >= 0) ? *(const u32x4*)(kin + (size_t)s * DM + dd) : z;
      }
#pragma unroll
      for (int i = 0; i < 4; ++i) vr[h2][i] = *(const u32x4*)(vin + (size_t)(sb + i) * DM + dd);
#pragma unroll
      for (int i = 0; i < 4; ++i) sk[h2][i] = *(const u32x4*)(shiftk + (size_t)dd * 4 + i * 8);
    }
#pragma unroll
    for (int h2 = 0; h2 < 2; ++h2) {
#pragma unroll
      for (int c = 0; c < 8; ++c) {
        const unsigned s01 = sk[h2][c >> 1][(c & 1) * 2], s23 = sk[h2][c >> 1][(c & 1) * 2 + 1];
        const float sk0 = bflo(s01), sk1 = bfhi(s01), sk2 = bflo(s23), sk3 = bfhi(s23);
        float kk[7];
#pragma unroll
        for (int i = 0; i < 7; ++i) kk[i] = (c & 1) ? bfhi(kr[h2][i][c >> 1]) : bflo(kr[h2][i][c >> 1]);
        float res[4];
#pragma unroll
        for (int i = 0; i < 4; ++i) {
          float a = rbf(kk[i] * sk3);
          a = rbf(a + rbf(kk[i + 1] * sk2));
          a = rbf(a + rbf(kk[i + 2] * sk1));
          a = rbf(a + rbf(kk[i + 3] * sk0));
          const float vv = (c & 1) ? bfhi(vr[h2][i][c >> 1]) : bflo(vr[h2][i][c >> 1]);
          res[i] = a * vv;
        }
        u32x2 o = {pack2(res[0], res[1]), pack2(res[2], res[3])};
        *(u32x2*)(smem + h2 * 17408 + (ch * 8 + c) * 136 + rg * 8) = o;
      }
    }
    __syncthreads();
    u16* kvT = (u16*)(ws + WS_KVT);
#pragma unroll
    for (int h2 = 0; h2 < 2; ++h2) {
      const int tile = pb * 2 + h2;
      const int s0 = (tile >> 2) * 64, d0 = (tile & 3) * 128;
#pragma unroll
      for (int i = 0; i < 4; ++i) {
        const int idx = t + 256 * i;
        const int dr = idx >> 3, c8 = idx & 7;
        const u32x2 a = *(const u32x2*)(smem + h2 * 17408 + dr * 136 + c8 * 16);
        const u32x2 b = *(const u32x2*)(smem + h2 * 17408 + dr * 136 + c8 * 16 + 8);
        u32x4 v = {a[0], a[1], b[0], b[1]};
        *(u32x4*)(kvT + (size_t)(d0 + dr) * SEQ + s0 + c8 * 8) = v;
      }
    }
  }
  {
    const int wid = pb * 4 + wv;
    float* g = (float*)(ws + WS_G);
    const u32x4 gw = *(const u32x4*)(gatew + l * 8);
    u32x4 xv[8];
#pragma unroll
    for (int it = 0; it < 8; ++it) xv[it] = *(const u32x4*)(x + (size_t)(wid * 8 + it) * DM + l * 8);
    float sums[8];
#pragma unroll
    for (int it = 0; it < 8; ++it) {
      float sum = 0.f;
#pragma unroll
      for (int i = 0; i < 4; ++i) {
        sum += rbf(bflo(xv[it][i]) * bflo(gw[i]));
        sum += rbf(bfhi(xv[it][i]) * bfhi(gw[i]));
      }
      sums[it] = sum;
    }
#pragma unroll
    for (int o = 32; o >= 1; o >>= 1)
#pragma unroll
      for (int it = 0; it < 8; ++it) sums[it] += __shfl_xor(sums[it], o);
    if (l < 8) {
      float sv = sums[0];
#pragma unroll
      for (int it = 1; it < 8; ++it) sv = (l == it) ? sums[it] : sv;
      const float z = rbf(sv);
      g[wid * 8 + l] = rbf(1.f / (1.f + __expf(-z)));
    }
  }
}

#define CV_KVSTR 80
#define CV_HSTR 4160
#define CV_HBYTES (8 * CV_HSTR)
#define CV_REDSTR 36
#define CV_SMEM 40960

DI void attn_wave(const char* __restrict__ ws, char* smem, int head, int qt) {
  const int l = threadIdx.x & 63, wv = threadIdx.x >> 6;
  const int r = l & 31, hh = l >> 5;
  const u16* Qh = (const u16*)(ws + WS_Q) + (size_t)head * SEQ * 16;
  const u16* Kh = (const u16*)(ws + WS_K) + (size_t)head * SEQ * 16;
  u16* O = (u16*)(ws + WS_O);
  const int q0 = qt * 32;
  const int kt0 = q0 >= 256 ? 0 : ((256 - q0) >> 5);
  const bf16x8 qf = *(const bf16x8*)(Qh + (size_t)(q0 + r) * 16 + 8 * hh);
  bf16x8 kf[9];
#pragma unroll
  for (int kt = 0; kt < 9; ++kt) {
    int key0 = q0 - 256 + 32 * kt;
    key0 = key0 < 0 ? 0 : key0;
    kf[kt] = *(const bf16x8*)(Kh + (size_t)(key0 + r) * 16 + 8 * hh);
  }
  char* vs = smem + wv * 9216;
  {
    const int dd = l >> 2, pc = (l & 3) ^ ((dd >> 2) & 3);
    const u16* vsrc = (const u16*)(ws + WS_VT) + ((size_t)head * 16 + dd) * SEQ + pc * 8;
#pragma unroll
    for (int kt = 0; kt < 9; ++kt) {
      int key0 = q0 - 256 + 32 * kt;
      key0 = key0 < 0 ? 0 : key0;
      GLDS16(vsrc + key0, vs + kt * 1024 + l * 16);
    }
  }
  unsigned sp[9][8];
  float mx = -3.0e38f;
  f32x16 zero;
#pragma unroll
  for (int e = 0; e < 16; ++e) zero[e] = 0.f;
#pragma unroll
  for (int kt = 0; kt < 9; ++kt) {
    f32x16 s = MFMA32(kf[kt], qf, zero);
    if (kt == 0) {
#pragma unroll
      for (int e = 0; e < 16; ++e) {
        const int rr = (e & 3) + 8 * (e >> 2) + 4 * hh;
        s[e] = (rr > r) ? s[e] : -3.0e38f;
      }
    }
    if (kt == 8) {
#pragma unroll
      for (int e = 0; e < 16; ++e) {
        const int rr = (e & 3) + 8 * (e >> 2) + 4 * hh;
        s[e] = (rr <= r) ? s[e] : -3.0e38f;
      }
    }
    if (kt < 8 && kt < kt0) {
#pragma unroll
      for (int e = 0; e < 8; ++e) sp[kt][e] = 0xff80ff80u;
    } else {
#pragma unroll
      for (int e = 0; e < 8; ++e) {
        mx = fmaxf(mx, fmaxf(s[2 * e], s[2 * e + 1]));
        sp[kt][e] = pack2(s[2 * e], s[2 * e + 1]);
      }
    }
  }
  mx = fmaxf(mx, __shfl_xor(mx, 32));
  mx = rbf(mx);
  const float c1 = 0.25f * 1.44269504088896340736f;
  const float mc = mx * c1;
  float sum = 0.f;
  f32x16 oacc = zero;
  WAIT_V(0);
  const int vd = r & 15;
  const char* vrd = vs + vd * 64;
  const int vsw = (vd >> 2) & 3;
#pragma unroll
  for (int kt = 0; kt < 9; ++kt) {
#pragma unroll
    for (int e = 0; e < 8; ++e) {
      const float e0 = __builtin_amdgcn_exp2f(bflo(sp[kt][e]) * c1 - mc);
      const float e1 = __builtin_amdgcn_exp2f(bfhi(sp[kt][e]) * c1 - mc);
      sum += e0 + e1;
      sp[kt][e] = pack2(e0, e1);
    }
#pragma unroll
    for (int s = 0; s < 2; ++s) {
      const bf16x8 vf = *(const bf16x8*)(vrd + kt * 1024 + (((2 * s + hh) ^ vsw) * 16));
      u32x4 pv = {sp[kt][4 * s], sp[kt][4 * s + 1], sp[kt][4 * s + 2], sp[kt][4 * s + 3]};
      oacc = MFMA32(vf, __builtin_bit_cast(bf16x8, pv), oacc);
    }
  }
  sum += __shfl_xor(sum, 32);
  const float inv = 1.f / sum;
  u16* dst = O + (size_t)(q0 + r) * DM + head * 16 + 4 * hh;
  u32x2 v0, v1;
  v0[0] = pack2(oacc[0] * inv, oacc[1] * inv);
  v0[1] = pack2(oacc[2] * inv, oacc[3] * inv);
  v1[0] = pack2(oacc[4] * inv, oacc[5] * inv);
  v1[1] = pack2(oacc[6] * inv, oacc[7] * inv);
  *(u32x2*)(dst) = v0;
  *(u32x2*)(dst + 8) = v1;
}

DI void conv_block(const u16* __restrict__ ssmk, const u16* __restrict__ ssmD, char* __restrict__ ws, char* smem,
                   int d) {
  const int t = threadIdx.x;
  const int l = t & 63, wv = t >> 6;
  const int r = l & 31, hh = l >> 5;
  char* hs = smem;
  char* kvs = smem + CV_HBYTES;
  const u16* kvT = (const u16*)(ws + WS_KVT) + (size_t)d * SEQ;
  const u16* hrow = ssmk + (size_t)d * SEQ;
  const u32x4 kvv = *(const u32x4*)(kvT + 8 * t);
  const u32x4 whi = *(const u32x4*)(hrow + 8 * t);
  const u32x4 wlo0 = *(const u32x4*)(hrow + 8 * (t > 0 ? t - 1 : 0));
  const float Dd = bf2f(ssmD[d]);
  *(u32x4*)(kvs + ((t >> 2) + 32) * CV_KVSTR + (t & 3) * 16) = kvv;
  if (t < 128) {
    u32x4 z = {0u, 0u, 0u, 0u};
    *(u32x4*)(kvs + (t >> 2) * CV_KVSTR + (t & 3) * 16) = z;
  }
  {
    const int E = t;
    u32x4 wlo = wlo0;
    if (E == 0) wlo = (u32x4){0u, 0u, 0u, 0u};
    unsigned z[8];
#pragma unroll
    for (int i = 0; i < 4; ++i) {
      const unsigned a = whi[3 - i];
      z[i] = (a >> 16) | (a << 16);
      const unsigned b = wlo[3 - i];
      z[4 + i] = (b >> 16) | (b << 16);
    }
    const int M = 255 - E;
#pragma unroll
    for (int p = 0; p < 8; ++p) {
      const int o = 7 - p;
      u32x4 v;
      if ((o & 1) == 0) {
        v[0] = z[o / 2];
        v[1] = z[o / 2 + 1];
        v[2] = z[o / 2 + 2];
        v[3] = z[o / 2 + 3];
      } else {
        v[0] = (z[o / 2] >> 16) | (z[o / 2 + 1] << 16);
        v[1] = (z[o / 2 + 1] >> 16) | (z[o / 2 + 2] << 16);
        v[2] = (z[o / 2 + 2] >> 16) | (z[o / 2 + 3] << 16);
        v[3] = (z[o / 2 + 3] >> 16) | (z[o / 2 + 4] << 16);
      }
      *(u32x4*)(hs + p * CV_HSTR + 16 * M) = v;
    }
    if (t < 24) {
      u32x4 zz = {0u, 0u, 0u, 0u};
      *(u32x4*)(hs + (t & 7) * CV_HSTR + 16 * (256 + (t >> 3))) = zz;
    }
  }
  __syncthreads();
  f32x16 acc0, acc1, acc2;
#pragma unroll
  for (int e = 0; e < 16; ++e) {
    acc0[e] = 0.f;
    acc1[e] = 0.f;
    acc2[e] = 0.f;
  }
  const char* abase = hs + (r & 7) * CV_HSTR + 16 * (255 - (r >> 3) + hh) - 64 * wv;
  const char* bbase = kvs + (r + 32 - wv) * CV_KVSTR + 16 * hh;
#pragma unroll
  for (int i = 0; i < 8; ++i) {
#pragma unroll
    for (int ks = 0; ks < 2; ++ks) {
      const bf16x8 a_lo = *(const bf16x8*)(abase - 256 * i + 32 * ks);
      const bf16x8 a_hi = *(const bf16x8*)(abase - 256 * i - 2048 + 32 * ks);
      const bf16x8 b1 = *(const bf16x8*)(bbase + (32 - 4 * i) * CV_KVSTR + 32 * ks);
      const bf16x8 b0 = *(const bf16x8*)(bbase - (4 * i) * CV_KVSTR + 32 * ks);
      acc1 = MFMA32(a_lo, b1, acc1);
      acc0 = MFMA32(a_lo, b0, acc0);
      acc2 = MFMA32(a_hi, b0, acc2);
    }
  }
  acc1 += acc2;
  __syncthreads();
  float* red = (float*)smem;
  {
    float* rw = red + wv * (64 * CV_REDSTR);
#pragma unroll
    for (int g = 0; g < 4; ++g) {
      f32x4 v0 = {acc0[4 * g], acc0[4 * g + 1], acc0[4 * g + 2], acc0[4 * g + 3]};
      f32x4 v1 = {acc1[4 * g], acc1[4 * g + 1], acc1[4 * g + 2], acc1[4 * g + 3]};
      *(f32x4*)(rw + r * CV_REDSTR + 8 * g + 4 * hh) = v0;
      *(f32x4*)(rw + (32 + r) * CV_REDSTR + 8 * g + 4 * hh) = v1;
    }
  }
  __syncthreads();
  {
    const int a = t >> 2, b0 = 8 * (t & 3);
    f32x4 s0 = {0.f, 0.f, 0.f, 0.f}, s1 = {0.f, 0.f, 0.f, 0.f};
#pragma unroll
    for (int w = 0; w < 4; ++w) {
      const float* rp = red + w * (64 * CV_REDSTR) + a * CV_REDSTR + b0;
      s0 += *(const f32x4*)(rp);
      s1 += *(const f32x4*)(rp + 4);
    }
    float yy[8] = {s0[0], s0[1], s0[2], s0[3], s1[0], s1[1], s1[2], s1[3]};
    u32x4 o;
#pragma unroll
    for (int i = 0; i < 4; ++i) {
      const float y0 = rbf(yy[2 * i]) + rbf(bflo(kvv[i]) * Dd);
      const float y1 = rbf(yy[2 * i + 1]) + rbf(bfhi(kvv[i]) * Dd);
      o[i] = pack2(y0, y1);
    }
    u16* y2T = (u16*)(ws + WS_Y2T) + (size_t)d * SEQ;
    *(u32x4*)(y2T + 8 * t) = o;
  }
}

__global__ __launch_bounds__(256, 3) void k2_kernel(const u16* __restrict__ ssmk, const u16* __restrict__ ssmD,
                                                    char* __restrict__ ws) {
  __shared__ __attribute__((aligned(16))) char smem[CV_SMEM];
  const int bid = blockIdx.x;
  if (bid & 1) {
    conv_block(ssmk, ssmD, ws, smem, bid >> 1);
  } else {
    const int ab = bid >> 1;
    const int wv = threadIdx.x >> 6;
    const int qt = ab >> 3;
    const int head = (ab & 7) * 4 + wv;
    attn_wave(ws, smem, head, qt);
  }
}

#define K3_STAGE 32768
#define K3_CSTR 68
#define K3_YSTR 136

__global__ __launch_bounds__(256) void k3_kernel(const u16* __restrict__ x, const u16* __restrict__ wo,
                                                 const char* __restrict__ ws, u16* __restrict__ out) {
  __shared__ __attribute__((aligned(16))) char smem[4 * K3_STAGE];
  const int t = threadIdx.x;
  const int l = t & 63, wv = t >> 6;
  const int r = l & 31, hh = l >> 5;
  const int wm = wv & 1, wn = wv >> 1;
  const int tm = blockIdx.x & 31, tn = blockIdx.x >> 5;
  const int m0 = tm * 64, n0 = tn * 64;
  const u16* o = (const u16*)(ws + WS_O);

  const u16* y2T = (const u16*)(ws + WS_Y2T);
  const float* gp = (const float*)(ws + WS_G);
  u32x4 yv[2], xq[2];
  float gv[2];
#pragma unroll
  for (int i = 0; i < 2; ++i) {
    const int idx = t + 256 * i;
    const int rr = idx >> 3, ch = idx & 7;
    yv[i] = *(const u32x4*)(y2T + (size_t)(n0 + rr) * SEQ + m0 + ch * 8);
    xq[i] = *(const u32x4*)(x + (size_t)(m0 + rr) * DM + n0 + ch * 8);
    gv[i] = gp[m0 + rr];
  }
  {
    char* ldsw = smem + wv * 4096 + l * 16;
#pragma unroll
    for (int st = 0; st < 4; ++st) {
#pragma unroll
      for (int i = 0; i < 4; ++i) {
        const int ar = 16 * wv + 4 * i + (l >> 4);
        GLDS16(o + (size_t)(m0 + ar) * DM + st * 128 + (((l & 15) ^ (ar & 15)) * 8), ldsw + st * K3_STAGE + i * 1024);
      }
#pragma unroll
      for (int i = 0; i < 4; ++i) {
        const int br = 32 * wv + 8 * i + (l >> 3);
        GLDS16(wo + (size_t)(st * 128 + br) * DM + n0 + (((l & 7) ^ (((br >> 1) & 1) << 2)) * 8),
               ldsw + st * K3_STAGE + 16384 + i * 1024);
      }
    }
  }
  f32x16 acc;
#pragma unroll
  for (int e = 0; e < 16; ++e) acc[e] = 0.f;
  const int blk = (l >> 4) & 1, q4 = (l & 15) >> 2, p4 = l & 3;
  const int arow = wm * 32 + r;
  const int a_off = arow * 256;
  const int a_sw = arow & 15;
  const int b_off = 16384 + (8 * hh + q4) * 128 + (((wn * 4 + 2 * blk + (p4 >> 1)) ^ (((q4 >> 1) & 1) << 2)) * 16) +
                    (p4 & 1) * 8;
#pragma unroll
  for (int st = 0; st < 4; ++st) {
    if (st == 0) wait_vm<24>();
    else if (st == 1) wait_vm<16>();
    else if (st == 2) wait_vm<8>();
    else wait_vm<0>();
    RAWBAR();
    const char* sb = smem + st * K3_STAGE;
#pragma unroll
    for (int ks = 0; ks < 8; ++ks) {
      const bf16x8 af = *(const bf16x8*)(sb + a_off + (((2 * ks + hh) ^ a_sw) * 16));
      s16x4 lo = tr_read(sb + b_off + (16 * ks) * 128);
      s16x4 hi = tr_read(sb + b_off + (16 * ks + 4) * 128);
      acc = MFMA32(af, cat_frag(lo, hi), acc);
    }
  }
  __syncthreads();
  float* Cs = (float*)smem;
  char* Ys = smem + 64 * K3_CSTR * 4;
#pragma unroll
  for (int e = 0; e < 16; ++e) {
    const int row = wm * 32 + (e & 3) + 8 * (e >> 2) + 4 * hh;
    Cs[row * K3_CSTR + wn * 32 + r] = acc[e];
  }
#pragma unroll
  for (int i = 0; i < 2; ++i) {
    const int idx = t + 256 * i;
    const int dr = idx >> 3, ch = idx & 7;
    u32x2 a = {yv[i][0], yv[i][1]}, b = {yv[i][2], yv[i][3]};
    *(u32x2*)(Ys + dr * K3_YSTR + ch * 16) = a;
    *(u32x2*)(Ys + dr * K3_YSTR + ch * 16 + 8) = b;
  }
  __syncthreads();
#pragma unroll
  for (int i = 0; i < 2; ++i) {
    const int idx = t + 256 * i;
    const int row = idx >> 3, ch = idx & 7;
    const int s = m0 + row;
    const float g = gv[i];
    const float omg = rbf(1.f - g);
    const f32x4 c0 = *(const f32x4*)(Cs + row * K3_CSTR + ch * 8);
    const f32x4 c1 = *(const f32x4*)(Cs + row * K3_CSTR + ch * 8 + 4);
    float bb[8] = {c0[0], c0[1], c0[2], c0[3], c1[0], c1[1], c1[2], c1[3]};
    float res[8];
#pragma unroll
    for (int j = 0; j < 8; ++j) {
      const float y2 = bf2f(*(const u16*)(Ys + (ch * 8 + j) * K3_YSTR + row * 2));
      const float qv = (j & 1) ? bfhi(xq[i][j >> 1]) : bflo(xq[i][j >> 1]);
      const float a = rbf(qv * y2);
      const float ga = rbf(g * a);
      const float gb = rbf(omg * rbf(bb[j]));
      res[j] = ga + gb;
    }
    u32x4 ov;
    ov[0] = pack2(res[0], res[1]);
    ov[1] = pack2(res[2], res[3]);
    ov[2] = pack2(res[4], res[5]);
    ov[3] = pack2(res[6], res[7]);
    *(u32x4*)(out + (size_t)s * DM + n0 + ch * 8) = ov;
  }
}

extern "C" void kernel_launch(void* const* d_in, const int* in_sizes, int n_in, void* d_out, int out_size,
                              void* d_ws, size_t ws_size, hipStream_t stream) {
  const u16* A_q = (const u16*)d_in[0];
  const u16* A_k = (const u16*)d_in[1];
  const u16* A_v = (const u16*)d_in[2];
  const u16* A_shift = (const u16*)d_in[3];
  const u16* A_ssmk = (const u16*)d_in[4];
  const u16* A_ssmD = (const u16*)d_in[5];
  const u16* B_wqkv = (const u16*)d_in[7];
  const u16* B_wo = (const u16*)d_in[8];
  const u16* gate_w = (const u16*)d_in[9];
  char* ws = (char*)d_ws;
  k1_kernel<<<256, 256, 0, stream>>>(A_q, A_k, A_v, A_shift, B_wqkv, gate_w, ws);
  k2_kernel<<<1024, 256, 0, stream>>>(A_ssmk, A_ssmD, ws);
  k3_kernel<<<256, 256, 0, stream>>>(A_q, B_wo, ws, (u16*)d_out);
}
```

```cpp
#include <hip/hip_runtime.h>
#include <hip/hip_bf16.h>
#include <stdint.h>

#define DI __device__ __forceinline__
typedef __attribute__((ext_vector_type(8))) __bf16 bf16x8;
typedef __attribute__((ext_vector_type(4))) __bf16 bf16x4;
typedef __attribute__((ext_vector_type(2))) __bf16 bf16x2;
typedef __attribute__((ext_vector_type(4))) short s16x4;
typedef __attribute__((ext_vector_type(16))) float f32x16;
typedef __attribute__((ext_vector_type(4))) float f32x4;
typedef __attribute__((ext_vector_type(2))) float f32x2;
typedef __attribute__((ext_vector_type(4))) unsigned u32x4;
typedef __attribute__((ext_vector_type(2))) unsigned u32x2;
typedef unsigned short u16;

#define SEQ 2048
#define DM 512

#define MFMA32(a, b, c) __builtin_amdgcn_mfma_f32_32x32x16_bf16((a), (b), (c), 0, 0, 0)

DI float bf2f(u16 v) { return __uint_as_float(((unsigned)v) << 16); }
DI float bflo(unsigned v) { return __uint_as_float(v << 16); }
DI float bfhi(unsigned v) { return __uint_as_float(v & 0xffff0000u); }
DI unsigned pack2(float a, float b) {
  f32x2 f = {a, b};
  bf16x2 r = __builtin_convertvector(f, bf16x2);
  return __builtin_bit_cast(unsigned, r);
}
DI float rbf(float a) {
  return (float)((__bf16)a);
}
DI s16x4 tr_read(const char* p) {
  typedef __attribute__((address_space(3))) s16x4* lp;
  return __builtin_amdgcn_ds_read_tr16_b64_v4i16((lp)(p));
}
DI bf16x8 cat_frag(s16x4 lo, s16x4 hi) {
  typedef __attribute__((ext_vector_type(8))) short s16x8;
  s16x8 v = __builtin_shufflevector(lo, hi, 0, 1, 2, 3, 4, 5, 6, 7);
  return __builtin_bit_cast(bf16x8, v);
}

#define STAMP(k, i) do {} while (0)
#define WS_Q 0
#define WS_K (2u << 20)
#define WS_VT (4u << 20)
#define WS_KVT (6u << 20)
#define WS_Y2T (8u << 20)
#define WS_O (10u << 20)
#define WS_G (12u << 20)

#define GLDS16(g, l) __builtin_amdgcn_global_load_lds((const unsigned*)(g), (unsigned*)(l), 16, 0, 0)
#define WAIT_V(n) asm volatile("s_waitcnt vmcnt(" #n ")" ::: "memory")
#define RAWBAR() do { asm volatile("s_waitcnt lgkmcnt(0)" ::: "memory"); __builtin_amdgcn_s_barrier(); } while (0)
#define K1_STAGE 32768

template <int N>
DI void wait_vm() {
  if constexpr (N == 0) WAIT_V(0);
  else if constexpr (N == 8) WAIT_V(8);
  else if constexpr (N == 16) WAIT_V(16);
  else if constexpr (N == 24) WAIT_V(24);
}

template <bool ISV>
DI void k1_gemm(const u16* __restrict__ x, const u16* __restrict__ w, char* __restrict__ ws, char* smem, int tm,
                int tn) {
  const int t = threadIdx.x;
  const int l = t & 63, wv = t >> 6;
  const int r = l & 31, hh = l >> 5;
  const int wm = wv & 1, wn = wv >> 1;
  const int m0 = tm * 128, n0 = tn * 128;

  const int arow0 = 32 * wv + (l >> 3);
  const int brow0 = 16 * wv + (l >> 4);
  const u16* xg[4];
  const u16* wg[4];
#pragma unroll
  for (int i = 0; i < 4; ++i) {
    const int ar = arow0 + 8 * i;
    xg[i] = x + (size_t)(m0 + ar) * DM + (((l & 7) ^ ((ar >> 1) & 7)) * 8);
    const int br = brow0 + 4 * i;
    wg[i] = w + (size_t)br * 1536 + n0 + (((l & 15) ^ ((br & 3) << 2)) * 8);
  }
  char* ldsw = smem + wv * 4096 + l * 16;

  auto issue = [&](int kt) {
    char* st = ldsw + (kt & 3) * K1_STAGE;
#pragma unroll
    for (int i = 0; i < 4; ++i) GLDS16(xg[i] + kt * 64, st + i * 1024);
#pragma unroll
    for (int i = 0; i < 4; ++i) GLDS16(wg[i] + (size_t)kt * 64 * 1536, st + 16384 + i * 1024);
  };

  f32x16 acc[2][2];
#pragma unroll
  for (int i = 0; i < 2; ++i)
#pragma unroll
    for (int j = 0; j < 2; ++j)
#pragma unroll
      for (int e = 0; e < 16; ++e) acc[i][j][e] = 0.f;

  const int blk = (l >> 4) & 1, q4 = (l & 15) >> 2, p4 = l & 3;
  const int a_row_off = (wm * 64 + r) * 128;
  const int a_sw = (r >> 1) & 7;
  const int b_base = 16384 + (8 * hh + q4) * 256 + (p4 & 1) * 8;
  const int b_ch0 = (wn * 8 + 2 * blk + (p4 >> 1));

  STAMP(0, 0);
  issue(0);
  issue(1);
  issue(2);
#pragma unroll
  for (int kt = 0; kt < 8; ++kt) {
    if (kt <= 5) wait_vm<16>();
    else if (kt == 6) wait_vm<8>();
    else wait_vm<0>();
    RAWBAR();
    STAMP(0, 1 + kt);
    if (kt + 3 <= 7) issue(kt + 3);
    const char* st = smem + (kt & 3) * K1_STAGE;
    bf16x8 af[2][2], bfr[2][2];
    auto ldfr = [&](int ks, bf16x8(&a)[2], bf16x8(&b)[2]) {
#pragma unroll
      for (int i = 0; i < 2; ++i)
        a[i] = *(const bf16x8*)(st + a_row_off + i * 32 * 128 + (((2 * ks + hh) ^ a_sw) * 16));
#pragma unroll
      for (int j = 0; j < 2; ++j) {
        const int ch = ((b_ch0 + 4 * j) ^ (q4 << 2)) * 16;
        s16x4 lo = tr_read(st + b_base + (16 * ks) * 256 + ch);
        s16x4 hi = tr_read(st + b_base + (16 * ks + 4) * 256 + ch);
        b[j] = cat_frag(lo, hi);
      }
    };
    ldfr(0, af[0], bfr[0]);
#pragma unroll
    for (int ks = 0; ks < 4; ++ks) {
      if (ks < 3) ldfr(ks + 1, af[(ks + 1) & 1], bfr[(ks + 1) & 1]);
      __builtin_amdgcn_sched_barrier(0);
#pragma unroll
      for (int i = 0; i < 2; ++i)
#pragma unroll
        for (int j = 0; j < 2; ++j) {
          if (ISV)
            acc[i][j] = MFMA32(af[ks & 1][i], bfr[ks & 1][j], acc[i][j]);
          else
            acc[i][j] = MFMA32(bfr[ks & 1][j], af[ks & 1][i], acc[i][j]);
        }
      __builtin_amdgcn_sched_barrier(0);
    }
  }

  STAMP(0, 9);
  if (ISV) {
    u16* Vt = (u16*)(ws + WS_VT);
#pragma unroll
    for (int i = 0; i < 2; ++i)
#pragma unroll
      for (int j = 0; j < 2; ++j) {
        const int n = (n0 - 1024) + wn * 64 + j * 32 + r;
        const int tok0 = m0 + wm * 64 + i * 32;
        u16* dst = Vt + (size_t)n * SEQ + tok0 + 8 * hh;
#pragma unroll
        for (int bb = 0; bb < 2; ++bb) {
          u32x4 v;
          v[0] = pack2(acc[i][j][8 * bb + 0], acc[i][j][8 * bb + 1]);
          v[1] = pack2(acc[i][j][8 * bb + 2], acc[i][j][8 * bb + 3]);
          v[2] = pack2(acc[i][j][8 * bb + 4], acc[i][j][8 * bb + 5]);
          v[3] = pack2(acc[i][j][8 * bb + 6], acc[i][j][8 * bb + 7]);
          *(u32x4*)(dst + 16 * bb) = v;
        }
      }
  } else {
    u16* base = (u16*)(ws + (n0 >= 512 ? WS_K : WS_Q));
    const int nn0 = n0 & 511;
#pragma unroll
    for (int i = 0; i < 2; ++i)
#pragma unroll
      for (int j = 0; j < 2; ++j) {
        const int tok = m0 + wm * 64 + i * 32 + r;
        const int nb = nn0 + wn * 64 + j * 32;
        const int head = nb >> 4;
#pragma unroll
        for (int g = 0; g < 4; ++g) {
          u32x2 v;
          v[0] = pack2(acc[i][j][4 * g + 0], acc[i][j][4 * g + 1]);
          v[1] = pack2(acc[i][j][4 * g + 2], acc[i][j][4 * g + 3]);
          *(u32x2*)(base + ((size_t)(head + (g >> 1)) * SEQ + tok) * 16 + 8 * (g & 1) + 4 * hh) = v;
        }
      }
  }
  STAMP(0, 10);
}

__global__ __launch_bounds__(256) void k1_kernel(const u16* __restrict__ x, const u16* __restrict__ kin,
                                                 const u16* __restrict__ vin, const u16* __restrict__ shiftk,
                                                 const u16* __restrict__ wqkv, const u16* __restrict__ gatew,
                                                 char* __restrict__ ws) {
  __shared__ __attribute__((aligned(16))) char smem[4 * K1_STAGE];
  const int xcd = blockIdx.x & 7, bidx = blockIdx.x >> 3;
  if (bidx < 24) {
    const int tm = 2 * xcd + (bidx & 1), tn = bidx >> 1;
    if (tn >= 8)
      k1_gemm<true>(x, wqkv, ws, smem, tm, tn);
    else
      k1_gemm<false>(x, wqkv, ws, smem, tm, tn);
    return;
  }
  const int l = threadIdx.x & 63, wv = threadIdx.x >> 6;
  const int t = threadIdx.x;
  const int pb = xcd * 8 + (bidx - 24);
  {
    const int ch = t & 15, rg = t >> 4;
    u32x4 kr[2][7], vr[2][4], sk[2][4];
#pragma unroll
    for (int h2 = 0; h2 < 2; ++h2) {
      const int tile = pb * 2 + h2;
      const int s0 = (tile >> 2) * 64, d0 = (tile & 3) * 128;
      const int sb = s0 + rg * 4;
      const int dd = d0 + ch * 8;
#pragma unroll
      for (int i = 0; i < 7; ++i) {
        const int s = sb - 3 + i;
        u32x4 z = {0u, 0u, 0u, 0u};
        kr[h2][i] = (s >= 0) ? *(const u32x4*)(kin + (size_t)s * DM + dd) : z;
      }
#pragma unroll
      for (int i = 0; i < 4; ++i) vr[h2][i] = *(const u32x4*)(vin + (size_t)(sb + i) * DM + dd);
#pragma unroll
      for (int i = 0; i < 4; ++i) sk[h2][i] = *(const u32x4*)(shiftk + (size_t)dd * 4 + i * 8);
    }
#pragma unroll
    for (int h2 = 0; h2 < 2; ++h2) {
#pragma unroll
      for (int c = 0; c < 8; ++c) {
        const unsigned s01 = sk[h2][c >> 1][(c & 1) * 2], s23 = sk[h2][c >> 1][(c & 1) * 2 + 1];
        const float sk0 = bflo(s01), sk1 = bfhi(s01), sk2 = bflo(s23), sk3 = bfhi(s23);
        float kk[7];
#pragma unroll
        for (int i = 0; i < 7; ++i) kk[i] = (c & 1) ? bfhi(kr[h2][i][c >> 1]) : bflo(kr[h2][i][c >> 1]);
        float res[4];
#pragma unroll
        for (int i = 0; i < 4; ++i) {
          float a = rbf(kk[i] * sk3);
          a = rbf(a + rbf(kk[i + 1] * sk2));
          a = rbf(a + rbf(kk[i + 2] * sk1));
          a = rbf(a + rbf(kk[i + 3] * sk0));
          const float vv = (c & 1) ? bfhi(vr[h2][i][c >> 1]) : bflo(vr[h2][i][c >> 1]);
          res[i] = a * vv;
        }
        u32x2 o = {pack2(res[0], res[1]), pack2(res[2], res[3])};
        *(u32x2*)(smem + h2 * 17408 + (ch * 8 + c) * 136 + rg * 8) = o;
      }
    }
    __syncthreads();
    u16* kvT = (u16*)(ws + WS_KVT);
#pragma unroll
    for (int h2 = 0; h2 < 2; ++h2) {
      const int tile = pb * 2 + h2;
      const int s0 = (tile >> 2) * 64, d0 = (tile & 3) * 128;
#pragma unroll
      for (int i = 0; i < 4; ++i) {
        const int idx = t + 256 * i;
        const int dr = idx >> 3, c8 = idx & 7;
        const u32x2 a = *(const u32x2*)(smem + h2 * 17408 + dr * 136 + c8 * 16);
        const u32x2 b = *(const u32x2*)(smem + h2 * 17408 + dr * 136 + c8 * 16 + 8);
        u32x4 v = {a[0], a[1], b[0], b[1]};
        *(u32x4*)(kvT + (size_t)(d0 + dr) * SEQ + s0 + c8 * 8) = v;
      }
    }
  }
  {
    const int wid = pb * 4 + wv;
    float* g = (float*)(ws + WS_G);
    const u32x4 gw = *(const u32x4*)(gatew + l * 8);
    u32x4 xv[8];
#pragma unroll
    for (int it = 0; it < 8; ++it) xv[it] = *(const u32x4*)(x + (size_t)(wid * 8 + it) * DM + l * 8);
    float sums[8];
#pragma unroll
    for (int it = 0; it < 8; ++it) {
      float sum = 0.f;
#pragma unroll
      for (int i = 0; i < 4; ++i) {
        sum += rbf(bflo(xv[it][i]) * bflo(gw[i]));
        sum += rbf(bfhi(xv[it][i]) * bfhi(gw[i]));
      }
      sums[it] = sum;
    }
#pragma unroll
    for (int o = 32; o >= 1; o >>= 1)
#pragma unroll
      for (int it = 0; it < 8; ++it) sums[it] += __shfl_xor(sums[it], o);
    if (l < 8) {
      float sv = sums[0];
#pragma unroll
      for (int it = 1; it < 8; ++it) sv = (l == it) ? sums[it] : sv;
      const float z = rbf(sv);
      g[wid * 8 + l] = rbf(1.f / (1.f + __expf(-z)));
    }
  }
}

#define CV_KVSTR 80
#define CV_HSTR 4160
#define CV_HBYTES (8 * CV_HSTR)
#define CV_REDSTR 36
#define CV_SMEM 40960

DI void attn_wave(const char* __restrict__ ws, char* smem, int head, int qt) {
  const int l = threadIdx.x & 63, wv = threadIdx.x >> 6;
  const int r = l & 31, hh = l >> 5;
  const u16* Qh = (const u16*)(ws + WS_Q) + (size_t)head * SEQ * 16;
  const u16* Kh = (const u16*)(ws + WS_K) + (size_t)head * SEQ * 16;
  u16* O = (u16*)(ws + WS_O);
  const int q0 = qt * 32;
  const int kt0 = q0 >= 256 ? 0 : ((256 - q0) >> 5);
  STAMP(1, 0);
  const bf16x8 qf = *(const bf16x8*)(Qh + (size_t)(q0 + r) * 16 + 8 * hh);
  bf16x8 kf[9];
#pragma unroll
  for (int kt = 0; kt < 9; ++kt) {
    int key0 = q0 - 256 + 32 * kt;
    key0 = key0 < 0 ? 0 : key0;
    kf[kt] = *(const bf16x8*)(Kh + (size_t)(key0 + r) * 16 + 8 * hh);
  }
  char* vs = smem + wv * 9216;
  {
    const int dd = l >> 2, pc = (l & 3) ^ ((dd >> 2) & 3);
    const u16* vsrc = (const u16*)(ws + WS_VT) + ((size_t)head * 16 + dd) * SEQ + pc * 8;
#pragma unroll
    for (int kt = 0; kt < 9; ++kt) {
      int key0 = q0 - 256 + 32 * kt;
      key0 = key0 < 0 ? 0 : key0;
      GLDS16(vsrc + key0, vs + kt * 1024 + l * 16);
    }
  }
  float mx = -3.0e38f;
  f32x16 zero;
#pragma unroll
  for (int e = 0; e < 16; ++e) zero[e] = 0.f;
#pragma unroll
  for (int kt = 0; kt < 9; ++kt) {
    if (kt == 8 || kt >= kt0) {
      f32x16 s = MFMA32(kf[kt], qf, zero);
      if (kt == 0) {
#pragma unroll
        for (int e = 0; e < 16; ++e) {
          const int rr = (e & 3) + 8 * (e >> 2) + 4 * hh;
          s[e] = (rr > r) ? s[e] : -3.0e38f;
        }
      }
      if (kt == 8) {
#pragma unroll
        for (int e = 0; e < 16; ++e) {
          const int rr = (e & 3) + 8 * (e >> 2) + 4 * hh;
          s[e] = (rr <= r) ? s[e] : -3.0e38f;
        }
      }
#pragma unroll
      for (int e = 0; e < 8; ++e) mx = fmaxf(fmaxf(s[2 * e], s[2 * e + 1]), mx);
    }
  }
  STAMP(1, 1);
  mx = fmaxf(mx, __shfl_xor(mx, 32));
  const float c1 = 0.25f * 1.44269504088896340736f;
  const float mc = mx * c1;
  f32x2 sum2 = {0.f, 0.f};
  f32x16 oacc = zero;
  WAIT_V(0);
  STAMP(1, 2);
  const int vd = r & 15;
  const char* vrd = vs + vd * 64;
  const int vsw = (vd >> 2) & 3;
#pragma unroll
  for (int kt = 0; kt < 9; ++kt) {
    if (kt == 8 || kt >= kt0) {
      f32x16 s = MFMA32(kf[kt], qf, zero);
      if (kt == 0) {
#pragma unroll
        for (int e = 0; e < 16; ++e) {
          const int rr = (e & 3) + 8 * (e >> 2) + 4 * hh;
          s[e] = (rr > r) ? s[e] : -3.0e38f;
        }
      }
      if (kt == 8) {
#pragma unroll
        for (int e = 0; e < 16; ++e) {
          const int rr = (e & 3) + 8 * (e >> 2) + 4 * hh;
          s[e] = (rr <= r) ? s[e] : -3.0e38f;
        }
      }
      u32x4 pv[2];
#pragma unroll
      for (int e = 0; e < 8; ++e) {
        f32x2 ee;
        ee[0] = __builtin_amdgcn_exp2f(s[2 * e] * c1 - mc);
        ee[1] = __builtin_amdgcn_exp2f(s[2 * e + 1] * c1 - mc);
        sum2 += ee;
        pv[e >> 2][e & 3] = pack2(ee[0], ee[1]);
      }
#pragma unroll
      for (int sx = 0; sx < 2; ++sx) {
        const bf16x8 vf = *(const bf16x8*)(vrd + kt * 1024 + (((2 * sx + hh) ^ vsw) * 16));
        oacc = MFMA32(vf, __builtin_bit_cast(bf16x8, pv[sx]), oacc);
      }
    }
  }
  STAMP(1, 3);
  float sum = sum2[0] + sum2[1];
  sum += __shfl_xor(sum, 32);
  const float inv = 1.f / sum;
  u16* dst = O + (size_t)(q0 + r) * DM + head * 16 + 4 * hh;
  u32x2 v0, v1;
  v0[0] = pack2(oacc[0] * inv, oacc[1] * inv);
  v0[1] = pack2(oacc[2] * inv, oacc[3] * inv);
  v1[0] = pack2(oacc[4] * inv, oacc[5] * inv);
  v1[1] = pack2(oacc[6] * inv, oacc[7] * inv);
  *(u32x2*)(dst) = v0;
  *(u32x2*)(dst + 8) = v1;
  STAMP(1, 4);
}

DI void conv_block(const u16* __restrict__ ssmk, const u16* __restrict__ ssmD, char* __restrict__ ws, char* smem,
                   int d) {
  const int t = threadIdx.x;
  const int l = t & 63, wv = t >> 6;
  const int r = l & 31, hh = l >> 5;
  char* hs = smem;
  char* kvs = smem + CV_HBYTES;
  const u16* kvT = (const u16*)(ws + WS_KVT) + (size_t)d * SEQ;
  const u16* hrow = ssmk + (size_t)d * SEQ;
  STAMP(1, 0);
  const u32x4 kvv = *(const u32x4*)(kvT + 8 * t);
  const u32x4 whi = *(const u32x4*)(hrow + 8 * t);
  const u32x4 wlo0 = *(const u32x4*)(hrow + 8 * (t > 0 ? t - 1 : 0));
  const float Dd = bf2f(ssmD[d]);
  *(u32x4*)(kvs + ((t >> 2) + 32) * CV_KVSTR + (t & 3) * 16) = kvv;
  if (t < 128) {
    u32x4 z = {0u, 0u, 0u, 0u};
    *(u32x4*)(kvs + (t >> 2) * CV_KVSTR + (t & 3) * 16) = z;
  }
  {
    const int E = t;
    u32x4 wlo = wlo0;
    if (E == 0) wlo = (u32x4){0u, 0u, 0u, 0u};
    unsigned z[8];
#pragma unroll
    for (int i = 0; i < 4; ++i) {
      const unsigned a = whi[3 - i];
      z[i] = (a >> 16) | (a << 16);
      const unsigned b = wlo[3 - i];
      z[4 + i] = (b >> 16) | (b << 16);
    }
    const int M = 255 - E;
#pragma unroll
    for (int p = 0; p < 8; ++p) {
      const int o = 7 - p;
      u32x4 v;
      if ((o & 1) == 0) {
        v[0] = z[o / 2];
        v[1] = z[o / 2 + 1];
        v[2] = z[o / 2 + 2];
        v[3] = z[o / 2 + 3];
      } else {
        v[0] = (z[o / 2] >> 16) | (z[o / 2 + 1] << 16);
        v[1] = (z[o / 2 + 1] >> 16) | (z[o / 2 + 2] << 16);
        v[2] = (z[o / 2 + 2] >> 16) | (z[o / 2 + 3] << 16);
        v[3] = (z[o / 2 + 3] >> 16) | (z[o / 2 + 4] << 16);
      }
      *(u32x4*)(hs + p * CV_HSTR + 16 * M) = v;
    }
    if (t < 24) {
      u32x4 zz = {0u, 0u, 0u, 0u};
      *(u32x4*)(hs + (t & 7) * CV_HSTR + 16 * (256 + (t >> 3))) = zz;
    }
  }
  __syncthreads();
  STAMP(1, 1);
  f32x16 acc0, acc1, acc2;
#pragma unroll
  for (int e = 0; e < 16; ++e) {
    acc0[e] = 0.f;
    acc1[e] = 0.f;
    acc2[e] = 0.f;
  }
  const char* abase = hs + (r & 7) * CV_HSTR + 16 * (255 - (r >> 3) + hh) - 64 * wv;
  const char* bbase = kvs + (r + 32 - wv) * CV_KVSTR + 16 * hh;
  {
    bf16x8 fa[2][2], fb[2][2];
    auto ldc = [&](int step, bf16x8(&a)[2], bf16x8(&b)[2]) {
      const int i = step >> 1, ks = step & 1;
      a[0] = *(const bf16x8*)(abase - 256 * i + 32 * ks);
      a[1] = *(const bf16x8*)(abase - 256 * i - 2048 + 32 * ks);
      b[1] = *(const bf16x8*)(bbase + (32 - 4 * i) * CV_KVSTR + 32 * ks);
      b[0] = *(const bf16x8*)(bbase - (4 * i) * CV_KVSTR + 32 * ks);
    };
    ldc(0, fa[0], fb[0]);
#pragma unroll
    for (int step = 0; step < 16; ++step) {
      if (step < 15) ldc(step + 1, fa[(step + 1) & 1], fb[(step + 1) & 1]);
      __builtin_amdgcn_sched_barrier(0);
      acc1 = MFMA32(fa[step & 1][0], fb[step & 1][1], acc1);
      acc0 = MFMA32(fa[step & 1][0], fb[step & 1][0], acc0);
      acc2 = MFMA32(fa[step & 1][1], fb[step & 1][0], acc2);
      __builtin_amdgcn_sched_barrier(0);
    }
  }
  acc1 += acc2;
  STAMP(1, 2);
  __syncthreads();
  float* red = (float*)smem;
  {
    float* rw = red + wv * (64 * CV_REDSTR);
#pragma unroll
    for (int g = 0; g < 4; ++g) {
      f32x4 v0 = {acc0[4 * g], acc0[4 * g + 1], acc0[4 * g + 2], acc0[4 * g + 3]};
      f32x4 v1 = {acc1[4 * g], acc1[4 * g + 1], acc1[4 * g + 2], acc1[4 * g + 3]};
      *(f32x4*)(rw + r * CV_REDSTR + 8 * g + 4 * hh) = v0;
      *(f32x4*)(rw + (32 + r) * CV_REDSTR + 8 * g + 4 * hh) = v1;
    }
  }
  __syncthreads();
  {
    const int a = t >> 2, b0 = 8 * (t & 3);
    f32x4 s0 = {0.f, 0.f, 0.f, 0.f}, s1 = {0.f, 0.f, 0.f, 0.f};
#pragma unroll
    for (int w = 0; w < 4; ++w) {
      const float* rp = red + w * (64 * CV_REDSTR) + a * CV_REDSTR + b0;
      s0 += *(const f32x4*)(rp);
      s1 += *(const f32x4*)(rp + 4);
    }
    float yy[8] = {s0[0], s0[1], s0[2], s0[3], s1[0], s1[1], s1[2], s1[3]};
    u32x4 o;
#pragma unroll
    for (int i = 0; i < 4; ++i) {
      const float y0 = rbf(yy[2 * i]) + rbf(bflo(kvv[i]) * Dd);
      const float y1 = rbf(yy[2 * i + 1]) + rbf(bfhi(kvv[i]) * Dd);
      o[i] = pack2(y0, y1);
    }
    u16* y2T = (u16*)(ws + WS_Y2T) + (size_t)d * SEQ;
    *(u32x4*)(y2T + 8 * t) = o;
  }
  STAMP(1, 4);
}

__global__ __launch_bounds__(256, 4) void k2_kernel(const u16* __restrict__ ssmk, const u16* __restrict__ ssmD,
                                                    char* __restrict__ ws) {
  __shared__ __attribute__((aligned(16))) char smem[CV_SMEM];
  const int xcd = blockIdx.x & 7, bidx = blockIdx.x >> 3;
  if (bidx & 1) {
    conv_block(ssmk, ssmD, ws, smem, xcd * 64 + (bidx >> 1));
  } else {
    const int ab = bidx >> 1;
    const int wv = threadIdx.x >> 6;
    const int qt = xcd * 8 + (ab >> 3);
    const int head = (ab & 7) * 4 + wv;
    attn_wave(ws, smem, head, qt);
  }
}

#define K3_STAGE 32768
#define K3_CSTR 68
#define K3_YSTR 136

__global__ __launch_bounds__(256) void k3_kernel(const u16* __restrict__ x, const u16* __restrict__ wo,
                                                 const char* __restrict__ ws, u16* __restrict__ out) {
  __shared__ __attribute__((aligned(16))) char smem[4 * K3_STAGE];
  const int t = threadIdx.x;
  const int l = t & 63, wv = t >> 6;
  const int r = l & 31, hh = l >> 5;
  const int wm = wv & 1, wn = wv >> 1;
  const int tm = (blockIdx.x & 7) * 4 + ((blockIdx.x >> 3) & 3), tn = blockIdx.x >> 5;
  const int m0 = tm * 64, n0 = tn * 64;
  const u16* o = (const u16*)(ws + WS_O);
  STAMP(2, 0);

  const u16* y2T = (const u16*)(ws + WS_Y2T);
  const float* gp = (const float*)(ws + WS_G);
  u32x4 yv[2], xq[2];
  float gv[2];
#pragma unroll
  for (int i = 0; i < 2; ++i) {
    const int idx = t + 256 * i;
    const int rr = idx >> 3, ch = idx & 7;
    yv[i] = *(const u32x4*)(y2T + (size_t)(n0 + rr) * SEQ + m0 + ch * 8);
    xq[i] = *(const u32x4*)(x + (size_t)(m0 + rr) * DM + n0 + ch * 8);
    gv[i] = gp[m0 + rr];
  }
  {
    char* ldsw = smem + wv * 4096 + l * 16;
#pragma unroll
    for (int st = 0; st < 4; ++st) {
#pragma unroll
      for (int i = 0; i < 4; ++i) {
        const int ar = 16 * wv + 4 * i + (l >> 4);
        GLDS16(o + (size_t)(m0 + ar) * DM + st * 128 + (((l & 15) ^ (ar & 15)) * 8), ldsw + st * K3_STAGE + i * 1024);
      }
#pragma unroll
      for (int i = 0; i < 4; ++i) {
        const int br = 32 * wv + 8 * i + (l >> 3);
        GLDS16(wo + (size_t)(st * 128 + br) * DM + n0 + (((l & 7) ^ (((br >> 1) & 1) << 2)) * 8),
               ldsw + st * K3_STAGE + 16384 + i * 1024);
      }
    }
  }
  f32x16 acc;
#pragma unroll
  for (int e = 0; e < 16; ++e) acc[e] = 0.f;
  const int blk = (l >> 4) & 1, q4 = (l & 15) >> 2, p4 = l & 3;
  const int arow = wm * 32 + r;
  const int a_off = arow * 256;
  const int a_sw = arow & 15;
  const int b_off = 16384 + (8 * hh + q4) * 128 + (((wn * 4 + 2 * blk + (p4 >> 1)) ^ (((q4 >> 1) & 1) << 2)) * 16) +
                    (p4 & 1) * 8;
#pragma unroll
  for (int st = 0; st < 4; ++st) {
    if (st == 0) wait_vm<24>();
    else if (st == 1) wait_vm<16>();
    else if (st == 2) wait_vm<8>();
    else wait_vm<0>();
    RAWBAR();
    STAMP(2, 1 + st);
    const char* sb = smem + st * K3_STAGE;
    bf16x8 af[8], bfr[8];
#pragma unroll
    for (int ks = 0; ks < 8; ++ks) {
      af[ks] = *(const bf16x8*)(sb + a_off + (((2 * ks + hh) ^ a_sw) * 16));
      s16x4 lo = tr_read(sb + b_off + (16 * ks) * 128);
      s16x4 hi = tr_read(sb + b_off + (16 * ks + 4) * 128);
      bfr[ks] = cat_frag(lo, hi);
    }
    __builtin_amdgcn_sched_barrier(0);
#pragma unroll
    for (int ks = 0; ks < 8; ++ks) acc = MFMA32(af[ks], bfr[ks], acc);
    __builtin_amdgcn_sched_barrier(0);
  }
  __syncthreads();
  STAMP(2, 5);
  float* Cs = (float*)smem;
  char* Ys = smem + 64 * K3_CSTR * 4;
#pragma unroll
  for (int e = 0; e < 16; ++e) {
    const int row = wm * 32 + (e & 3) + 8 * (e >> 2) + 4 * hh;
    Cs[row * K3_CSTR + wn * 32 + r] = acc[e];
  }
#pragma unroll
  for (int i = 0; i < 2; ++i) {
    const int idx = t + 256 * i;
    const int dr = idx >> 3, ch = idx & 7;
    u32x2 a = {yv[i][0], yv[i][1]}, b = {yv[i][2], yv[i][3]};
    *(u32x2*)(Ys + dr * K3_YSTR + ch * 16) = a;
    *(u32x2*)(Ys + dr * K3_YSTR + ch * 16 + 8) = b;
  }
  __syncthreads();
#pragma unroll
  for (int i = 0; i < 2; ++i) {
    const int idx = t + 256 * i;
    const int row = idx >> 3, ch = idx & 7;
    const int s = m0 + row;
    const float g = gv[i];
    const float omg = rbf(1.f - g);
    const f32x4 c0 = *(const f32x4*)(Cs + row * K3_CSTR + ch * 8);
    const f32x4 c1 = *(const f32x4*)(Cs + row * K3_CSTR + ch * 8 + 4);
    float bb[8] = {c0[0], c0[1], c0[2], c0[3], c1[0], c1[1], c1[2], c1[3]};
    float res[8];
#pragma unroll
    for (int j = 0; j < 8; ++j) {
      const float y2 = bf2f(*(const u16*)(Ys + (ch * 8 + j) * K3_YSTR + row * 2));
      const float qv = (j & 1) ? bfhi(xq[i][j >> 1]) : bflo(xq[i][j >> 1]);
      const float a = rbf(qv * y2);
      const float ga = rbf(g * a);
      const float gb = rbf(omg * rbf(bb[j]));
      res[j] = ga + gb;
    }
    u32x4 ov;
    ov[0] = pack2(res[0], res[1]);
    ov[1] = pack2(res[2], res[3]);
    ov[2] = pack2(res[4], res[5]);
    ov[3] = pack2(res[6], res[7]);
    *(u32x4*)(out + (size_t)s * DM + n0 + ch * 8) = ov;
  }
  STAMP(2, 6);
}

extern "C" void kernel_launch(void* const* d_in, const int* in_sizes, int n_in, void* d_out, int out_size,
                              void* d_ws, size_t ws_size, hipStream_t stream) {
  const u16* A_q = (const u16*)d_in[0];
  const u16* A_k = (const u16*)d_in[1];
  const u16* A_v = (const u16*)d_in[2];
  const u16* A_shift = (const u16*)d_in[3];
  const u16* A_ssmk = (const u16*)d_in[4];
  const u16* A_ssmD = (const u16*)d_in[5];
  const u16* B_wqkv = (const u16*)d_in[7];
  const u16* B_wo = (const u16*)d_in[8];
  const u16* gate_w = (const u16*)d_in[9];
  char* ws = (char*)d_ws;
  k1_kernel<<<256, 256, 0, stream>>>(A_q, A_k, A_v, A_shift, B_wqkv, gate_w, ws);
  k2_kernel<<<1024, 256, 0, stream>>>(A_ssmk, A_ssmD, ws);
  k3_kernel<<<256, 256, 0, stream>>>(A_q, B_wo, ws, (u16*)d_out);
}
```

```cpp
#include <hip/hip_runtime.h>
#include <hip/hip_bf16.h>
#include <stdint.h>

#define DI __device__ __forceinline__
typedef __attribute__((ext_vector_type(8))) __bf16 bf16x8;
typedef __attribute__((ext_vector_type(4))) __bf16 bf16x4;
typedef __attribute__((ext_vector_type(2))) __bf16 bf16x2;
typedef __attribute__((ext_vector_type(4))) short s16x4;
typedef __attribute__((ext_vector_type(16))) float f32x16;
typedef __attribute__((ext_vector_type(4))) float f32x4;
typedef __attribute__((ext_vector_type(2))) float f32x2;
typedef __attribute__((ext_vector_type(4))) unsigned u32x4;
typedef __attribute__((ext_vector_type(2))) unsigned u32x2;
typedef unsigned short u16;

#define SEQ 2048
#define DM 512

#define MFMA32(a, b, c) __builtin_amdgcn_mfma_f32_32x32x16_bf16((a), (b), (c), 0, 0, 0)

DI float bf2f(u16 v) { return __uint_as_float(((unsigned)v) << 16); }
DI float bflo(unsigned v) { return __uint_as_float(v << 16); }
DI float bfhi(unsigned v) { return __uint_as_float(v & 0xffff0000u); }
DI unsigned pack2(float a, float b) {
  f32x2 f = {a, b};
  bf16x2 r = __builtin_convertvector(f, bf16x2);
  return __builtin_bit_cast(unsigned, r);
}
DI float rbf(float a) {
  return (float)((__bf16)a);
}
DI s16x4 tr_read(const char* p) {
  typedef __attribute__((address_space(3))) s16x4* lp;
  return __builtin_amdgcn_ds_read_tr16_b64_v4i16((lp)(p));
}
DI bf16x8 cat_frag(s16x4 lo, s16x4 hi) {
  typedef __attribute__((ext_vector_type(8))) short s16x8;
  s16x8 v = __builtin_shufflevector(lo, hi, 0, 1, 2, 3, 4, 5, 6, 7);
  return __builtin_bit_cast(bf16x8, v);
}

#define STAMP(k, i) do {} while (0)
#define WS_Q 0
#define WS_K (2u << 20)
#define WS_VT (4u << 20)
#define WS_KVT (6u << 20)
#define WS_Y2T (8u << 20)
#define WS_O (10u << 20)
#define WS_G (12u << 20)

#define GLDS16(g, l) __builtin_amdgcn_global_load_lds((const unsigned*)(g), (unsigned*)(l), 16, 0, 0)
#define WAIT_V(n) asm volatile("s_waitcnt vmcnt(" #n ")" ::: "memory")
#define RAWBAR() do { asm volatile("s_waitcnt lgkmcnt(0)" ::: "memory"); __builtin_amdgcn_s_barrier(); } while (0)
#define K1_STAGE 32768

template <int N>
DI void wait_vm() {
  if constexpr (N == 0) WAIT_V(0);
  else if constexpr (N == 8) WAIT_V(8);
  else if constexpr (N == 16) WAIT_V(16);
  else if constexpr (N == 24) WAIT_V(24);
}

template <bool ISV>
DI void k1_gemm(const u16* __restrict__ x, const u16* __restrict__ w, char* __restrict__ ws, char* smem, int tm,
                int tn) {
  const int t = threadIdx.x;
  const int l = t & 63, wv = t >> 6;
  const int r = l & 31, hh = l >> 5;
  const int wm = wv & 1, wn = wv >> 1;
  const int m0 = tm * 128, n0 = tn * 128;

  const int arow0 = 32 * wv + (l >> 3);
  const int brow0 = 16 * wv + (l >> 4);
  const u16* xg[4];
  const u16* wg[4];
#pragma unroll
  for (int i = 0; i < 4; ++i) {
    const int ar = arow0 + 8 * i;
    xg[i] = x + (size_t)(m0 + ar) * DM + (((l & 7) ^ ((ar >> 1) & 7)) * 8);
    const int br = brow0 + 4 * i;
    wg[i] = w + (size_t)br * 1536 + n0 + (((l & 15) ^ ((br & 3) << 2)) * 8);
  }
  char* ldsw = smem + wv * 4096 + l * 16;

  auto issue = [&](int kt) {
    char* st = ldsw + (kt & 3) * K1_STAGE;
#pragma unroll
    for (int i = 0; i < 4; ++i) GLDS16(xg[i] + kt * 64, st + i * 1024);
#pragma unroll
    for (int i = 0; i < 4; ++i) GLDS16(wg[i] + (size_t)kt * 64 * 1536, st + 16384 + i * 1024);
  };

  f32x16 acc[2][2];
#pragma unroll
  for (int i = 0; i < 2; ++i)
#pragma unroll
    for (int j = 0; j < 2; ++j)
#pragma unroll
      for (int e = 0; e < 16; ++e) acc[i][j][e] = 0.f;

  const int blk = (l >> 4) & 1, q4 = (l & 15) >> 2, p4 = l & 3;
  const int a_row_off = (wm * 64 + r) * 128;
  const int a_sw = (r >> 1) & 7;
  const int b_base = 16384 + (8 * hh + q4) * 256 + (p4 & 1) * 8;
  const int b_ch0 = (wn * 8 + 2 * blk + (p4 >> 1));

  STAMP(0, 0);
  issue(0);
  issue(1);
  issue(2);
#pragma unroll
  for (int kt = 0; kt < 8; ++kt) {
    if (kt <= 5) wait_vm<16>();
    else if (kt == 6) wait_vm<8>();
    else wait_vm<0>();
    RAWBAR();
    STAMP(0, 1 + kt);
    if (kt + 3 <= 7) issue(kt + 3);
    const char* st = smem + (kt & 3) * K1_STAGE;
    bf16x8 af[2][2], bfr[2][2];
    auto ldfr = [&](int ks, bf16x8(&a)[2], bf16x8(&b)[2]) {
#pragma unroll
      for (int i = 0; i < 2; ++i)
        a[i] = *(const bf16x8*)(st + a_row_off + i * 32 * 128 + (((2 * ks + hh) ^ a_sw) * 16));
#pragma unroll
      for (int j = 0; j < 2; ++j) {
        const int ch = ((b_ch0 + 4 * j) ^ (q4 << 2)) * 16;
        s16x4 lo = tr_read(st + b_base + (16 * ks) * 256 + ch);
        s16x4 hi = tr_read(st + b_base + (16 * ks + 4) * 256 + ch);
        b[j] = cat_frag(lo, hi);
      }
    };
    ldfr(0, af[0], bfr[0]);
#pragma unroll
    for (int ks = 0; ks < 4; ++ks) {
      if (ks < 3) ldfr(ks + 1, af[(ks + 1) & 1], bfr[(ks + 1) & 1]);
      __builtin_amdgcn_sched_barrier(0);
#pragma unroll
      for (int i = 0; i < 2; ++i)
#pragma unroll
        for (int j = 0; j < 2; ++j) {
          if (ISV)
            acc[i][j] = MFMA32(af[ks & 1][i], bfr[ks & 1][j], acc[i][j]);
          else
            acc[i][j] = MFMA32(bfr[ks & 1][j], af[ks & 1][i], acc[i][j]);
        }
      __builtin_amdgcn_sched_barrier(0);
    }
  }

  STAMP(0, 9);
  if (ISV) {
    u16* Vt = (u16*)(ws + WS_VT);
#pragma unroll
    for (int i = 0; i < 2; ++i)
#pragma unroll
      for (int j = 0; j < 2; ++j) {
        const int n = (n0 - 1024) + wn * 64 + j * 32 + r;
        const int tok0 = m0 + wm * 64 + i * 32;
        u16* dst = Vt + (size_t)n * SEQ + tok0 + 8 * hh;
#pragma unroll
        for (int bb = 0; bb < 2; ++bb) {
          u32x4 v;
          v[0] = pack2(acc[i][j][8 * bb + 0], acc[i][j][8 * bb + 1]);
          v[1] = pack2(acc[i][j][8 * bb + 2], acc[i][j][8 * bb + 3]);
          v[2] = pack2(acc[i][j][8 * bb + 4], acc[i][j][8 * bb + 5]);
          v[3] = pack2(acc[i][j][8 * bb + 6], acc[i][j][8 * bb + 7]);
          *(u32x4*)(dst + 16 * bb) = v;
        }
      }
  } else {
    u16* base = (u16*)(ws + (n0 >= 512 ? WS_K : WS_Q));
    const int nn0 = n0 & 511;
#pragma unroll
    for (int i = 0; i < 2; ++i)
#pragma unroll
      for (int j = 0; j < 2; ++j) {
        const int tok = m0 + wm * 64 + i * 32 + r;
        const int nb = nn0 + wn * 64 + j * 32;
        const int head = nb >> 4;
#pragma unroll
        for (int g = 0; g < 4; ++g) {
          u32x2 v;
          v[0] = pack2(acc[i][j][4 * g + 0], acc[i][j][4 * g + 1]);
          v[1] = pack2(acc[i][j][4 * g + 2], acc[i][j][4 * g + 3]);
          *(u32x2*)(base + ((size_t)(head + (g >> 1)) * SEQ + tok) * 16 + 8 * (g & 1) + 4 * hh) = v;
        }
      }
  }
  STAMP(0, 10);
}

__global__ __launch_bounds__(256) void k1_kernel(const u16* __restrict__ x, const u16* __restrict__ kin,
                                                 const u16* __restrict__ vin, const u16* __restrict__ shiftk,
                                                 const u16* __restrict__ wqkv, const u16* __restrict__ gatew,
                                                 char* __restrict__ ws) {
  __shared__ __attribute__((aligned(16))) char smem[4 * K1_STAGE];
  const int xcd = blockIdx.x & 7, bidx = blockIdx.x >> 3;
  if (bidx < 24) {
    const int tm = 2 * xcd + (bidx & 1), tn = bidx >> 1;
    if (tn >= 8)
      k1_gemm<true>(x, wqkv, ws, smem, tm, tn);
    else
      k1_gemm<false>(x, wqkv, ws, smem, tm, tn);
    return;
  }
  const int l = threadIdx.x & 63, wv = threadIdx.x >> 6;
  const int t = threadIdx.x;
  const int pb = xcd * 8 + (bidx - 24);
  {
    const int ch = t & 15, rg = t >> 4;
    u32x4 kr[2][7], vr[2][4], sk[2][4];
#pragma unroll
    for (int h2 = 0; h2 < 2; ++h2) {
      const int tile = pb * 2 + h2;
      const int s0 = (tile >> 2) * 64, d0 = (tile & 3) * 128;
      const int sb = s0 + rg * 4;
      const int dd = d0 + ch * 8;
#pragma unroll
      for (int i = 0; i < 7; ++i) {
        const int s = sb - 3 + i;
        u32x4 z = {0u, 0u, 0u, 0u};
        kr[h2][i] = (s >= 0) ? *(const u32x4*)(kin + (size_t)s * DM + dd) : z;
      }
#pragma unroll
      for (int i = 0; i < 4; ++i) vr[h2][i] = *(const u32x4*)(vin + (size_t)(sb + i) * DM + dd);
#pragma unroll
      for (int i = 0; i < 4; ++i) sk[h2][i] = *(const u32x4*)(shiftk + (size_t)dd * 4 + i * 8);
    }
#pragma unroll
    for (int h2 = 0; h2 < 2; ++h2) {
#pragma unroll
      for (int c = 0; c < 8; ++c) {
        const unsigned s01 = sk[h2][c >> 1][(c & 1) * 2], s23 = sk[h2][c >> 1][(c & 1) * 2 + 1];
        const float sk0 = bflo(s01), sk1 = bfhi(s01), sk2 = bflo(s23), sk3 = bfhi(s23);
        float kk[7];
#pragma unroll
        for (int i = 0; i < 7; ++i) kk[i] = (c & 1) ? bfhi(kr[h2][i][c >> 1]) : bflo(kr[h2][i][c >> 1]);
        float res[4];
#pragma unroll
        for (int i = 0; i < 4; ++i) {
          float a = rbf(kk[i] * sk3);
          a = rbf(a + rbf(kk[i + 1] * sk2));
          a = rbf(a + rbf(kk[i + 2] * sk1));
          a = rbf(a + rbf(kk[i + 3] * sk0));
          const float vv = (c & 1) ? bfhi(vr[h2][i][c >> 1]) : bflo(vr[h2][i][c >> 1]);
          res[i] = a * vv;
        }
        u32x2 o = {pack2(res[0], res[1]), pack2(res[2], res[3])};
        *(u32x2*)(smem + h2 * 17408 + (ch * 8 + c) * 136 + rg * 8) = o;
      }
    }
    __syncthreads();
    u16* kvT = (u16*)(ws + WS_KVT);
#pragma unroll
    for (int h2 = 0; h2 < 2; ++h2) {
      const int tile = pb * 2 + h2;
      const int s0 = (tile >> 2) * 64, d0 = (tile & 3) * 128;
#pragma unroll
      for (int i = 0; i < 4; ++i) {
        const int idx = t + 256 * i;
        const int dr = idx >> 3, c8 = idx & 7;
        const u32x2 a = *(const u32x2*)(smem + h2 * 17408 + dr * 136 + c8 * 16);
        const u32x2 b = *(const u32x2*)(smem + h2 * 17408 + dr * 136 + c8 * 16 + 8);
        u32x4 v = {a[0], a[1], b[0], b[1]};
        *(u32x4*)(kvT + (size_t)(d0 + dr) * SEQ + s0 + c8 * 8) = v;
      }
    }
  }
  {
    const int wid = pb * 4 + wv;
    float* g = (float*)(ws + WS_G);
    const u32x4 gw = *(const u32x4*)(gatew + l * 8);
    u32x4 xv[8];
#pragma unroll
    for (int it = 0; it < 8; ++it) xv[it] = *(const u32x4*)(x + (size_t)(wid * 8 + it) * DM + l * 8);
    float sums[8];
#pragma unroll
    for (int it = 0; it < 8; ++it) {
      float sum = 0.f;
#pragma unroll
      for (int i = 0; i < 4; ++i) {
        sum += rbf(bflo(xv[it][i]) * bflo(gw[i]));
        sum += rbf(bfhi(xv[it][i]) * bfhi(gw[i]));
      }
      sums[it] = sum;
    }
#pragma unroll
    for (int o = 32; o >= 1; o >>= 1)
#pragma unroll
      for (int it = 0; it < 8; ++it) sums[it] += __shfl_xor(sums[it], o);
    if (l < 8) {
      float sv = sums[0];
#pragma unroll
      for (int it = 1; it < 8; ++it) sv = (l == it) ? sums[it] : sv;
      const float z = rbf(sv);
      g[wid * 8 + l] = rbf(1.f / (1.f + __expf(-z)));
    }
  }
}

#define CV_KVSTR 80
#define CV_HSTR 4160
#define CV_HBYTES (8 * CV_HSTR)
#define CV_REDSTR 36
#define CV_SMEM 40960

#define MFMA16(a, b, c) __builtin_amdgcn_mfma_f32_16x16x16bf16_1k((a), (b), (c), 0, 0, 0)
DI void attn_block(const char* __restrict__ ws, char* smem, int head, int qb) {
  const int l = threadIdx.x & 63, wv = threadIdx.x >> 6;
  const int c = l & 15, gq = l >> 4;
  const u16* Qh = (const u16*)(ws + WS_Q) + (size_t)head * SEQ * 16;
  const u16* Kh = (const u16*)(ws + WS_K) + (size_t)head * SEQ * 16;
  const u16* Vh = (const u16*)(ws + WS_VT) + (size_t)head * 16 * SEQ;
  u16* O = (u16*)(ws + WS_O);
  const int q0 = qb + 16 * wv;
  const int kt0 = q0 >= 256 ? 0 : ((256 - q0) >> 4);
  STAMP(1, 0);
  const s16x4 qf = *(const s16x4*)(Qh + (size_t)(q0 + c) * 16 + 4 * gq);
  char* ks_ = smem;
  char* vs_ = smem + 10240;
  {
    const int key = l >> 1;
    const u16* ksrc = Kh + (size_t)key * 16 + (((l & 1) ^ ((key >> 3) & 1)) * 8);
    const int dd = l >> 2;
    const u16* vsrc = Vh + (size_t)dd * SEQ + (((l & 3) ^ ((dd >> 2) & 3)) * 8);
#pragma unroll
    for (int i = 0; i < 3; ++i) {
      const int u = wv + 4 * i;
      if (u < 10) {
        int key0 = qb - 256 + 32 * u;
        key0 = key0 < 0 ? 0 : key0;
        GLDS16(ksrc + (size_t)key0 * 16, ks_ + u * 1024 + l * 16);
        GLDS16(vsrc + key0, vs_ + u * 1024 + l * 16);
      }
    }
  }
  WAIT_V(0);
  RAWBAR();
  STAMP(1, 1);
  const char* krd = ks_ + (16 * wv + c) * 32 + (((gq >> 1) ^ ((c >> 3) & 1)) * 16) + (gq & 1) * 8;
  f32x4 sc[17];
  float mx = -3.0e38f;
  const f32x4 zero = {0.f, 0.f, 0.f, 0.f};
#pragma unroll
  for (int kt = 0; kt < 17; ++kt) {
    const s16x4 kf = *(const s16x4*)(krd + kt * 512);
    f32x4 s = MFMA16(kf, qf, zero);
    if (kt == 0) {
#pragma unroll
      for (int e = 0; e < 4; ++e) s[e] = (4 * gq + e > c) ? s[e] : -3.0e38f;
    }
    if (kt == 16) {
#pragma unroll
      for (int e = 0; e < 4; ++e) s[e] = (4 * gq + e <= c) ? s[e] : -3.0e38f;
    }
    if (kt < 16 && kt < kt0) {
      s = (f32x4){-3.0e38f, -3.0e38f, -3.0e38f, -3.0e38f};
    }
    sc[kt] = s;
    mx = fmaxf(fmaxf(s[0], s[1]), mx);
    mx = fmaxf(fmaxf(s[2], s[3]), mx);
  }
  mx = fmaxf(mx, __shfl_xor(mx, 16));
  mx = fmaxf(mx, __shfl_xor(mx, 32));
  const float c1 = 0.25f * 1.44269504088896340736f;
  const float mc = mx * c1;
  const f32x2 c1v = {c1, c1}, nmcv = {-mc, -mc};
  f32x2 sum2 = {0.f, 0.f};
  f32x4 oacc = zero;
  STAMP(1, 2);
  const int vsw = (c >> 2) & 3;
#pragma unroll
  for (int kt = 0; kt < 17; ++kt) {
    f32x2 a0 = __builtin_elementwise_fma((f32x2){sc[kt][0], sc[kt][1]}, c1v, nmcv);
    f32x2 a1 = __builtin_elementwise_fma((f32x2){sc[kt][2], sc[kt][3]}, c1v, nmcv);
    f32x2 e0, e1;
    e0[0] = __builtin_amdgcn_exp2f(a0[0]);
    e0[1] = __builtin_amdgcn_exp2f(a0[1]);
    e1[0] = __builtin_amdgcn_exp2f(a1[0]);
    e1[1] = __builtin_amdgcn_exp2f(a1[1]);
    sum2 += e0;
    sum2 += e1;
    u32x2 pp = {pack2(e0[0], e0[1]), pack2(e1[0], e1[1])};
    const int T = wv + kt;
    const s16x4 vf = *(const s16x4*)(vs_ + (T >> 1) * 1024 + c * 64 + (((2 * (T & 1) + (gq & 1)) ^ vsw) * 16) +
                                     (gq >> 1) * 8);
    oacc = MFMA16(vf, __builtin_bit_cast(s16x4, pp), oacc);
  }
  STAMP(1, 3);
  float sum = sum2[0] + sum2[1];
  sum += __shfl_xor(sum, 16);
  sum += __shfl_xor(sum, 32);
  const float inv = 1.f / sum;
  u32x2 v0 = {pack2(oacc[0] * inv, oacc[1] * inv), pack2(oacc[2] * inv, oacc[3] * inv)};
  *(u32x2*)(O + (size_t)(q0 + c) * DM + head * 16 + 4 * gq) = v0;
  STAMP(1, 4);
}

DI void conv_block(const u16* __restrict__ ssmk, const u16* __restrict__ ssmD, char* __restrict__ ws, char* smem,
                   int d) {
  const int t = threadIdx.x;
  const int l = t & 63, wv = t >> 6;
  const int r = l & 31, hh = l >> 5;
  char* hs = smem;
  char* kvs = smem + CV_HBYTES;
  const u16* kvT = (const u16*)(ws + WS_KVT) + (size_t)d * SEQ;
  const u16* hrow = ssmk + (size_t)d * SEQ;
  STAMP(1, 0);
  const u32x4 kvv = *(const u32x4*)(kvT + 8 * t);
  const u32x4 whi = *(const u32x4*)(hrow + 8 * t);
  const u32x4 wlo0 = *(const u32x4*)(hrow + 8 * (t > 0 ? t - 1 : 0));
  const float Dd = bf2f(ssmD[d]);
  *(u32x4*)(kvs + ((t >> 2) + 32) * CV_KVSTR + (t & 3) * 16) = kvv;
  if (t < 128) {
    u32x4 z = {0u, 0u, 0u, 0u};
    *(u32x4*)(kvs + (t >> 2) * CV_KVSTR + (t & 3) * 16) = z;
  }
  {
    const int E = t;
    u32x4 wlo = wlo0;
    if (E == 0) wlo = (u32x4){0u, 0u, 0u, 0u};
    unsigned z[8];
#pragma unroll
    for (int i = 0; i < 4; ++i) {
      const unsigned a = whi[3 - i];
      z[i] = (a >> 16) | (a << 16);
      const unsigned b = wlo[3 - i];
      z[4 + i] = (b >> 16) | (b << 16);
    }
    const int M = 255 - E;
#pragma unroll
    for (int p = 0; p < 8; ++p) {
      const int o = 7 - p;
      u32x4 v;
      if ((o & 1) == 0) {
        v[0] = z[o / 2];
        v[1] = z[o / 2 + 1];
        v[2] = z[o / 2 + 2];
        v[3] = z[o / 2 + 3];
      } else {
        v[0] = (z[o / 2] >> 16) | (z[o / 2 + 1] << 16);
        v[1] = (z[o / 2 + 1] >> 16) | (z[o / 2 + 2] << 16);
        v[2] = (z[o / 2 + 2] >> 16) | (z[o / 2 + 3] << 16);
        v[3] = (z[o / 2 + 3] >> 16) | (z[o / 2 + 4] << 16);
      }
      *(u32x4*)(hs + p * CV_HSTR + 16 * M) = v;
    }
    if (t < 24) {
      u32x4 zz = {0u, 0u, 0u, 0u};
      *(u32x4*)(hs + (t & 7) * CV_HSTR + 16 * (256 + (t >> 3))) = zz;
    }
  }
  __syncthreads();
  STAMP(1, 1);
  f32x16 acc0, acc1, acc2;
#pragma unroll
  for (int e = 0; e < 16; ++e) {
    acc0[e] = 0.f;
    acc1[e] = 0.f;
    acc2[e] = 0.f;
  }
  const char* abase = hs + (r & 7) * CV_HSTR + 16 * (255 - (r >> 3) + hh) - 64 * wv;
  const char* bbase = kvs + (r + 32 - wv) * CV_KVSTR + 16 * hh;
  {
    bf16x8 fa[2][2], fb[2][2];
    auto ldc = [&](int step, bf16x8(&a)[2], bf16x8(&b)[2]) {
      const int i = step >> 1, ks = step & 1;
      a[0] = *(const bf16x8*)(abase - 256 * i + 32 * ks);
      a[1] = *(const bf16x8*)(abase - 256 * i - 2048 + 32 * ks);
      b[1] = *(const bf16x8*)(bbase + (32 - 4 * i) * CV_KVSTR + 32 * ks);
      b[0] = *(const bf16x8*)(bbase - (4 * i) * CV_KVSTR + 32 * ks);
    };
    ldc(0, fa[0], fb[0]);
#pragma unroll
    for (int step = 0; step < 16; ++step) {
      if (step < 15) ldc(step + 1, fa[(step + 1) & 1], fb[(step + 1) & 1]);
      __builtin_amdgcn_sched_barrier(0);
      acc1 = MFMA32(fa[step & 1][0], fb[step & 1][1], acc1);
      acc0 = MFMA32(fa[step & 1][0], fb[step & 1][0], acc0);
      acc2 = MFMA32(fa[step & 1][1], fb[step & 1][0], acc2);
      __builtin_amdgcn_sched_barrier(0);
    }
  }
  acc1 += acc2;
  STAMP(1, 2);
  __syncthreads();
  float* red = (float*)smem;
  {
    float* rw = red + wv * (64 * CV_REDSTR);
#pragma unroll
    for (int g = 0; g < 4; ++g) {
      f32x4 v0 = {acc0[4 * g], acc0[4 * g + 1], acc0[4 * g + 2], acc0[4 * g + 3]};
      f32x4 v1 = {acc1[4 * g], acc1[4 * g + 1], acc1[4 * g + 2], acc1[4 * g + 3]};
      *(f32x4*)(rw + r * CV_REDSTR + 8 * g + 4 * hh) = v0;
      *(f32x4*)(rw + (32 + r) * CV_REDSTR + 8 * g + 4 * hh) = v1;
    }
  }
  __syncthreads();
  {
    const int a = t >> 2, b0 = 8 * (t & 3);
    f32x4 s0 = {0.f, 0.f, 0.f, 0.f}, s1 = {0.f, 0.f, 0.f, 0.f};
#pragma unroll
    for (int w = 0; w < 4; ++w) {
      const float* rp = red + w * (64 * CV_REDSTR) + a * CV_REDSTR + b0;
      s0 += *(const f32x4*)(rp);
      s1 += *(const f32x4*)(rp + 4);
    }
    float yy[8] = {s0[0], s0[1], s0[2], s0[3], s1[0], s1[1], s1[2], s1[3]};
    u32x4 o;
#pragma unroll
    for (int i = 0; i < 4; ++i) {
      const float y0 = rbf(yy[2 * i]) + rbf(bflo(kvv[i]) * Dd);
      const float y1 = rbf(yy[2 * i + 1]) + rbf(bfhi(kvv[i]) * Dd);
      o[i] = pack2(y0, y1);
    }
    u16* y2T = (u16*)(ws + WS_Y2T) + (size_t)d * SEQ;
    *(u32x4*)(y2T + 8 * t) = o;
  }
  STAMP(1, 4);
}

__global__ __launch_bounds__(256, 4) void k2_kernel(const u16* __restrict__ ssmk, const u16* __restrict__ ssmD,
                                                    char* __restrict__ ws) {
  __shared__ __attribute__((aligned(16))) char smem[CV_SMEM];
  const int xcd = blockIdx.x & 7, bidx = blockIdx.x >> 3;
  const int b3 = bidx / 3, br = bidx - 3 * b3;
  if (br == 0) {
    conv_block(ssmk, ssmD, ws, smem, xcd * 64 + b3);
  } else {
    const int ab = b3 * 2 + (br - 1);
    const int qb = (xcd * 4 + (ab >> 5)) * 64;
    const int head = ab & 31;
    attn_block(ws, smem, head, qb);
  }
}

#define K3_STAGE 32768
#define K3_CSTR 68
#define K3_YSTR 136

__global__ __launch_bounds__(256) void k3_kernel(const u16* __restrict__ x, const u16* __restrict__ wo,
                                                 const char* __restrict__ ws, u16* __restrict__ out) {
  __shared__ __attribute__((aligned(16))) char smem[4 * K3_STAGE];
  const int t = threadIdx.x;
  const int l = t & 63, wv = t >> 6;
  const int r = l & 31, hh = l >> 5;
  const int wm = wv & 1, wn = wv >> 1;
  const int tm = (blockIdx.x & 7) * 4 + ((blockIdx.x >> 3) & 3), tn = blockIdx.x >> 5;
  const int m0 = tm * 64, n0 = tn * 64;
  const u16* o = (const u16*)(ws + WS_O);
  STAMP(2, 0);

  const u16* y2T = (const u16*)(ws + WS_Y2T);
  const float* gp = (const float*)(ws + WS_G);
  u32x4 yv[2], xq[2];
  float gv[2];
#pragma unroll
  for (int i = 0; i < 2; ++i) {
    const int idx = t + 256 * i;
    const int rr = idx >> 3, ch = idx & 7;
    yv[i] = *(const u32x4*)(y2T + (size_t)(n0 + rr) * SEQ + m0 + ch * 8);
    xq[i] = *(const u32x4*)(x + (size_t)(m0 + rr) * DM + n0 + ch * 8);
    gv[i] = gp[m0 + rr];
  }
  {
    char* ldsw = smem + wv * 4096 + l * 16;
#pragma unroll
    for (int st = 0; st < 4; ++st) {
#pragma unroll
      for (int i = 0; i < 4; ++i) {
        const int ar = 16 * wv + 4 * i + (l >> 4);
        GLDS16(o + (size_t)(m0 + ar) * DM + st * 128 + (((l & 15) ^ (ar & 15)) * 8), ldsw + st * K3_STAGE + i * 1024);
      }
#pragma unroll
      for (int i = 0; i < 4; ++i) {
        const int br = 32 * wv + 8 * i + (l >> 3);
        GLDS16(wo + (size_t)(st * 128 + br) * DM + n0 + (((l & 7) ^ (((br >> 1) & 1) << 2)) * 8),
               ldsw + st * K3_STAGE + 16384 + i * 1024);
      }
    }
  }
  f32x16 acc;
#pragma unroll
  for (int e = 0; e < 16; ++e) acc[e] = 0.f;
  const int blk = (l >> 4) & 1, q4 = (l & 15) >> 2, p4 = l & 3;
  const int arow = wm * 32 + r;
  const int a_off = arow * 256;
  const int a_sw = arow & 15;
  const int b_off = 16384 + (8 * hh + q4) * 128 + (((wn * 4 + 2 * blk + (p4 >> 1)) ^ (((q4 >> 1) & 1) << 2)) * 16) +
                    (p4 & 1) * 8;
#pragma unroll
  for (int st = 0; st < 4; ++st) {
    if (st == 0) wait_vm<24>();
    else if (st == 1) wait_vm<16>();
    else if (st == 2) wait_vm<8>();
    else wait_vm<0>();
    RAWBAR();
    STAMP(2, 1 + st);
    const char* sb = smem + st * K3_STAGE;
    bf16x8 af[8], bfr[8];
#pragma unroll
    for (int ks = 0; ks < 8; ++ks) {
      af[ks] = *(const bf16x8*)(sb + a_off + (((2 * ks + hh) ^ a_sw) * 16));
      s16x4 lo = tr_read(sb + b_off + (16 * ks) * 128);
      s16x4 hi = tr_read(sb + b_off + (16 * ks + 4) * 128);
      bfr[ks] = cat_frag(lo, hi);
    }
    __builtin_amdgcn_sched_barrier(0);
#pragma unroll
    for (int ks = 0; ks < 8; ++ks) acc = MFMA32(af[ks], bfr[ks], acc);
    __builtin_amdgcn_sched_barrier(0);
  }
  __syncthreads();
  STAMP(2, 5);
  float* Cs = (float*)smem;
  char* Ys = smem + 64 * K3_CSTR * 4;
#pragma unroll
  for (int e = 0; e < 16; ++e) {
    const int row = wm * 32 + (e & 3) + 8 * (e >> 2) + 4 * hh;
    Cs[row * K3_CSTR + wn * 32 + r] = acc[e];
  }
#pragma unroll
  for (int i = 0; i < 2; ++i) {
    const int idx = t + 256 * i;
    const int dr = idx >> 3, ch = idx & 7;
    u32x2 a = {yv[i][0], yv[i][1]}, b = {yv[i][2], yv[i][3]};
    *(u32x2*)(Ys + dr * K3_YSTR + ch * 16) = a;
    *(u32x2*)(Ys + dr * K3_YSTR + ch * 16 + 8) = b;
  }
  __syncthreads();
#pragma unroll
  for (int i = 0; i < 2; ++i) {
    const int idx = t + 256 * i;
    const int row = idx >> 3, ch = idx & 7;
    const int s = m0 + row;
    const float g = gv[i];
    const float omg = rbf(1.f - g);
    const f32x4 c0 = *(const f32x4*)(Cs + row * K3_CSTR + ch * 8);
    const f32x4 c1 = *(const f32x4*)(Cs + row * K3_CSTR + ch * 8 + 4);
    float bb[8] = {c0[0], c0[1], c0[2], c0[3], c1[0], c1[1], c1[2], c1[3]};
    float res[8];
#pragma unroll
    for (int j = 0; j < 8; ++j) {
      const float y2 = bf2f(*(const u16*)(Ys + (ch * 8 + j) * K3_YSTR + row * 2));
      const float qv = (j & 1) ? bfhi(xq[i][j >> 1]) : bflo(xq[i][j >> 1]);
      const float a = rbf(qv * y2);
      const float ga = rbf(g * a);
      const float gb = rbf(omg * rbf(bb[j]));
      res[j] = ga + gb;
    }
    u32x4 ov;
    ov[0] = pack2(res[0], res[1]);
    ov[1] = pack2(res[2], res[3]);
    ov[2] = pack2(res[4], res[5]);
    ov[3] = pack2(res[6], res[7]);
    *(u32x4*)(out + (size_t)s * DM + n0 + ch * 8) = ov;
  }
  STAMP(2, 6);
}

extern "C" void kernel_launch(void* const* d_in, const int* in_sizes, int n_in, void* d_out, int out_size,
                              void* d_ws, size_t ws_size, hipStream_t stream) {
  const u16* A_q = (const u16*)d_in[0];
  const u16* A_k = (const u16*)d_in[1];
  const u16* A_v = (const u16*)d_in[2];
  const u16* A_shift = (const u16*)d_in[3];
  const u16* A_ssmk = (const u16*)d_in[4];
  const u16* A_ssmD = (const u16*)d_in[5];
  const u16* B_wqkv = (const u16*)d_in[7];
  const u16* B_wo = (const u16*)d_in[8];
  const u16* gate_w = (const u16*)d_in[9];
  char* ws = (char*)d_ws;
  k1_kernel<<<256, 256, 0, stream>>>(A_q, A_k, A_v, A_shift, B_wqkv, gate_w, ws);
  k2_kernel<<<1536, 256, 0, stream>>>(A_ssmk, A_ssmD, ws);
  k3_kernel<<<256, 256, 0, stream>>>(A_q, B_wo, ws, (u16*)d_out);
}
```

```cpp
#include <hip/hip_runtime.h>
#include <hip/hip_bf16.h>
#include <stdint.h>

#define DI __device__ __forceinline__
typedef __attribute__((ext_vector_type(8))) __bf16 bf16x8;
typedef __attribute__((ext_vector_type(4))) __bf16 bf16x4;
typedef __attribute__((ext_vector_type(2))) __bf16 bf16x2;
typedef __attribute__((ext_vector_type(4))) short s16x4;
typedef __attribute__((ext_vector_type(16))) float f32x16;
typedef __attribute__((ext_vector_type(4))) float f32x4;
typedef __attribute__((ext_vector_type(2))) float f32x2;
typedef __attribute__((ext_vector_type(4))) unsigned u32x4;
typedef __attribute__((ext_vector_type(2))) unsigned u32x2;
typedef unsigned short u16;

#define SEQ 2048
#define DM 512

#define MFMA32(a, b, c) __builtin_amdgcn_mfma_f32_32x32x16_bf16((a), (b), (c), 0, 0, 0)

DI float bf2f(u16 v) { return __uint_as_float(((unsigned)v) << 16); }
DI float bflo(unsigned v) { return __uint_as_float(v << 16); }
DI float bfhi(unsigned v) { return __uint_as_float(v & 0xffff0000u); }
DI unsigned pack2(float a, float b) {
  f32x2 f = {a, b};
  bf16x2 r = __builtin_convertvector(f, bf16x2);
  return __builtin_bit_cast(unsigned, r);
}
DI float rbf(float a) {
  return (float)((__bf16)a);
}
DI s16x4 tr_read(const char* p) {
  typedef __attribute__((address_space(3))) s16x4* lp;
  return __builtin_amdgcn_ds_read_tr16_b64_v4i16((lp)(p));
}
DI bf16x8 cat_frag(s16x4 lo, s16x4 hi) {
  typedef __attribute__((ext_vector_type(8))) short s16x8;
  s16x8 v = __builtin_shufflevector(lo, hi, 0, 1, 2, 3, 4, 5, 6, 7);
  return __builtin_bit_cast(bf16x8, v);
}

#define STAMP(k, i) do {} while (0)
#define WS_Q 0
#define WS_K (2u << 20)
#define WS_VT (4u << 20)
#define WS_KVT (6u << 20)
#define WS_Y2T (8u << 20)
#define WS_O (10u << 20)
#define WS_G (12u << 20)

#define GLDS16(g, l) __builtin_amdgcn_global_load_lds((const unsigned*)(g), (unsigned*)(l), 16, 0, 0)
#define WAIT_V(n) asm volatile("s_waitcnt vmcnt(" #n ")" ::: "memory")
#define RAWBAR() do { asm volatile("s_waitcnt lgkmcnt(0)" ::: "memory"); __builtin_amdgcn_s_barrier(); } while (0)
#define K1_STAGE 32768

template <int N>
DI void wait_vm() {
  if constexpr (N == 0) WAIT_V(0);
  else if constexpr (N == 8) WAIT_V(8);
  else if constexpr (N == 16) WAIT_V(16);
  else if constexpr (N == 24) WAIT_V(24);
}

template <bool ISV>
DI void k1_gemm(const u16* __restrict__ x, const u16* __restrict__ w, char* __restrict__ ws, char* smem, int tm,
                int tn) {
  const int t = threadIdx.x;
  const int l = t & 63, wv = t >> 6;
  const int r = l & 31, hh = l >> 5;
  const int wm = wv & 1, wn = wv >> 1;
  const int m0 = tm * 128, n0 = tn * 128;

  const int arow0 = 32 * wv + (l >> 3);
  const int brow0 = 16 * wv + (l >> 4);
  const u16* xg[4];
  const u16* wg[4];
#pragma unroll
  for (int i = 0; i < 4; ++i) {
    const int ar = arow0 + 8 * i;
    xg[i] = x + (size_t)(m0 + ar) * DM + (((l & 7) ^ ((ar >> 1) & 7)) * 8);
    const int br = brow0 + 4 * i;
    wg[i] = w + (size_t)br * 1536 + n0 + (((l & 15) ^ ((br & 3) << 2)) * 8);
  }
  char* ldsw = smem + wv * 4096 + l * 16;

  auto issue = [&](int kt) {
    char* st = ldsw + (kt & 3) * K1_STAGE;
#pragma unroll
    for (int i = 0; i < 4; ++i) GLDS16(xg[i] + kt * 64, st + i * 1024);
#pragma unroll
    for (int i = 0; i < 4; ++i) GLDS16(wg[i] + (size_t)kt * 64 * 1536, st + 16384 + i * 1024);
  };

  f32x16 acc[2][2];
#pragma unroll
  for (int i = 0; i < 2; ++i)
#pragma unroll
    for (int j = 0; j < 2; ++j)
#pragma unroll
      for (int e = 0; e < 16; ++e) acc[i][j][e] = 0.f;

  const int blk = (l >> 4) & 1, q4 = (l & 15) >> 2, p4 = l & 3;
  const int a_row_off = (wm * 64 + r) * 128;
  const int a_sw = (r >> 1) & 7;
  const int b_base = 16384 + (8 * hh + q4) * 256 + (p4 & 1) * 8;
  const int b_ch0 = (wn * 8 + 2 * blk + (p4 >> 1));

  STAMP(0, 0);
  issue(0);
  issue(1);
  issue(2);
#pragma unroll
  for (int kt = 0; kt < 8; ++kt) {
    if (kt <= 5) wait_vm<16>();
    else if (kt == 6) wait_vm<8>();
    else wait_vm<0>();
    RAWBAR();
    STAMP(0, 1 + kt);
    if (kt + 3 <= 7) issue(kt + 3);
    const char* st = smem + (kt & 3) * K1_STAGE;
    bf16x8 af[2][2], bfr[2][2];
    auto ldfr = [&](int ks, bf16x8(&a)[2], bf16x8(&b)[2]) {
#pragma unroll
      for (int i = 0; i < 2; ++i)
        a[i] = *(const bf16x8*)(st + a_row_off + i * 32 * 128 + (((2 * ks + hh) ^ a_sw) * 16));
#pragma unroll
      for (int j = 0; j < 2; ++j) {
        const int ch = ((b_ch0 + 4 * j) ^ (q4 << 2)) * 16;
        s16x4 lo = tr_read(st + b_base + (16 * ks) * 256 + ch);
        s16x4 hi = tr_read(st + b_base + (16 * ks + 4) * 256 + ch);
        b[j] = cat_frag(lo, hi);
      }
    };
    ldfr(0, af[0], bfr[0]);
#pragma unroll
    for (int ks = 0; ks < 4; ++ks) {
      if (ks < 3) ldfr(ks + 1, af[(ks + 1) & 1], bfr[(ks + 1) & 1]);
      __builtin_amdgcn_sched_barrier(0);
#pragma unroll
      for (int i = 0; i < 2; ++i)
#pragma unroll
        for (int j = 0; j < 2; ++j) {
          if (ISV)
            acc[i][j] = MFMA32(af[ks & 1][i], bfr[ks & 1][j], acc[i][j]);
          else
            acc[i][j] = MFMA32(bfr[ks & 1][j], af[ks & 1][i], acc[i][j]);
        }
      __builtin_amdgcn_sched_barrier(0);
    }
  }

  STAMP(0, 9);
  if (ISV) {
    u16* Vt = (u16*)(ws + WS_VT);
#pragma unroll
    for (int i = 0; i < 2; ++i)
#pragma unroll
      for (int j = 0; j < 2; ++j) {
        const int n = (n0 - 1024) + wn * 64 + j * 32 + r;
        const int tok0 = m0 + wm * 64 + i * 32;
        u16* dst = Vt + (size_t)n * SEQ + tok0 + 8 * hh;
#pragma unroll
        for (int bb = 0; bb < 2; ++bb) {
          u32x4 v;
          v[0] = pack2(acc[i][j][8 * bb + 0], acc[i][j][8 * bb + 1]);
          v[1] = pack2(acc[i][j][8 * bb + 2], acc[i][j][8 * bb + 3]);
          v[2] = pack2(acc[i][j][8 * bb + 4], acc[i][j][8 * bb + 5]);
          v[3] = pack2(acc[i][j][8 * bb + 6], acc[i][j][8 * bb + 7]);
          *(u32x4*)(dst + 16 * bb) = v;
        }
      }
  } else {
    u16* base = (u16*)(ws + (n0 >= 512 ? WS_K : WS_Q));
    const int nn0 = n0 & 511;
#pragma unroll
    for (int i = 0; i < 2; ++i)
#pragma unroll
      for (int j = 0; j < 2; ++j) {
        const int tok = m0 + wm * 64 + i * 32 + r;
        const int nb = nn0 + wn * 64 + j * 32;
        const int head = nb >> 4;
#pragma unroll
        for (int g = 0; g < 4; ++g) {
          u32x2 v;
          v[0] = pack2(acc[i][j][4 * g + 0], acc[i][j][4 * g + 1]);
          v[1] = pack2(acc[i][j][4 * g + 2], acc[i][j][4 * g + 3]);
          *(u32x2*)(base + ((size_t)(head + (g >> 1)) * SEQ + tok) * 16 + 8 * (g & 1) + 4 * hh) = v;
        }
      }
  }
  STAMP(0, 10);
}

__global__ __launch_bounds__(256) void k1_kernel(const u16* __restrict__ x, const u16* __restrict__ kin,
                                                 const u16* __restrict__ vin, const u16* __restrict__ shiftk,
                                                 const u16* __restrict__ wqkv, const u16* __restrict__ gatew,
                                                 char* __restrict__ ws) {
  __shared__ __attribute__((aligned(16))) char smem[4 * K1_STAGE];
  const int xcd = blockIdx.x & 7, bidx = blockIdx.x >> 3;
  if (bidx < 24) {
    const int tm = 2 * xcd + (bidx & 1), tn = bidx >> 1;
    if (tn >= 8)
      k1_gemm<true>(x, wqkv, ws, smem, tm, tn);
    else
      k1_gemm<false>(x, wqkv, ws, smem, tm, tn);
    return;
  }
  const int l = threadIdx.x & 63, wv = threadIdx.x >> 6;
  const int t = threadIdx.x;
  const int pb = xcd * 8 + (bidx - 24);
  {
    const int ch = t & 15, rg = t >> 4;
    u32x4 kr[2][7], vr[2][4], sk[2][4];
#pragma unroll
    for (int h2 = 0; h2 < 2; ++h2) {
      const int tile = pb * 2 + h2;
      const int s0 = (tile >> 2) * 64, d0 = (tile & 3) * 128;
      const int sb = s0 + rg * 4;
      const int dd = d0 + ch * 8;
#pragma unroll
      for (int i = 0; i < 7; ++i) {
        const int s = sb - 3 + i;
        u32x4 z = {0u, 0u, 0u, 0u};
        kr[h2][i] = (s >= 0) ? *(const u32x4*)(kin + (size_t)s * DM + dd) : z;
      }
#pragma unroll
      for (int i = 0; i < 4; ++i) vr[h2][i] = *(const u32x4*)(vin + (size_t)(sb + i) * DM + dd);
#pragma unroll
      for (int i = 0; i < 4; ++i) sk[h2][i] = *(const u32x4*)(shiftk + (size_t)dd * 4 + i * 8);
    }
#pragma unroll
    for (int h2 = 0; h2 < 2; ++h2) {
#pragma unroll
      for (int c = 0; c < 8; ++c) {
        const unsigned s01 = sk[h2][c >> 1][(c & 1) * 2], s23 = sk[h2][c >> 1][(c & 1) * 2 + 1];
        const float sk0 = bflo(s01), sk1 = bfhi(s01), sk2 = bflo(s23), sk3 = bfhi(s23);
        float kk[7];
#pragma unroll
        for (int i = 0; i < 7; ++i) kk[i] = (c & 1) ? bfhi(kr[h2][i][c >> 1]) : bflo(kr[h2][i][c >> 1]);
        float res[4];
#pragma unroll
        for (int i = 0; i < 4; ++i) {
          float a = rbf(kk[i] * sk3);
          a = rbf(a + rbf(kk[i + 1] * sk2));
          a = rbf(a + rbf(kk[i + 2] * sk1));
          a = rbf(a + rbf(kk[i + 3] * sk0));
          const float vv = (c & 1) ? bfhi(vr[h2][i][c >> 1]) : bflo(vr[h2][i][c >> 1]);
          res[i] = a * vv;
        }
        u32x2 o = {pack2(res[0], res[1]), pack2(res[2], res[3])};
        *(u32x2*)(smem + h2 * 17408 + (ch * 8 + c) * 136 + rg * 8) = o;
      }
    }
    __syncthreads();
    u16* kvT = (u16*)(ws + WS_KVT);
#pragma unroll
    for (int h2 = 0; h2 < 2; ++h2) {
      const int tile = pb * 2 + h2;
      const int s0 = (tile >> 2) * 64, d0 = (tile & 3) * 128;
#pragma unroll
      for (int i = 0; i < 4; ++i) {
        const int idx = t + 256 * i;
        const int dr = idx >> 3, c8 = idx & 7;
        const u32x2 a = *(const u32x2*)(smem + h2 * 17408 + dr * 136 + c8 * 16);
        const u32x2 b = *(const u32x2*)(smem + h2 * 17408 + dr * 136 + c8 * 16 + 8);
        u32x4 v = {a[0], a[1], b[0], b[1]};
        *(u32x4*)(kvT + (size_t)(d0 + dr) * SEQ + s0 + c8 * 8) = v;
      }
    }
  }
  {
    const int wid = pb * 4 + wv;
    float* g = (float*)(ws + WS_G);
    const u32x4 gw = *(const u32x4*)(gatew + l * 8);
    u32x4 xv[8];
#pragma unroll
    for (int it = 0; it < 8; ++it) xv[it] = *(const u32x4*)(x + (size_t)(wid * 8 + it) * DM + l * 8);
    float sums[8];
#pragma unroll
    for (int it = 0; it < 8; ++it) {
      float sum = 0.f;
#pragma unroll
      for (int i = 0; i < 4; ++i) {
        sum += rbf(bflo(xv[it][i]) * bflo(gw[i]));
        sum += rbf(bfhi(xv[it][i]) * bfhi(gw[i]));
      }
      sums[it] = sum;
    }
#pragma unroll
    for (int o = 32; o >= 1; o >>= 1)
#pragma unroll
      for (int it = 0; it < 8; ++it) sums[it] += __shfl_xor(sums[it], o);
    if (l < 8) {
      float sv = sums[0];
#pragma unroll
      for (int it = 1; it < 8; ++it) sv = (l == it) ? sums[it] : sv;
      const float z = rbf(sv);
      g[wid * 8 + l] = rbf(1.f / (1.f + __expf(-z)));
    }
  }
}

#define CV_KVSTR 80
#define CV_HSTR 4160
#define CV_HBYTES (8 * CV_HSTR)
#define CV_REDSTR 36
#define CV_SMEM 40960

template <bool FIRST>
DI void attn_block(const char* __restrict__ ws, char* smem, int head, int qb) {
  const int l = threadIdx.x & 63, wv = threadIdx.x >> 6;
  const int r = l & 31, hh = l >> 5;
  const u16* Qh = (const u16*)(ws + WS_Q) + (size_t)head * SEQ * 16;
  const u16* Kh = (const u16*)(ws + WS_K) + (size_t)head * SEQ * 16;
  const u16* Vh = (const u16*)(ws + WS_VT) + (size_t)head * 16 * SEQ;
  u16* O = (u16*)(ws + WS_O);
  const int q0 = qb + 32 * wv;
  const int kt0 = FIRST ? (8 - wv) : 0;
  STAMP(1, 0);
  char* ks_ = smem;
  char* vs_ = smem + 16384;
  char* cs_ = smem + 32768;
  {
    const int key = l >> 1;
    const u16* ksrc = Kh + (size_t)key * 16 + (((l & 1) ^ ((key >> 3) & 1)) * 8);
    const int dd = l >> 2;
    const u16* vsrc = Vh + (size_t)dd * SEQ + (((l & 3) ^ ((dd >> 2) & 3)) * 8);
#pragma unroll
    for (int i = 0; i < 2; ++i) {
      const int u = wv + 8 * i;
      int key0 = qb - 256 + 32 * u;
      key0 = key0 < 0 ? 0 : key0;
      GLDS16(ksrc + (size_t)key0 * 16, ks_ + u * 1024 + l * 16);
      GLDS16(vsrc + key0, vs_ + u * 1024 + l * 16);
    }
  }
  const bf16x8 qf = *(const bf16x8*)(Qh + (size_t)(q0 + r) * 16 + 8 * hh);
  if (threadIdx.x < 2) {
    const unsigned o = threadIdx.x == 0 ? 0x3f803f80u : 0u;
    *(u32x4*)(cs_ + threadIdx.x * 16) = (u32x4){o, o, o, o};
  }
  WAIT_V(0);
  RAWBAR();
  STAMP(1, 1);
  const char* krd = ks_ + (32 * wv + r) * 32 + ((hh ^ ((r >> 3) & 1)) * 16);
  f32x16 zero;
#pragma unroll
  for (int e = 0; e < 16; ++e) zero[e] = 0.f;
  const float NEG = -3.0e38f;
  auto mask = [&](int kt, f32x16& s) {
    if (kt == 0) {
#pragma unroll
      for (int e = 0; e < 16; ++e) s[e] = ((e & 3) + 8 * (e >> 2) + 4 * hh > r) ? s[e] : NEG;
    }
    if (kt == 8) {
#pragma unroll
      for (int e = 0; e < 16; ++e) s[e] = ((e & 3) + 8 * (e >> 2) + 4 * hh <= r) ? s[e] : NEG;
    }
  };
  float mx = NEG;
  {
    bf16x8 kf[9];
#pragma unroll
    for (int kt = 0; kt < 9; ++kt) kf[kt] = *(const bf16x8*)(krd + kt * 1024);
    f32x16 s[2];
    s[0] = MFMA32(kf[0], qf, zero);
#pragma unroll
    for (int kt = 0; kt < 9; ++kt) {
      if (kt < 8) s[(kt + 1) & 1] = MFMA32(kf[kt + 1], qf, zero);
      f32x16& c = s[kt & 1];
      mask(kt, c);
      float m = NEG;
#pragma unroll
      for (int e = 0; e < 16; e += 2) m = fmaxf(fmaxf(c[e], c[e + 1]), m);
      if (FIRST) m = (kt >= kt0) ? m : NEG;
      mx = fmaxf(mx, m);
    }
  }
  mx = fmaxf(mx, __shfl_xor(mx, 32));
  const float c1 = 0.25f * 1.44269504088896340736f;
  const float mc = mx * c1;
  const f32x2 c1v = {c1, c1}, nmcv = {-mc, -mc};
  STAMP(1, 2);
  const int vsw = (r >> 2) & 3;
  const bool isd = r < 16;
  const int cad = 32768 + ((r == 16 || r == 20) ? 0 : 16);
  int va0 = isd ? 16384 + wv * 1024 + r * 64 + ((hh ^ vsw) * 16) : cad;
  int va1 = isd ? 16384 + wv * 1024 + r * 64 + (((2 + hh) ^ vsw) * 16) : cad;
  const int vstep = isd ? 1024 : 0;
  f32x16 oacc = zero;
  {
    f32x16 s[2];
    bf16x8 kf = *(const bf16x8*)(krd);
    s[0] = MFMA32(kf, qf, zero);
#pragma unroll
    for (int kt = 0; kt < 9; ++kt) {
      const bf16x8 vf0 = *(const bf16x8*)(smem + va0);
      const bf16x8 vf1 = *(const bf16x8*)(smem + va1);
      va0 += vstep;
      va1 += vstep;
      if (kt < 8) {
        kf = *(const bf16x8*)(krd + (kt + 1) * 1024);
        s[(kt + 1) & 1] = MFMA32(kf, qf, zero);
      }
      f32x16& c = s[kt & 1];
      mask(kt, c);
      if (!FIRST || kt >= kt0) {
        u32x4 pp[2];
#pragma unroll
        for (int e = 0; e < 16; e += 2) {
          f32x2 a = __builtin_elementwise_fma((f32x2){c[e], c[e + 1]}, c1v, nmcv);
          pp[e >> 3][(e >> 1) & 3] = pack2(__builtin_amdgcn_exp2f(a[0]), __builtin_amdgcn_exp2f(a[1]));
        }
        oacc = MFMA32(vf0, __builtin_bit_cast(bf16x8, pp[0]), oacc);
        oacc = MFMA32(vf1, __builtin_bit_cast(bf16x8, pp[1]), oacc);
      }
    }
  }
  STAMP(1, 3);
  const float inv = 1.f / oacc[8];
  u32x2 v0 = {pack2(oacc[0] * inv, oacc[1] * inv), pack2(oacc[2] * inv, oacc[3] * inv)};
  u32x2 v1 = {pack2(oacc[4] * inv, oacc[5] * inv), pack2(oacc[6] * inv, oacc[7] * inv)};
  u16* op = O + (size_t)(q0 + r) * DM + head * 16 + 4 * hh;
  *(u32x2*)(op) = v0;
  *(u32x2*)(op + 8) = v1;
  STAMP(1, 4);
}

DI void conv_block(const u16* __restrict__ ssmk, const u16* __restrict__ ssmD, char* __restrict__ ws, char* smem,
                   int d) {
  const int t = threadIdx.x & 255;
  const int l = t & 63, wv = t >> 6;
  const int r = l & 31, hh = l >> 5;
  char* hs = smem;
  char* kvs = smem + CV_HBYTES;
  const u16* kvT = (const u16*)(ws + WS_KVT) + (size_t)d * SEQ;
  const u16* hrow = ssmk + (size_t)d * SEQ;
  STAMP(1, 0);
  const u32x4 kvv = *(const u32x4*)(kvT + 8 * t);
  const u32x4 whi = *(const u32x4*)(hrow + 8 * t);
  const u32x4 wlo0 = *(const u32x4*)(hrow + 8 * (t > 0 ? t - 1 : 0));
  const float Dd = bf2f(ssmD[d]);
  *(u32x4*)(kvs + ((t >> 2) + 32) * CV_KVSTR + (t & 3) * 16) = kvv;
  if (t < 128) {
    u32x4 z = {0u, 0u, 0u, 0u};
    *(u32x4*)(kvs + (t >> 2) * CV_KVSTR + (t & 3) * 16) = z;
  }
  {
    const int E = t;
    u32x4 wlo = wlo0;
    if (E == 0) wlo = (u32x4){0u, 0u, 0u, 0u};
    unsigned z[8];
#pragma unroll
    for (int i = 0; i < 4; ++i) {
      const unsigned a = whi[3 - i];
      z[i] = (a >> 16) | (a << 16);
      const unsigned b = wlo[3 - i];
      z[4 + i] = (b >> 16) | (b << 16);
    }
    const int M = 255 - E;
#pragma unroll
    for (int p = 0; p < 8; ++p) {
      const int o = 7 - p;
      u32x4 v;
      if ((o & 1) == 0) {
        v[0] = z[o / 2];
        v[1] = z[o / 2 + 1];
        v[2] = z[o / 2 + 2];
        v[3] = z[o / 2 + 3];
      } else {
        v[0] = (z[o / 2] >> 16) | (z[o / 2 + 1] << 16);
        v[1] = (z[o / 2 + 1] >> 16) | (z[o / 2 + 2] << 16);
        v[2] = (z[o / 2 + 2] >> 16) | (z[o / 2 + 3] << 16);
        v[3] = (z[o / 2 + 3] >> 16) | (z[o / 2 + 4] << 16);
      }
      *(u32x4*)(hs + p * CV_HSTR + 16 * M) = v;
    }
    if (t < 24) {
      u32x4 zz = {0u, 0u, 0u, 0u};
      *(u32x4*)(hs + (t & 7) * CV_HSTR + 16 * (256 + (t >> 3))) = zz;
    }
  }
  __syncthreads();
  STAMP(1, 1);
  f32x16 acc0, acc1, acc2;
#pragma unroll
  for (int e = 0; e < 16; ++e) {
    acc0[e] = 0.f;
    acc1[e] = 0.f;
    acc2[e] = 0.f;
  }
  const char* abase = hs + (r & 7) * CV_HSTR + 16 * (255 - (r >> 3) + hh) - 64 * wv;
  const char* bbase = kvs + (r + 32 - wv) * CV_KVSTR + 16 * hh;
  {
    bf16x8 fa[2][2], fb[2][2];
    auto ldc = [&](int step, bf16x8(&a)[2], bf16x8(&b)[2]) {
      const int i = step >> 1, ks = step & 1;
      a[0] = *(const bf16x8*)(abase - 256 * i + 32 * ks);
      a[1] = *(const bf16x8*)(abase - 256 * i - 2048 + 32 * ks);
      b[1] = *(const bf16x8*)(bbase + (32 - 4 * i) * CV_KVSTR + 32 * ks);
      b[0] = *(const bf16x8*)(bbase - (4 * i) * CV_KVSTR + 32 * ks);
    };
    ldc(0, fa[0], fb[0]);
#pragma unroll
    for (int step = 0; step < 16; ++step) {
      if (step < 15) ldc(step + 1, fa[(step + 1) & 1], fb[(step + 1) & 1]);
      __builtin_amdgcn_sched_barrier(0);
      acc1 = MFMA32(fa[step & 1][0], fb[step & 1][1], acc1);
      acc0 = MFMA32(fa[step & 1][0], fb[step & 1][0], acc0);
      acc2 = MFMA32(fa[step & 1][1], fb[step & 1][0], acc2);
      __builtin_amdgcn_sched_barrier(0);
    }
  }
  acc1 += acc2;
  STAMP(1, 2);
  __syncthreads();
  float* red = (float*)smem;
  {
    float* rw = red + wv * (64 * CV_REDSTR);
#pragma unroll
    for (int g = 0; g < 4; ++g) {
      f32x4 v0 = {acc0[4 * g], acc0[4 * g + 1], acc0[4 * g + 2], acc0[4 * g + 3]};
      f32x4 v1 = {acc1[4 * g], acc1[4 * g + 1], acc1[4 * g + 2], acc1[4 * g + 3]};
      *(f32x4*)(rw + r * CV_REDSTR + 8 * g + 4 * hh) = v0;
      *(f32x4*)(rw + (32 + r) * CV_REDSTR + 8 * g + 4 * hh) = v1;
    }
  }
  __syncthreads();
  {
    const int a = t >> 2, b0 = 8 * (t & 3);
    f32x4 s0 = {0.f, 0.f, 0.f, 0.f}, s1 = {0.f, 0.f, 0.f, 0.f};
#pragma unroll
    for (int w = 0; w < 4; ++w) {
      const float* rp = red + w * (64 * CV_REDSTR) + a * CV_REDSTR + b0;
      s0 += *(const f32x4*)(rp);
      s1 += *(const f32x4*)(rp + 4);
    }
    float yy[8] = {s0[0], s0[1], s0[2], s0[3], s1[0], s1[1], s1[2], s1[3]};
    u32x4 o;
#pragma unroll
    for (int i = 0; i < 4; ++i) {
      const float y0 = rbf(yy[2 * i]) + rbf(bflo(kvv[i]) * Dd);
      const float y1 = rbf(yy[2 * i + 1]) + rbf(bfhi(kvv[i]) * Dd);
      o[i] = pack2(y0, y1);
    }
    u16* y2T = (u16*)(ws + WS_Y2T) + (size_t)d * SEQ;
    *(u32x4*)(y2T + 8 * t) = o;
  }
  STAMP(1, 4);
}

__global__ __launch_bounds__(512, 4) void k2_kernel(const u16* __restrict__ ssmk, const u16* __restrict__ ssmD,
                                                    char* __restrict__ ws) {
  __shared__ __attribute__((aligned(16))) char smem[2 * CV_SMEM];
  const int xcd = blockIdx.x & 7, bidx = blockIdx.x >> 3;
  if (bidx >= 32) {
    const int half = threadIdx.x >> 8;
    conv_block(ssmk, ssmD, ws, smem + half * CV_SMEM, xcd * 64 + (bidx - 32) * 2 + half);
  } else {
    if (xcd == 0)
      attn_block<true>(ws, smem, bidx, 0);
    else
      attn_block<false>(ws, smem, bidx, xcd * 256);
  }
}

#define K3_STAGE 32768
#define K3_CSTR 68
#define K3_YSTR 136

__global__ __launch_bounds__(256) void k3_kernel(const u16* __restrict__ x, const u16* __restrict__ wo,
                                                 const char* __restrict__ ws, u16* __restrict__ out) {
  __shared__ __attribute__((aligned(16))) char smem[4 * K3_STAGE];
  const int t = threadIdx.x;
  const int l = t & 63, wv = t >> 6;
  const int r = l & 31, hh = l >> 5;
  const int wm = wv & 1, wn = wv >> 1;
  const int tm = (blockIdx.x & 7) * 4 + ((blockIdx.x >> 3) & 3), tn = blockIdx.x >> 5;
  const int m0 = tm * 64, n0 = tn * 64;
  const u16* o = (const u16*)(ws + WS_O);
  STAMP(2, 0);

  const u16* y2T = (const u16*)(ws + WS_Y2T);
  const float* gp = (const float*)(ws + WS_G);
  u32x4 yv[2], xq[2];
  float gv[2];
#pragma unroll
  for (int i = 0; i < 2; ++i) {
    const int idx = t + 256 * i;
    const int rr = idx >> 3, ch = idx & 7;
    yv[i] = *(const u32x4*)(y2T + (size_t)(n0 + rr) * SEQ + m0 + ch * 8);
    xq[i] = *(const u32x4*)(x + (size_t)(m0 + rr) * DM + n0 + ch * 8);
    gv[i] = gp[m0 + rr];
  }
  {
    char* ldsw = smem + wv * 4096 + l * 16;
#pragma unroll
    for (int st = 0; st < 4; ++st) {
#pragma unroll
      for (int i = 0; i < 4; ++i) {
        const int ar = 16 * wv + 4 * i + (l >> 4);
        GLDS16(o + (size_t)(m0 + ar) * DM + st * 128 + (((l & 15) ^ (ar & 15)) * 8), ldsw + st * K3_STAGE + i * 1024);
      }
#pragma unroll
      for (int i = 0; i < 4; ++i) {
        const int br = 32 * wv + 8 * i + (l >> 3);
        GLDS16(wo + (size_t)(st * 128 + br) * DM + n0 + (((l & 7) ^ (((br >> 1) & 1) << 2)) * 8),
               ldsw + st * K3_STAGE + 16384 + i * 1024);
      }
    }
  }
  f32x16 acc;
#pragma unroll
  for (int e = 0; e < 16; ++e) acc[e] = 0.f;
  const int blk = (l >> 4) & 1, q4 = (l & 15) >> 2, p4 = l & 3;
  const int arow = wm * 32 + r;
  const int a_off = arow * 256;
  const int a_sw = arow & 15;
  const int b_off = 16384 + (8 * hh + q4) * 128 + (((wn * 4 + 2 * blk + (p4 >> 1)) ^ (((q4 >> 1) & 1) << 2)) * 16) +
                    (p4 & 1) * 8;
#pragma unroll
  for (int st = 0; st < 4; ++st) {
    if (st == 0) wait_vm<24>();
    else if (st == 1) wait_vm<16>();
    else if (st == 2) wait_vm<8>();
    else wait_vm<0>();
    RAWBAR();
    STAMP(2, 1 + st);
    const char* sb = smem + st * K3_STAGE;
    bf16x8 af[8], bfr[8];
#pragma unroll
    for (int ks = 0; ks < 8; ++ks) {
      af[ks] = *(const bf16x8*)(sb + a_off + (((2 * ks + hh) ^ a_sw) * 16));
      s16x4 lo = tr_read(sb + b_off + (16 * ks) * 128);
      s16x4 hi = tr_read(sb + b_off + (16 * ks + 4) * 128);
      bfr[ks] = cat_frag(lo, hi);
    }
    __builtin_amdgcn_sched_barrier(0);
#pragma unroll
    for (int ks = 0; ks < 8; ++ks) acc = MFMA32(af[ks], bfr[ks], acc);
    __builtin_amdgcn_sched_barrier(0);
  }
  __syncthreads();
  STAMP(2, 5);
  float* Cs = (float*)smem;
  char* Ys = smem + 64 * K3_CSTR * 4;
#pragma unroll
  for (int e = 0; e < 16; ++e) {
    const int row = wm * 32 + (e & 3) + 8 * (e >> 2) + 4 * hh;
    Cs[row * K3_CSTR + wn * 32 + r] = acc[e];
  }
#pragma unroll
  for (int i = 0; i < 2; ++i) {
    const int idx = t + 256 * i;
    const int dr = idx >> 3, ch = idx & 7;
    u32x2 a = {yv[i][0], yv[i][1]}, b = {yv[i][2], yv[i][3]};
    *(u32x2*)(Ys + dr * K3_YSTR + ch * 16) = a;
    *(u32x2*)(Ys + dr * K3_YSTR + ch * 16 + 8) = b;
  }
  __syncthreads();
#pragma unroll
  for (int i = 0; i < 2; ++i) {
    const int idx = t + 256 * i;
    const int row = idx >> 3, ch = idx & 7;
    const int s = m0 + row;
    const float g = gv[i];
    const float omg = rbf(1.f - g);
    const f32x4 c0 = *(const f32x4*)(Cs + row * K3_CSTR + ch * 8);
    const f32x4 c1 = *(const f32x4*)(Cs + row * K3_CSTR + ch * 8 + 4);
    float bb[8] = {c0[0], c0[1], c0[2], c0[3], c1[0], c1[1], c1[2], c1[3]};
    float res[8];
#pragma unroll
    for (int j = 0; j < 8; ++j) {
      const float y2 = bf2f(*(const u16*)(Ys + (ch * 8 + j) * K3_YSTR + row * 2));
      const float qv = (j & 1) ? bfhi(xq[i][j >> 1]) : bflo(xq[i][j >> 1]);
      const float a = rbf(qv * y2);
      const float ga = rbf(g * a);
      const float gb = rbf(omg * rbf(bb[j]));
      res[j] = ga + gb;
    }
    u32x4 ov;
    ov[0] = pack2(res[0], res[1]);
    ov[1] = pack2(res[2], res[3]);
    ov[2] = pack2(res[4], res[5]);
    ov[3] = pack2(res[6], res[7]);
    *(u32x4*)(out + (size_t)s * DM + n0 + ch * 8) = ov;
  }
  STAMP(2, 6);
}

extern "C" void kernel_launch(void* const* d_in, const int* in_sizes, int n_in, void* d_out, int out_size,
                              void* d_ws, size_t ws_size, hipStream_t stream) {
  const u16* A_q = (const u16*)d_in[0];
  const u16* A_k = (const u16*)d_in[1];
  const u16* A_v = (const u16*)d_in[2];
  const u16* A_shift = (const u16*)d_in[3];
  const u16* A_ssmk = (const u16*)d_in[4];
  const u16* A_ssmD = (const u16*)d_in[5];
  const u16* B_wqkv = (const u16*)d_in[7];
  const u16* B_wo = (const u16*)d_in[8];
  const u16* gate_w = (const u16*)d_in[9];
  char* ws = (char*)d_ws;
  k1_kernel<<<256, 256, 0, stream>>>(A_q, A_k, A_v, A_shift, B_wqkv, gate_w, ws);
  k2_kernel<<<512, 512, 0, stream>>>(A_ssmk, A_ssmD, ws);
  k3_kernel<<<256, 256, 0, stream>>>(A_q, B_wo, ws, (u16*)d_out);
}
```

```cpp
#include <hip/hip_runtime.h>
#include <hip/hip_bf16.h>
#include <stdint.h>

#define DI __device__ __forceinline__
typedef __attribute__((ext_vector_type(8))) __bf16 bf16x8;
typedef __attribute__((ext_vector_type(4))) __bf16 bf16x4;
typedef __attribute__((ext_vector_type(2))) __bf16 bf16x2;
typedef __attribute__((ext_vector_type(4))) short s16x4;
typedef __attribute__((ext_vector_type(16))) float f32x16;
typedef __attribute__((ext_vector_type(4))) float f32x4;
typedef __attribute__((ext_vector_type(2))) float f32x2;
typedef __attribute__((ext_vector_type(4))) unsigned u32x4;
typedef __attribute__((ext_vector_type(2))) unsigned u32x2;
typedef unsigned short u16;

#define SEQ 2048
#define DM 512

#define MFMA32(a, b, c) __builtin_amdgcn_mfma_f32_32x32x16_bf16((a), (b), (c), 0, 0, 0)

DI float bf2f(u16 v) { return __uint_as_float(((unsigned)v) << 16); }
DI float bflo(unsigned v) { return __uint_as_float(v << 16); }
DI float bfhi(unsigned v) { return __uint_as_float(v & 0xffff0000u); }
DI unsigned pack2(float a, float b) {
  f32x2 f = {a, b};
  bf16x2 r = __builtin_convertvector(f, bf16x2);
  return __builtin_bit_cast(unsigned, r);
}
DI float rbf(float a) {
  return (float)((__bf16)a);
}
DI s16x4 tr_read(const char* p) {
  typedef __attribute__((address_space(3))) s16x4* lp;
  return __builtin_amdgcn_ds_read_tr16_b64_v4i16((lp)(p));
}
DI bf16x8 cat_frag(s16x4 lo, s16x4 hi) {
  typedef __attribute__((ext_vector_type(8))) short s16x8;
  s16x8 v = __builtin_shufflevector(lo, hi, 0, 1, 2, 3, 4, 5, 6, 7);
  return __builtin_bit_cast(bf16x8, v);
}

#define STAMP(k, i) do {} while (0)
#define WS_Q 0
#define WS_K (2u << 20)
#define WS_VT (4u << 20)
#define WS_KVT (6u << 20)
#define WS_Y2T (8u << 20)
#define WS_O (10u << 20)
#define WS_G (12u << 20)

#define GLDS16(g, l) __builtin_amdgcn_global_load_lds((const unsigned*)(g), (unsigned*)(l), 16, 0, 0)
#define WAIT_V(n) asm volatile("s_waitcnt vmcnt(" #n ")" ::: "memory")
#define RAWBAR() do { asm volatile("s_waitcnt lgkmcnt(0)" ::: "memory"); __builtin_amdgcn_s_barrier(); } while (0)
#define K1_STAGE 32768

template <int N>
DI void wait_vm() {
  if constexpr (N == 0) WAIT_V(0);
  else if constexpr (N == 4) WAIT_V(4);
  else if constexpr (N == 8) WAIT_V(8);
  else if constexpr (N == 16) WAIT_V(16);
  else if constexpr (N == 24) WAIT_V(24);
}

#define QSCALE (0.25f * 1.44269504088896340736f)

template <int MODE>
DI void k1_gemm(const u16* __restrict__ x, const u16* __restrict__ w, char* __restrict__ ws, char* smem, int tm,
                int tn) {
  const int t = threadIdx.x;
  const int l = t & 63, wv = t >> 6;
  const int r = l & 31, hh = l >> 5;
  const int wm = wv & 1, wn = wv >> 1;
  const int m0 = tm * 128, n0 = tn * 128;

  const u16* xg[2];
  const u16* wg[2];
#pragma unroll
  for (int i = 0; i < 2; ++i) {
    const int ar = 16 * wv + 8 * i + (l >> 3);
    xg[i] = x + (size_t)(m0 + ar) * DM + (((l & 7) ^ ((ar >> 1) & 7)) * 8);
    const int br = 8 * wv + 4 * i + (l >> 4);
    wg[i] = w + (size_t)br * 1536 + n0 + (((l & 15) ^ ((br & 3) << 2)) * 8);
  }
  char* ldsw = smem + wv * 2048 + l * 16;

  auto issue = [&](int kt) {
    char* st = ldsw + (kt & 3) * K1_STAGE;
#pragma unroll
    for (int i = 0; i < 2; ++i) GLDS16(xg[i] + kt * 64, st + i * 1024);
#pragma unroll
    for (int i = 0; i < 2; ++i) GLDS16(wg[i] + (size_t)kt * 64 * 1536, st + 16384 + i * 1024);
  };

  f32x16 acc[2];
#pragma unroll
  for (int i = 0; i < 2; ++i)
#pragma unroll
    for (int e = 0; e < 16; ++e) acc[i][e] = 0.f;

  const int blk = (l >> 4) & 1, q4 = (l & 15) >> 2, p4 = l & 3;
  const int a_row_off = (wm * 64 + r) * 128;
  const int a_sw = (r >> 1) & 7;
  const int b_base = 16384 + (8 * hh + q4) * 256 + (p4 & 1) * 8 + (((wn * 4 + 2 * blk + (p4 >> 1)) ^ (q4 << 2)) * 16);

  STAMP(0, 0);
  issue(0);
  issue(1);
  issue(2);
#pragma unroll
  for (int kt = 0; kt < 8; ++kt) {
    if (kt <= 5) wait_vm<8>();
    else if (kt == 6) wait_vm<4>();
    else wait_vm<0>();
    RAWBAR();
    STAMP(0, 1 + kt);
    const char* st = smem + (kt & 3) * K1_STAGE;
    bf16x8 af[2][2], bfr[2];
    auto ldfr = [&](int ks, bf16x8(&a)[2], bf16x8& b) {
#pragma unroll
      for (int i = 0; i < 2; ++i)
        a[i] = *(const bf16x8*)(st + a_row_off + i * 32 * 128 + (((2 * ks + hh) ^ a_sw) * 16));
      s16x4 lo = tr_read(st + b_base + (16 * ks) * 256);
      s16x4 hi = tr_read(st + b_base + (16 * ks + 4) * 256);
      b = cat_frag(lo, hi);
    };
    ldfr(0, af[0], bfr[0]);
#pragma unroll
    for (int ks = 0; ks < 4; ++ks) {
      if (ks < 3) ldfr(ks + 1, af[(ks + 1) & 1], bfr[(ks + 1) & 1]);
      __builtin_amdgcn_sched_barrier(0);
#pragma unroll
      for (int i = 0; i < 2; ++i) {
        if (MODE == 2)
          acc[i] = MFMA32(af[ks & 1][i], bfr[ks & 1], acc[i]);
        else
          acc[i] = MFMA32(bfr[ks & 1], af[ks & 1][i], acc[i]);
      }
      __builtin_amdgcn_sched_barrier(0);
      if (kt + 3 <= 7) {
        char* st3 = ldsw + ((kt + 3) & 3) * K1_STAGE;
        if (ks < 2)
          GLDS16(xg[ks] + (kt + 3) * 64, st3 + ks * 1024);
        else
          GLDS16(wg[ks - 2] + (size_t)(kt + 3) * 64 * 1536, st3 + 16384 + (ks - 2) * 1024);
      }
      __builtin_amdgcn_sched_barrier(0);
    }
  }

  STAMP(0, 9);
  if (MODE == 2) {
    u16* Vt = (u16*)(ws + WS_VT);
#pragma unroll
    for (int i = 0; i < 2; ++i) {
      const int n = (n0 - 1024) + wn * 32 + r;
      const int tok0 = m0 + wm * 64 + i * 32;
      u16* dst = Vt + (size_t)n * SEQ + tok0 + 8 * hh;
#pragma unroll
      for (int bb = 0; bb < 2; ++bb) {
        u32x4 v;
        v[0] = pack2(acc[i][8 * bb + 0], acc[i][8 * bb + 1]);
        v[1] = pack2(acc[i][8 * bb + 2], acc[i][8 * bb + 3]);
        v[2] = pack2(acc[i][8 * bb + 4], acc[i][8 * bb + 5]);
        v[3] = pack2(acc[i][8 * bb + 6], acc[i][8 * bb + 7]);
        *(u32x4*)(dst + 16 * bb) = v;
      }
    }
  } else {
    u16* base = (u16*)(ws + (MODE == 1 ? WS_K : WS_Q));
    const int nn0 = n0 & 511;
#pragma unroll
    for (int i = 0; i < 2; ++i) {
      const int tok = m0 + wm * 64 + i * 32 + r;
      const int nb = nn0 + wn * 32;
      const int head = nb >> 4;
#pragma unroll
      for (int g = 0; g < 4; ++g) {
        u32x2 v;
        v[0] = pack2(acc[i][4 * g + 0], acc[i][4 * g + 1]);
        v[1] = pack2(acc[i][4 * g + 2], acc[i][4 * g + 3]);
        *(u32x2*)(base + ((size_t)(head + (g >> 1)) * SEQ + tok) * 16 + 8 * (g & 1) + 4 * hh) = v;
      }
    }
  }
  STAMP(0, 10);
}

__global__ __launch_bounds__(512) void k1_kernel(const u16* __restrict__ x, const u16* __restrict__ kin,
                                                 const u16* __restrict__ vin, const u16* __restrict__ shiftk,
                                                 const u16* __restrict__ wqkv, const u16* __restrict__ gatew,
                                                 char* __restrict__ ws) {
  __shared__ __attribute__((aligned(16))) char smem[4 * K1_STAGE];
  const int xcd = blockIdx.x & 7, bidx = blockIdx.x >> 3;
  if (bidx < 24) {
    const int tm = 2 * xcd + (bidx & 1), tn = bidx >> 1;
    if (tn >= 8)
      k1_gemm<2>(x, wqkv, ws, smem, tm, tn);
    else if (tn >= 4)
      k1_gemm<1>(x, wqkv, ws, smem, tm, tn);
    else
      k1_gemm<0>(x, wqkv, ws, smem, tm, tn);
    return;
  }
  const int l = threadIdx.x & 63, wv = threadIdx.x >> 6;
  const int t = threadIdx.x & 255, h2 = threadIdx.x >> 8;
  const int pb = xcd * 8 + (bidx - 24);
  STAMP(0, 0);
  const int wid = pb * 8 + wv;
  const u32x4 gw = *(const u32x4*)(gatew + l * 8);
  u32x4 xv[4];
#pragma unroll
  for (int it = 0; it < 4; ++it) xv[it] = *(const u32x4*)(x + (size_t)(wid * 4 + it) * DM + l * 8);
  {
    const int ch = t & 15, rg = t >> 4;
    u32x4 kr[7], vr[4], sk[4];
    const int tile = pb * 2 + h2;
    const int s0 = (tile >> 2) * 64, d0 = (tile & 3) * 128;
    {
      const int sb = s0 + rg * 4;
      const int dd = d0 + ch * 8;
#pragma unroll
      for (int i = 0; i < 7; ++i) {
        const int s = sb - 3 + i;
        u32x4 z = {0u, 0u, 0u, 0u};
        kr[i] = (s >= 0) ? *(const u32x4*)(kin + (size_t)s * DM + dd) : z;
      }
#pragma unroll
      for (int i = 0; i < 4; ++i) vr[i] = *(const u32x4*)(vin + (size_t)(sb + i) * DM + dd);
#pragma unroll
      for (int i = 0; i < 4; ++i) sk[i] = *(const u32x4*)(shiftk + (size_t)dd * 4 + i * 8);
    }
    char* sm = smem + h2 * 17408;
#pragma unroll
    for (int c = 0; c < 8; ++c) {
      const unsigned s01 = sk[c >> 1][(c & 1) * 2], s23 = sk[c >> 1][(c & 1) * 2 + 1];
      const float sk0 = bflo(s01), sk1 = bfhi(s01), sk2 = bflo(s23), sk3 = bfhi(s23);
      float kk[7];
#pragma unroll
      for (int i = 0; i < 7; ++i) kk[i] = (c & 1) ? bfhi(kr[i][c >> 1]) : bflo(kr[i][c >> 1]);
      unsigned o[2];
#pragma unroll
      for (int i = 0; i < 4; i += 2) {
        const unsigned p3 = pack2(kk[i] * sk3, kk[i + 1] * sk3);
        const unsigned p2 = pack2(kk[i + 1] * sk2, kk[i + 2] * sk2);
        const unsigned p1 = pack2(kk[i + 2] * sk1, kk[i + 3] * sk1);
        const unsigned p0 = pack2(kk[i + 3] * sk0, kk[i + 4] * sk0);
        unsigned a = pack2(bflo(p3) + bflo(p2), bfhi(p3) + bfhi(p2));
        a = pack2(bflo(a) + bflo(p1), bfhi(a) + bfhi(p1));
        a = pack2(bflo(a) + bflo(p0), bfhi(a) + bfhi(p0));
        const float v0 = (c & 1) ? bfhi(vr[i][c >> 1]) : bflo(vr[i][c >> 1]);
        const float v1 = (c & 1) ? bfhi(vr[i + 1][c >> 1]) : bflo(vr[i + 1][c >> 1]);
        o[i >> 1] = pack2(bflo(a) * v0, bfhi(a) * v1);
      }
      *(u32x2*)(sm + (ch * 8 + c) * 136 + rg * 8) = (u32x2){o[0], o[1]};
    }
    __syncthreads();
    u16* kvT = (u16*)(ws + WS_KVT);
#pragma unroll
    for (int i = 0; i < 4; ++i) {
      const int idx = t + 256 * i;
      const int dr = idx >> 3, c8 = idx & 7;
      const u32x2 a = *(const u32x2*)(sm + dr * 136 + c8 * 16);
      const u32x2 b = *(const u32x2*)(sm + dr * 136 + c8 * 16 + 8);
      u32x4 v = {a[0], a[1], b[0], b[1]};
      *(u32x4*)(kvT + (size_t)(d0 + dr) * SEQ + s0 + c8 * 8) = v;
    }
  }
  STAMP(0, 11);
  {
    float* g = (float*)(ws + WS_G);
    float sums[4];
#pragma unroll
    for (int it = 0; it < 4; ++it) {
      float sum = 0.f;
#pragma unroll
      for (int i = 0; i < 4; ++i) {
        const unsigned pr = pack2(bflo(xv[it][i]) * bflo(gw[i]), bfhi(xv[it][i]) * bfhi(gw[i]));
        sum += bflo(pr);
        sum += bfhi(pr);
      }
      sums[it] = sum;
    }
#pragma unroll
    for (int o = 32; o >= 1; o >>= 1)
#pragma unroll
      for (int it = 0; it < 4; ++it) sums[it] += __shfl_xor(sums[it], o);
    if (l < 4) {
      float sv = sums[0];
#pragma unroll
      for (int it = 1; it < 4; ++it) sv = (l == it) ? sums[it] : sv;
      const float z = rbf(sv);
      g[wid * 4 + l] = rbf(1.f / (1.f + __expf(-z)));
    }
  }
  STAMP(0, 12);
}

#define CV_KVSTR 80
#define CV_HSTR 4160
#define CV_HBYTES (8 * CV_HSTR)
#define CV_REDSTR 36
#define CV_SMEM 40960

template <bool FIRST>
DI void attn_block(const char* __restrict__ ws, char* smem, int head, int qb) {
  const int l = threadIdx.x & 63, wv = threadIdx.x >> 6;
  const int r = l & 31, hh = l >> 5;
  const u16* Qh = (const u16*)(ws + WS_Q) + (size_t)head * SEQ * 16;
  const u16* Kh = (const u16*)(ws + WS_K) + (size_t)head * SEQ * 16;
  const u16* Vh = (const u16*)(ws + WS_VT) + (size_t)head * 16 * SEQ;
  u16* O = (u16*)(ws + WS_O);
  const int q0 = qb + 32 * wv;
  const int kt0 = FIRST ? (8 - wv) : 0;
  STAMP(1, 0);
  char* ks_ = smem;
  char* vs_ = smem + 16384;
  char* cs_ = smem + 32768;
  {
    const int key = l >> 1;
    const u16* ksrc = Kh + (size_t)key * 16 + (((l & 1) ^ ((key >> 3) & 1)) * 8);
    const int dd = l >> 2;
    const u16* vsrc = Vh + (size_t)dd * SEQ + (((l & 3) ^ ((dd >> 2) & 3)) * 8);
#pragma unroll
    for (int i = 0; i < 2; ++i) {
      const int u = wv + 8 * i;
      int key0 = qb - 256 + 32 * u;
      key0 = key0 < 0 ? 0 : key0;
      GLDS16(ksrc + (size_t)key0 * 16, ks_ + u * 1024 + l * 16);
      GLDS16(vsrc + key0, vs_ + u * 1024 + l * 16);
    }
  }
  const bf16x8 qf = *(const bf16x8*)(Qh + (size_t)(q0 + r) * 16 + 8 * hh);
  if (threadIdx.x < 2) {
    const unsigned o = threadIdx.x == 0 ? 0x3f803f80u : 0u;
    *(u32x4*)(cs_ + threadIdx.x * 16) = (u32x4){o, o, o, o};
  }
  WAIT_V(0);
  RAWBAR();
  STAMP(1, 1);
  const char* krd = ks_ + (32 * wv + r) * 32 + ((hh ^ ((r >> 3) & 1)) * 16);
  f32x16 zero;
#pragma unroll
  for (int e = 0; e < 16; ++e) zero[e] = 0.f;
  const float NEG = -3.0e38f;
  auto mask = [&](int kt, f32x16& s) {
    if (kt == 0) {
#pragma unroll
      for (int e = 0; e < 16; ++e) s[e] = ((e & 3) + 8 * (e >> 2) + 4 * hh > r) ? s[e] : NEG;
    }
    if (kt == 8) {
#pragma unroll
      for (int e = 0; e < 16; ++e) s[e] = ((e & 3) + 8 * (e >> 2) + 4 * hh <= r) ? s[e] : NEG;
    }
  };
  float mx = NEG;
  {
    bf16x8 kf[9];
#pragma unroll
    for (int kt = 0; kt < 9; ++kt) kf[kt] = *(const bf16x8*)(krd + kt * 1024);
    f32x16 s[2];
    s[0] = MFMA32(kf[0], qf, zero);
#pragma unroll
    for (int kt = 0; kt < 9; ++kt) {
      if (kt < 8) s[(kt + 1) & 1] = MFMA32(kf[kt + 1], qf, zero);
      f32x16& c = s[kt & 1];
      mask(kt, c);
      float m = NEG;
#pragma unroll
      for (int e = 0; e < 16; e += 2) m = fmaxf(fmaxf(c[e], c[e + 1]), m);
      if (FIRST) m = (kt >= kt0) ? m : NEG;
      mx = fmaxf(mx, m);
    }
  }
  mx = fmaxf(mx, __shfl_xor(mx, 32));
  const float c1 = 0.25f * 1.44269504088896340736f;
  const float mc = mx * c1;
  const f32x2 c1v = {c1, c1}, nmcv = {-mc, -mc};
  STAMP(1, 2);
  const int vsw = (r >> 2) & 3;
  const bool isd = r < 16;
  const int cad = 32768 + ((r == 16 || r == 20) ? 0 : 16);
  int va0 = isd ? 16384 + wv * 1024 + r * 64 + ((hh ^ vsw) * 16) : cad;
  int va1 = isd ? 16384 + wv * 1024 + r * 64 + (((2 + hh) ^ vsw) * 16) : cad;
  const int vstep = isd ? 1024 : 0;
  f32x16 oacc = zero;
  {
    f32x16 s[2];
    bf16x8 kf = *(const bf16x8*)(krd);
    s[0] = MFMA32(kf, qf, zero);
#pragma unroll
    for (int kt = 0; kt < 9; ++kt) {
      const bf16x8 vf0 = *(const bf16x8*)(smem + va0);
      const bf16x8 vf1 = *(const bf16x8*)(smem + va1);
      va0 += vstep;
      va1 += vstep;
      if (kt < 8) {
        kf = *(const bf16x8*)(krd + (kt + 1) * 1024);
        s[(kt + 1) & 1] = MFMA32(kf, qf, zero);
      }
      f32x16& c = s[kt & 1];
      mask(kt, c);
      if (!FIRST || kt >= kt0) {
        u32x4 pp[2];
#pragma unroll
        for (int e = 0; e < 16; e += 2) {
          f32x2 a = __builtin_elementwise_fma((f32x2){c[e], c[e + 1]}, c1v, nmcv);
          pp[e >> 3][(e >> 1) & 3] = pack2(__builtin_amdgcn_exp2f(a[0]), __builtin_amdgcn_exp2f(a[1]));
        }
        oacc = MFMA32(vf0, __builtin_bit_cast(bf16x8, pp[0]), oacc);
        oacc = MFMA32(vf1, __builtin_bit_cast(bf16x8, pp[1]), oacc);
      }
    }
  }
  STAMP(1, 3);
  const float inv = 1.f / oacc[8];
  u32x2 v0 = {pack2(oacc[0] * inv, oacc[1] * inv), pack2(oacc[2] * inv, oacc[3] * inv)};
  u32x2 v1 = {pack2(oacc[4] * inv, oacc[5] * inv), pack2(oacc[6] * inv, oacc[7] * inv)};
  u16* op = O + (size_t)(q0 + r) * DM + head * 16 + 4 * hh;
  *(u32x2*)(op) = v0;
  *(u32x2*)(op + 8) = v1;
  STAMP(1, 4);
}

DI void conv_block(const u16* __restrict__ ssmk, const u16* __restrict__ ssmD, char* __restrict__ ws, char* smem,
                   int d) {
  const int t = threadIdx.x & 255;
  const int l = t & 63, wv = t >> 6;
  const int r = l & 31, hh = l >> 5;
  char* hs = smem;
  char* kvs = smem + CV_HBYTES;
  const u16* kvT = (const u16*)(ws + WS_KVT) + (size_t)d * SEQ;
  const u16* hrow = ssmk + (size_t)d * SEQ;
  STAMP(1, 0);
  const u32x4 kvv = *(const u32x4*)(kvT + 8 * t);
  const u32x4 whi = *(const u32x4*)(hrow + 8 * t);
  const u32x4 wlo0 = *(const u32x4*)(hrow + 8 * (t > 0 ? t - 1 : 0));
  const float Dd = bf2f(ssmD[d]);
  *(u32x4*)(kvs + ((t >> 2) + 32) * CV_KVSTR + (t & 3) * 16) = kvv;
  if (t < 128) {
    u32x4 z = {0u, 0u, 0u, 0u};
    *(u32x4*)(kvs + (t >> 2) * CV_KVSTR + (t & 3) * 16) = z;
  }
  {
    const int E = t;
    u32x4 wlo = wlo0;
    if (E == 0) wlo = (u32x4){0u, 0u, 0u, 0u};
    unsigned z[8];
#pragma unroll
    for (int i = 0; i < 4; ++i) {
      const unsigned a = whi[3 - i];
      z[i] = (a >> 16) | (a << 16);
      const unsigned b = wlo[3 - i];
      z[4 + i] = (b >> 16) | (b << 16);
    }
    const int M = 255 - E;
#pragma unroll
    for (int p = 0; p < 8; ++p) {
      const int o = 7 - p;
      u32x4 v;
      if ((o & 1) == 0) {
        v[0] = z[o / 2];
        v[1] = z[o / 2 + 1];
        v[2] = z[o / 2 + 2];
        v[3] = z[o / 2 + 3];
      } else {
        v[0] = (z[o / 2] >> 16) | (z[o / 2 + 1] << 16);
        v[1] = (z[o / 2 + 1] >> 16) | (z[o / 2 + 2] << 16);
        v[2] = (z[o / 2 + 2] >> 16) | (z[o / 2 + 3] << 16);
        v[3] = (z[o / 2 + 3] >> 16) | (z[o / 2 + 4] << 16);
      }
      *(u32x4*)(hs + p * CV_HSTR + 16 * M) = v;
    }
    if (t < 24) {
      u32x4 zz = {0u, 0u, 0u, 0u};
      *(u32x4*)(hs + (t & 7) * CV_HSTR + 16 * (256 + (t >> 3))) = zz;
    }
  }
  __syncthreads();
  STAMP(1, 1);
  f32x16 acc0, acc1, acc2;
#pragma unroll
  for (int e = 0; e < 16; ++e) {
    acc0[e] = 0.f;
    acc1[e] = 0.f;
    acc2[e] = 0.f;
  }
  const char* abase = hs + (r & 7) * CV_HSTR + 16 * (255 - (r >> 3) + hh) - 64 * wv;
  const char* bbase = kvs + (r + 32 - wv) * CV_KVSTR + 16 * hh;
  {
    bf16x8 fa[2][2], fb[2][2];
    auto ldc = [&](int step, bf16x8(&a)[2], bf16x8(&b)[2]) {
      const int i = step >> 1, ks = step & 1;
      a[0] = *(const bf16x8*)(abase - 256 * i + 32 * ks);
      a[1] = *(const bf16x8*)(abase - 256 * i - 2048 + 32 * ks);
      b[1] = *(const bf16x8*)(bbase + (32 - 4 * i) * CV_KVSTR + 32 * ks);
      b[0] = *(const bf16x8*)(bbase - (4 * i) * CV_KVSTR + 32 * ks);
    };
    ldc(0, fa[0], fb[0]);
#pragma unroll
    for (int step = 0; step < 16; ++step) {
      if (step < 15) ldc(step + 1, fa[(step + 1) & 1], fb[(step + 1) & 1]);
      __builtin_amdgcn_sched_barrier(0);
      acc1 = MFMA32(fa[step & 1][0], fb[step & 1][1], acc1);
      acc0 = MFMA32(fa[step & 1][0], fb[step & 1][0], acc0);
      acc2 = MFMA32(fa[step & 1][1], fb[step & 1][0], acc2);
      __builtin_amdgcn_sched_barrier(0);
    }
  }
  acc1 += acc2;
  STAMP(1, 2);
  __syncthreads();
  STAMP(1, 6);
  float* red = (float*)smem;
  {
    float* rw = red + wv * (64 * CV_REDSTR);
#pragma unroll
    for (int g = 0; g < 4; ++g) {
      f32x4 v0 = {acc0[4 * g], acc0[4 * g + 1], acc0[4 * g + 2], acc0[4 * g + 3]};
      f32x4 v1 = {acc1[4 * g], acc1[4 * g + 1], acc1[4 * g + 2], acc1[4 * g + 3]};
      *(f32x4*)(rw + r * CV_REDSTR + 8 * g + 4 * hh) = v0;
      *(f32x4*)(rw + (32 + r) * CV_REDSTR + 8 * g + 4 * hh) = v1;
    }
  }
  __syncthreads();
  STAMP(1, 7);
  {
    const int a = t >> 2, b0 = 8 * (t & 3);
    f32x4 s0 = {0.f, 0.f, 0.f, 0.f}, s1 = {0.f, 0.f, 0.f, 0.f};
#pragma unroll
    for (int w = 0; w < 4; ++w) {
      const float* rp = red + w * (64 * CV_REDSTR) + a * CV_REDSTR + b0;
      s0 += *(const f32x4*)(rp);
      s1 += *(const f32x4*)(rp + 4);
    }
    float yy[8] = {s0[0], s0[1], s0[2], s0[3], s1[0], s1[1], s1[2], s1[3]};
    u32x4 o;
#pragma unroll
    for (int i = 0; i < 4; ++i) {
      const float y0 = rbf(yy[2 * i]) + rbf(bflo(kvv[i]) * Dd);
      const float y1 = rbf(yy[2 * i + 1]) + rbf(bfhi(kvv[i]) * Dd);
      o[i] = pack2(y0, y1);
    }
    u16* y2T = (u16*)(ws + WS_Y2T) + (size_t)d * SEQ;
    *(u32x4*)(y2T + 8 * t) = o;
  }
  STAMP(1, 4);
}

__global__ __launch_bounds__(512, 4) void k2_kernel(const u16* __restrict__ ssmk, const u16* __restrict__ ssmD,
                                                    char* __restrict__ ws) {
  __shared__ __attribute__((aligned(16))) char smem[2 * CV_SMEM];
  const int xcd = blockIdx.x & 7, bidx = (blockIdx.x >> 3) ^ 32;
  if (bidx >= 32) {
    const int half = threadIdx.x >> 8;
    conv_block(ssmk, ssmD, ws, smem + half * CV_SMEM, xcd * 64 + (bidx - 32) * 2 + half);
  } else {
    if (xcd == 0)
      attn_block<true>(ws, smem, bidx, 0);
    else
      attn_block<false>(ws, smem, bidx, xcd * 256);
  }
}

#define K3_STAGE 32768
#define K3_CSTR 68
#define K3_YSTR 136

__global__ __launch_bounds__(256) void k3_kernel(const u16* __restrict__ x, const u16* __restrict__ wo,
                                                 const char* __restrict__ ws, u16* __restrict__ out) {
  __shared__ __attribute__((aligned(16))) char smem[4 * K3_STAGE];
  const int t = threadIdx.x;
  const int l = t & 63, wv = t >> 6;
  const int r = l & 31, hh = l >> 5;
  const int wm = wv & 1, wn = wv >> 1;
  const int tm = (blockIdx.x & 7) * 4 + ((blockIdx.x >> 3) & 3), tn = blockIdx.x >> 5;
  const int m0 = tm * 64, n0 = tn * 64;
  const u16* o = (const u16*)(ws + WS_O);
  STAMP(2, 0);

  const u16* y2T = (const u16*)(ws + WS_Y2T);
  const float* gp = (const float*)(ws + WS_G);
  u32x4 yv[2], xq[2];
  float gv[2];
#pragma unroll
  for (int i = 0; i < 2; ++i) {
    const int idx = t + 256 * i;
    const int rr = idx >> 3, ch = idx & 7;
    yv[i] = *(const u32x4*)(y2T + (size_t)(n0 + rr) * SEQ + m0 + ch * 8);
    xq[i] = *(const u32x4*)(x + (size_t)(m0 + rr) * DM + n0 + ch * 8);
    gv[i] = gp[m0 + rr];
  }
  {
    char* ldsw = smem + wv * 4096 + l * 16;
#pragma unroll
    for (int st = 0; st < 4; ++st) {
#pragma unroll
      for (int i = 0; i < 4; ++i) {
        const int ar = 16 * wv + 4 * i + (l >> 4);
        GLDS16(o + (size_t)(m0 + ar) * DM + st * 128 + (((l & 15) ^ (ar & 15)) * 8), ldsw + st * K3_STAGE + i * 1024);
      }
#pragma unroll
      for (int i = 0; i < 4; ++i) {
        const int br = 32 * wv + 8 * i + (l >> 3);
        GLDS16(wo + (size_t)(st * 128 + br) * DM + n0 + (((l & 7) ^ (((br >> 1) & 1) << 2)) * 8),
               ldsw + st * K3_STAGE + 16384 + i * 1024);
      }
    }
  }
  f32x16 acc;
#pragma unroll
  for (int e = 0; e < 16; ++e) acc[e] = 0.f;
  const int blk = (l >> 4) & 1, q4 = (l & 15) >> 2, p4 = l & 3;
  const int arow = wm * 32 + r;
  const int a_off = arow * 256;
  const int a_sw = arow & 15;
  const int b_off = 16384 + (8 * hh + q4) * 128 + (((wn * 4 + 2 * blk + (p4 >> 1)) ^ (((q4 >> 1) & 1) << 2)) * 16) +
                    (p4 & 1) * 8;
#pragma unroll
  for (int st = 0; st < 4; ++st) {
    if (st == 0) wait_vm<24>();
    else if (st == 1) wait_vm<16>();
    else if (st == 2) wait_vm<8>();
    else wait_vm<0>();
    RAWBAR();
    STAMP(2, 1 + st);
    const char* sb = smem + st * K3_STAGE;
    bf16x8 af[8], bfr[8];
#pragma unroll
    for (int ks = 0; ks < 8; ++ks) {
      af[ks] = *(const bf16x8*)(sb + a_off + (((2 * ks + hh) ^ a_sw) * 16));
      s16x4 lo = tr_read(sb + b_off + (16 * ks) * 128);
      s16x4 hi = tr_read(sb + b_off + (16 * ks + 4) * 128);
      bfr[ks] = cat_frag(lo, hi);
    }
    __builtin_amdgcn_sched_barrier(0);
#pragma unroll
    for (int ks = 0; ks < 8; ++ks) acc = MFMA32(af[ks], bfr[ks], acc);
    __builtin_amdgcn_sched_barrier(0);
  }
  __syncthreads();
  STAMP(2, 5);
  float* Cs = (float*)smem;
  char* Ys = smem + 64 * K3_CSTR * 4;
#pragma unroll
  for (int e = 0; e < 16; ++e) {
    const int row = wm * 32 + (e & 3) + 8 * (e >> 2) + 4 * hh;
    Cs[row * K3_CSTR + wn * 32 + r] = acc[e];
  }
#pragma unroll
  for (int i = 0; i < 2; ++i) {
    const int idx = t + 256 * i;
    const int dr = idx >> 3, ch = idx & 7;
    u32x2 a = {yv[i][0], yv[i][1]}, b = {yv[i][2], yv[i][3]};
    *(u32x2*)(Ys + dr * K3_YSTR + ch * 16) = a;
    *(u32x2*)(Ys + dr * K3_YSTR + ch * 16 + 8) = b;
  }
  __syncthreads();
#pragma unroll
  for (int i = 0; i < 2; ++i) {
    const int idx = t + 256 * i;
    const int row = idx >> 3, ch = idx & 7;
    const int s = m0 + row;
    const float g = gv[i];
    const float omg = rbf(1.f - g);
    const f32x4 c0 = *(const f32x4*)(Cs + row * K3_CSTR + ch * 8);
    const f32x4 c1 = *(const f32x4*)(Cs + row * K3_CSTR + ch * 8 + 4);
    float bb[8] = {c0[0], c0[1], c0[2], c0[3], c1[0], c1[1], c1[2], c1[3]};
    float res[8];
#pragma unroll
    for (int j = 0; j < 8; ++j) {
      const float y2 = bf2f(*(const u16*)(Ys + (ch * 8 + j) * K3_YSTR + row * 2));
      const float qv = (j & 1) ? bfhi(xq[i][j >> 1]) : bflo(xq[i][j >> 1]);
      const float a = rbf(qv * y2);
      const float ga = rbf(g * a);
      const float gb = rbf(omg * rbf(bb[j]));
      res[j] = ga + gb;
    }
    u32x4 ov;
    ov[0] = pack2(res[0], res[1]);
    ov[1] = pack2(res[2], res[3]);
    ov[2] = pack2(res[4], res[5]);
    ov[3] = pack2(res[6], res[7]);
    *(u32x4*)(out + (size_t)s * DM + n0 + ch * 8) = ov;
  }
  STAMP(2, 6);
}

extern "C" void kernel_launch(void* const* d_in, const int* in_sizes, int n_in, void* d_out, int out_size,
                              void* d_ws, size_t ws_size, hipStream_t stream) {
  const u16* A_q = (const u16*)d_in[0];
  const u16* A_k = (const u16*)d_in[1];
  const u16* A_v = (const u16*)d_in[2];
  const u16* A_shift = (const u16*)d_in[3];
  const u16* A_ssmk = (const u16*)d_in[4];
  const u16* A_ssmD = (const u16*)d_in[5];
  const u16* B_wqkv = (const u16*)d_in[7];
  const u16* B_wo = (const u16*)d_in[8];
  const u16* gate_w = (const u16*)d_in[9];
  char* ws = (char*)d_ws;
  k1_kernel<<<256, 512, 0, stream>>>(A_q, A_k, A_v, A_shift, B_wqkv, gate_w, ws);
  k2_kernel<<<512, 512, 0, stream>>>(A_ssmk, A_ssmD, ws);
  k3_kernel<<<256, 256, 0, stream>>>(A_q, B_wo, ws, (u16*)d_out);
}
```

```cpp
#include <hip/hip_runtime.h>
#include <hip/hip_bf16.h>
#include <stdint.h>

#define DI __device__ __forceinline__
typedef __attribute__((ext_vector_type(8))) __bf16 bf16x8;
typedef __attribute__((ext_vector_type(4))) __bf16 bf16x4;
typedef __attribute__((ext_vector_type(2))) __bf16 bf16x2;
typedef __attribute__((ext_vector_type(4))) short s16x4;
typedef __attribute__((ext_vector_type(16))) float f32x16;
typedef __attribute__((ext_vector_type(4))) float f32x4;
typedef __attribute__((ext_vector_type(2))) float f32x2;
typedef __attribute__((ext_vector_type(4))) unsigned u32x4;
typedef __attribute__((ext_vector_type(2))) unsigned u32x2;
typedef unsigned short u16;

#define SEQ 2048
#define DM 512

#define MFMA32(a, b, c) __builtin_amdgcn_mfma_f32_32x32x16_bf16((a), (b), (c), 0, 0, 0)

DI float bf2f(u16 v) { return __uint_as_float(((unsigned)v) << 16); }
DI float bflo(unsigned v) { return __uint_as_float(v << 16); }
DI float bfhi(unsigned v) { return __uint_as_float(v & 0xffff0000u); }
DI unsigned pack2(float a, float b) {
  f32x2 f = {a, b};
  bf16x2 r = __builtin_convertvector(f, bf16x2);
  return __builtin_bit_cast(unsigned, r);
}
DI float rbf(float a) {
  return (float)((__bf16)a);
}
DI s16x4 tr_read(const char* p) {
  typedef __attribute__((address_space(3))) s16x4* lp;
  return __builtin_amdgcn_ds_read_tr16_b64_v4i16((lp)(p));
}
DI bf16x8 cat_frag(s16x4 lo, s16x4 hi) {
  typedef __attribute__((ext_vector_type(8))) short s16x8;
  s16x8 v = __builtin_shufflevector(lo, hi, 0, 1, 2, 3, 4, 5, 6, 7);
  return __builtin_bit_cast(bf16x8, v);
}

#define STAMP(k, i) do {} while (0)
#define WS_Q 0
#define WS_K (2u << 20)
#define WS_VT (4u << 20)
#define WS_KVT (6u << 20)
#define WS_Y2T (8u << 20)
#define WS_O (10u << 20)
#define WS_G (12u << 20)

#define WT_AUX 0
typedef __amdgpu_buffer_rsrc_t rsrc_t;
DI rsrc_t ws_rsrc(const char* ws) { return __builtin_amdgcn_make_buffer_rsrc((void*)ws, (short)0, 16 << 20, 0x00020000); }
DI void wst16(rsrc_t rs, unsigned off, u32x4 v) { __builtin_amdgcn_raw_buffer_store_b128(v, rs, (int)off, 0, WT_AUX); }
DI void wst8(rsrc_t rs, unsigned off, u32x2 v) { __builtin_amdgcn_raw_buffer_store_b64(v, rs, (int)off, 0, WT_AUX); }

#define GLDS16(g, l) __builtin_amdgcn_global_load_lds((const unsigned*)(g), (unsigned*)(l), 16, 0, 0)
#define WAIT_V(n) asm volatile("s_waitcnt vmcnt(" #n ")" ::: "memory")
#define RAWBAR() do { asm volatile("s_waitcnt lgkmcnt(0)" ::: "memory"); __builtin_amdgcn_s_barrier(); } while (0)
#define K1_STAGE 32768

template <int N>
DI void wait_vm() {
  if constexpr (N == 0) WAIT_V(0);
  else if constexpr (N == 4) WAIT_V(4);
  else if constexpr (N == 8) WAIT_V(8);
  else if constexpr (N == 12) WAIT_V(12);
  else if constexpr (N == 16) WAIT_V(16);
  else if constexpr (N == 24) WAIT_V(24);
}

#define QSCALE (0.25f * 1.44269504088896340736f)

template <int MODE>
DI void k1_gemm(const u16* __restrict__ x, const u16* __restrict__ w, char* __restrict__ ws, char* smem, int tm,
                int tn) {
  const int t = threadIdx.x;
  const int l = t & 63, wv = t >> 6;
  const int r = l & 31, hh = l >> 5;
  const int wm = wv & 1, wn = wv >> 1;
  const int m0 = tm * 128, n0 = tn * 128;

  const u16* xg[2];
  const u16* wg[2];
#pragma unroll
  for (int i = 0; i < 2; ++i) {
    const int ar = 16 * wv + 8 * i + (l >> 3);
    xg[i] = x + (size_t)(m0 + ar) * DM + (((l & 7) ^ ((ar >> 1) & 7)) * 8);
    const int br = 8 * wv + 4 * i + (l >> 4);
    wg[i] = w + (size_t)br * 1536 + n0 + (((l & 15) ^ ((br & 3) << 2)) * 8);
  }
  char* ldsw = smem + wv * 2048 + l * 16;

  const int rot = 0;
  auto issue = [&](int kt) {
    char* st = ldsw + (kt & 3) * K1_STAGE;
    const int kk = (kt + rot) & 7;
#pragma unroll
    for (int i = 0; i < 2; ++i) GLDS16(xg[i] + kk * 64, st + i * 1024);
#pragma unroll
    for (int i = 0; i < 2; ++i) GLDS16(wg[i] + (size_t)kk * 64 * 1536, st + 16384 + i * 1024);
  };

  f32x16 acc[2];
#pragma unroll
  for (int i = 0; i < 2; ++i)
#pragma unroll
    for (int e = 0; e < 16; ++e) acc[i][e] = 0.f;

  const int blk = (l >> 4) & 1, q4 = (l & 15) >> 2, p4 = l & 3;
  const int a_row_off = (wm * 64 + r) * 128;
  const int a_sw = (r >> 1) & 7;
  const int b_base = 16384 + (8 * hh + q4) * 256 + (p4 & 1) * 8 + (((wn * 4 + 2 * blk + (p4 >> 1)) ^ (q4 << 2)) * 16);

  STAMP(0, 0);
  issue(0);
  issue(1);
  issue(2);
  issue(3);
  bf16x8 fa[2][4][2], fb[2][4];
  auto ldfr = [&](int kt, bf16x8(&a)[4][2], bf16x8(&b)[4]) {
    const char* st = smem + (kt & 3) * K1_STAGE;
#pragma unroll
    for (int ks = 0; ks < 4; ++ks) {
#pragma unroll
      for (int i = 0; i < 2; ++i)
        a[ks][i] = *(const bf16x8*)(st + a_row_off + i * 32 * 128 + (((2 * ks + hh) ^ a_sw) * 16));
      s16x4 lo = tr_read(st + b_base + (16 * ks) * 256);
      s16x4 hi = tr_read(st + b_base + (16 * ks + 4) * 256);
      b[ks] = cat_frag(lo, hi);
    }
  };
  wait_vm<12>();
  RAWBAR();
  ldfr(0, fa[0], fb[0]);
#pragma unroll
  for (int kt = 0; kt < 8; ++kt) {
    if (kt < 7) {
      if (kt <= 4) wait_vm<8>();
      else if (kt == 5) wait_vm<4>();
      else wait_vm<0>();
      RAWBAR();
      ldfr(kt + 1, fa[(kt + 1) & 1], fb[(kt + 1) & 1]);
    }
    __builtin_amdgcn_sched_barrier(0);
#pragma unroll
    for (int ks = 0; ks < 4; ++ks) {
#pragma unroll
      for (int i = 0; i < 2; ++i) {
        if (MODE == 2)
          acc[i] = MFMA32(fa[kt & 1][ks][i], fb[kt & 1][ks], acc[i]);
        else
          acc[i] = MFMA32(fb[kt & 1][ks], fa[kt & 1][ks][i], acc[i]);
      }
      __builtin_amdgcn_sched_barrier(0);
      if (kt + 4 <= 7) {
        char* st4 = ldsw + (kt & 3) * K1_STAGE;
        const int kk = (kt + 4 + rot) & 7;
        if (ks < 2)
          GLDS16(xg[ks] + kk * 64, st4 + ks * 1024);
        else
          GLDS16(wg[ks - 2] + (size_t)kk * 64 * 1536, st4 + 16384 + (ks - 2) * 1024);
      }
      __builtin_amdgcn_sched_barrier(0);
    }
  }

  STAMP(0, 9);
  const rsrc_t rs = ws_rsrc(ws);
  if (MODE == 2) {
#pragma unroll
    for (int i = 0; i < 2; ++i) {
      const int n = (n0 - 1024) + wn * 32 + r;
      const int tok0 = m0 + wm * 64 + i * 32;
      const unsigned dst = WS_VT + (unsigned)(n * SEQ + tok0 + 8 * hh) * 2;
#pragma unroll
      for (int bb = 0; bb < 2; ++bb) {
        u32x4 v;
        v[0] = pack2(acc[i][8 * bb + 0], acc[i][8 * bb + 1]);
        v[1] = pack2(acc[i][8 * bb + 2], acc[i][8 * bb + 3]);
        v[2] = pack2(acc[i][8 * bb + 4], acc[i][8 * bb + 5]);
        v[3] = pack2(acc[i][8 * bb + 6], acc[i][8 * bb + 7]);
        wst16(rs, dst + 32 * bb, v);
      }
    }
  } else {
    const unsigned base = (MODE == 1 ? WS_K : WS_Q);
    const int nn0 = n0 & 511;
#pragma unroll
    for (int i = 0; i < 2; ++i) {
      const int tok = m0 + wm * 64 + i * 32 + r;
      const int nb = nn0 + wn * 32;
      const int head = nb >> 4;
#pragma unroll
      for (int g = 0; g < 4; ++g) {
        u32x2 v;
        const float sc = (MODE == 0) ? QSCALE : 1.f;
        v[0] = pack2(acc[i][4 * g + 0] * sc, acc[i][4 * g + 1] * sc);
        v[1] = pack2(acc[i][4 * g + 2] * sc, acc[i][4 * g + 3] * sc);
        wst8(rs, base + (unsigned)(((head + (g >> 1)) * SEQ + tok) * 16 + 8 * (g & 1) + 4 * hh) * 2, v);
      }
    }
  }
  STAMP(0, 10);
}

__global__ __launch_bounds__(512) void k1_kernel(const u16* __restrict__ x, const u16* __restrict__ kin,
                                                 const u16* __restrict__ vin, const u16* __restrict__ shiftk,
                                                 const u16* __restrict__ wqkv, const u16* __restrict__ gatew,
                                                 char* __restrict__ ws) {
  __shared__ __attribute__((aligned(16))) char smem[4 * K1_STAGE];
  const int xcd = blockIdx.x & 7, bidx = blockIdx.x >> 3;
  if (bidx < 24) {
    const int tm = 2 * xcd + (bidx & 1), tn = bidx >> 1;
    if (tn >= 8)
      k1_gemm<2>(x, wqkv, ws, smem, tm, tn);
    else if (tn >= 4)
      k1_gemm<1>(x, wqkv, ws, smem, tm, tn);
    else
      k1_gemm<0>(x, wqkv, ws, smem, tm, tn);
    return;
  }
  const int l = threadIdx.x & 63, wv = threadIdx.x >> 6;
  const int t = threadIdx.x & 255, h2 = threadIdx.x >> 8;
  const int pb = xcd * 8 + (bidx - 24);
  STAMP(0, 0);
  const int wid = pb * 8 + wv;
  const u32x4 gw = *(const u32x4*)(gatew + l * 8);
  u32x4 xv[4];
#pragma unroll
  for (int it = 0; it < 4; ++it) xv[it] = *(const u32x4*)(x + (size_t)(wid * 4 + it) * DM + l * 8);
  {
    const int ch = t & 15, rg = t >> 4;
    u32x4 kr[7], vr[4], sk[4];
    const int tile = pb * 2 + h2;
    const int s0 = (tile >> 2) * 64, d0 = (tile & 3) * 128;
    {
      const int sb = s0 + rg * 4;
      const int dd = d0 + ch * 8;
#pragma unroll
      for (int i = 0; i < 7; ++i) {
        const int s = sb - 3 + i;
        u32x4 z = {0u, 0u, 0u, 0u};
        kr[i] = (s >= 0) ? *(const u32x4*)(kin + (size_t)s * DM + dd) : z;
      }
#pragma unroll
      for (int i = 0; i < 4; ++i) vr[i] = *(const u32x4*)(vin + (size_t)(sb + i) * DM + dd);
#pragma unroll
      for (int i = 0; i < 4; ++i) sk[i] = *(const u32x4*)(shiftk + (size_t)dd * 4 + i * 8);
    }
    char* sm = smem + h2 * 17408;
#pragma unroll
    for (int c = 0; c < 8; ++c) {
      const unsigned s01 = sk[c >> 1][(c & 1) * 2], s23 = sk[c >> 1][(c & 1) * 2 + 1];
      const float sk0 = bflo(s01), sk1 = bfhi(s01), sk2 = bflo(s23), sk3 = bfhi(s23);
      float kk[7];
#pragma unroll
      for (int i = 0; i < 7; ++i) kk[i] = (c & 1) ? bfhi(kr[i][c >> 1]) : bflo(kr[i][c >> 1]);
      unsigned o[2];
#pragma unroll
      for (int i = 0; i < 4; i += 2) {
        const unsigned p3 = pack2(kk[i] * sk3, kk[i + 1] * sk3);
        const unsigned p2 = pack2(kk[i + 1] * sk2, kk[i + 2] * sk2);
        const unsigned p1 = pack2(kk[i + 2] * sk1, kk[i + 3] * sk1);
        const unsigned p0 = pack2(kk[i + 3] * sk0, kk[i + 4] * sk0);
        unsigned a = pack2(bflo(p3) + bflo(p2), bfhi(p3) + bfhi(p2));
        a = pack2(bflo(a) + bflo(p1), bfhi(a) + bfhi(p1));
        a = pack2(bflo(a) + bflo(p0), bfhi(a) + bfhi(p0));
        const float v0 = (c & 1) ? bfhi(vr[i][c >> 1]) : bflo(vr[i][c >> 1]);
        const float v1 = (c & 1) ? bfhi(vr[i + 1][c >> 1]) : bflo(vr[i + 1][c >> 1]);
        o[i >> 1] = pack2(bflo(a) * v0, bfhi(a) * v1);
      }
      *(u32x2*)(sm + (ch * 8 + c) * 136 + rg * 8) = (u32x2){o[0], o[1]};
    }
    __syncthreads();
    const rsrc_t rs = ws_rsrc(ws);
#pragma unroll
    for (int i = 0; i < 4; ++i) {
      const int idx = t + 256 * i;
      const int dr = idx >> 3, c8 = idx & 7;
      const u32x2 a = *(const u32x2*)(sm + dr * 136 + c8 * 16);
      const u32x2 b = *(const u32x2*)(sm + dr * 136 + c8 * 16 + 8);
      u32x4 v = {a[0], a[1], b[0], b[1]};
      wst16(rs, WS_KVT + (unsigned)((d0 + dr) * SEQ + s0 + c8 * 8) * 2, v);
    }
  }
  STAMP(0, 11);
  {
    float* g = (float*)(ws + WS_G);
    float sums[4];
#pragma unroll
    for (int it = 0; it < 4; ++it) {
      float sum = 0.f;
#pragma unroll
      for (int i = 0; i < 4; ++i) {
        const unsigned pr = pack2(bflo(xv[it][i]) * bflo(gw[i]), bfhi(xv[it][i]) * bfhi(gw[i]));
        sum += bflo(pr);
        sum += bfhi(pr);
      }
      sums[it] = sum;
    }
#pragma unroll
    for (int o = 32; o >= 1; o >>= 1)
#pragma unroll
      for (int it = 0; it < 4; ++it) sums[it] += __shfl_xor(sums[it], o);
    if (l < 4) {
      float sv = sums[0];
#pragma unroll
      for (int it = 1; it < 4; ++it) sv = (l == it) ? sums[it] : sv;
      const float z = rbf(sv);
      g[wid * 4 + l] = rbf(1.f / (1.f + __expf(-z)));
    }
  }
  STAMP(0, 12);
}

#define CV_KVSTR 80
#define CV_HSTR 4160
#define CV_HBYTES (8 * CV_HSTR)
#define CV_REDSTR 36
#define CV_SMEM 40960

template <bool FIRST>
DI void attn_block(const char* __restrict__ ws, char* smem, int head, int qb) {
  const int l = threadIdx.x & 63, wv = threadIdx.x >> 6;
  const int r = l & 31, hh = l >> 5;
  const u16* Qh = (const u16*)(ws + WS_Q) + (size_t)head * SEQ * 16;
  const u16* Kh = (const u16*)(ws + WS_K) + (size_t)head * SEQ * 16;
  const u16* Vh = (const u16*)(ws + WS_VT) + (size_t)head * 16 * SEQ;
  u16* O = (u16*)(ws + WS_O);
  const int q0 = qb + 32 * wv;
  const int kt0 = FIRST ? (8 - wv) : 0;
  STAMP(1, 0);
  char* ks_ = smem;
  char* vs_ = smem + 16384;
  char* cs_ = smem + 32768;
  {
    const int key = l >> 1;
    const u16* ksrc = Kh + (size_t)key * 16 + (((l & 1) ^ ((key >> 3) & 1)) * 8);
    const int dd = l >> 2;
    const u16* vsrc = Vh + (size_t)dd * SEQ + (((l & 3) ^ ((dd >> 2) & 3)) * 8);
#pragma unroll
    for (int i = 0; i < 2; ++i) {
      const int u = wv + 8 * i;
      int key0 = qb - 256 + 32 * u;
      key0 = key0 < 0 ? 0 : key0;
      GLDS16(ksrc + (size_t)key0 * 16, ks_ + u * 1024 + l * 16);
      GLDS16(vsrc + key0, vs_ + u * 1024 + l * 16);
    }
  }
  const bf16x8 qf = *(const bf16x8*)(Qh + (size_t)(q0 + r) * 16 + 8 * hh);
  if (threadIdx.x < 2) {
    const unsigned o = threadIdx.x == 0 ? 0x3f803f80u : 0u;
    *(u32x4*)(cs_ + threadIdx.x * 16) = (u32x4){o, o, o, o};
  }
  WAIT_V(0);
  RAWBAR();
  STAMP(1, 1);
  const char* krd = ks_ + (32 * wv + r) * 32 + ((hh ^ ((r >> 3) & 1)) * 16);
  f32x16 zero;
#pragma unroll
  for (int e = 0; e < 16; ++e) zero[e] = 0.f;
  const float NEG = -3.0e38f;
  auto mask = [&](int kt, f32x16& s) {
    if (kt == 0) {
#pragma unroll
      for (int e = 0; e < 16; ++e) s[e] = ((e & 3) + 8 * (e >> 2) + 4 * hh > r) ? s[e] : NEG;
    }
    if (kt == 8) {
#pragma unroll
      for (int e = 0; e < 16; ++e) s[e] = ((e & 3) + 8 * (e >> 2) + 4 * hh <= r) ? s[e] : NEG;
    }
  };
  float mx = NEG;
  {
    bf16x8 kf[9];
#pragma unroll
    for (int kt = 0; kt < 9; ++kt) kf[kt] = *(const bf16x8*)(krd + kt * 1024);
    f32x16 s[2];
    s[0] = MFMA32(kf[0], qf, zero);
#pragma unroll
    for (int kt = 0; kt < 9; ++kt) {
      if (kt < 8) s[(kt + 1) & 1] = MFMA32(kf[kt + 1], qf, zero);
      f32x16& c = s[kt & 1];
      mask(kt, c);
      float m = NEG;
#pragma unroll
      for (int e = 0; e < 16; e += 2) m = fmaxf(fmaxf(c[e], c[e + 1]), m);
      if (FIRST) m = (kt >= kt0) ? m : NEG;
      mx = fmaxf(mx, m);
    }
  }
  mx = fmaxf(mx, __shfl_xor(mx, 32));
  f32x16 negm;
#pragma unroll
  for (int e = 0; e < 16; ++e) negm[e] = -mx;
  STAMP(1, 2);
  const int vsw = (r >> 2) & 3;
  const bool isd = r < 16;
  const int cad = 32768 + ((r == 16 || r == 20) ? 0 : 16);
  int va0 = isd ? 16384 + wv * 1024 + r * 64 + ((hh ^ vsw) * 16) : cad;
  int va1 = isd ? 16384 + wv * 1024 + r * 64 + (((2 + hh) ^ vsw) * 16) : cad;
  const int vstep = isd ? 1024 : 0;
  f32x16 oacc = zero;
  {
    f32x16 s[2];
    bf16x8 kf = *(const bf16x8*)(krd);
    s[0] = MFMA32(kf, qf, negm);
#pragma unroll
    for (int kt = 0; kt < 9; ++kt) {
      const bf16x8 vf0 = *(const bf16x8*)(smem + va0);
      const bf16x8 vf1 = *(const bf16x8*)(smem + va1);
      va0 += vstep;
      va1 += vstep;
      if (kt < 8) {
        kf = *(const bf16x8*)(krd + (kt + 1) * 1024);
        s[(kt + 1) & 1] = MFMA32(kf, qf, negm);
      }
      f32x16& c = s[kt & 1];
      mask(kt, c);
      if (!FIRST || kt >= kt0) {
        u32x4 pp[2];
#pragma unroll
        for (int e = 0; e < 16; e += 2) {
          pp[e >> 3][(e >> 1) & 3] = pack2(__builtin_amdgcn_exp2f(c[e]), __builtin_amdgcn_exp2f(c[e + 1]));
        }
        oacc = MFMA32(vf0, __builtin_bit_cast(bf16x8, pp[0]), oacc);
        oacc = MFMA32(vf1, __builtin_bit_cast(bf16x8, pp[1]), oacc);
      }
    }
  }
  STAMP(1, 3);
  const float inv = 1.f / oacc[8];
  u32x2 v0 = {pack2(oacc[0] * inv, oacc[1] * inv), pack2(oacc[2] * inv, oacc[3] * inv)};
  u32x2 v1 = {pack2(oacc[4] * inv, oacc[5] * inv), pack2(oacc[6] * inv, oacc[7] * inv)};
  const rsrc_t rs = ws_rsrc(ws);
  const unsigned op = WS_O + (unsigned)((q0 + r) * DM + head * 16 + 4 * hh) * 2;
  wst8(rs, op, v0);
  wst8(rs, op + 16, v1);
  STAMP(1, 4);
}

DI void conv_block(const u16* __restrict__ ssmk, const u16* __restrict__ ssmD, char* __restrict__ ws, char* smem,
                   int d) {
  const int t = threadIdx.x & 255;
  const int l = t & 63, wv = t >> 6;
  const int r = l & 31, hh = l >> 5;
  char* hs = smem;
  char* kvs = smem + CV_HBYTES;
  const u16* kvT = (const u16*)(ws + WS_KVT) + (size_t)d * SEQ;
  const u16* hrow = ssmk + (size_t)d * SEQ;
  STAMP(1, 0);
  const u32x4 kvv = *(const u32x4*)(kvT + 8 * t);
  const u32x4 whi = *(const u32x4*)(hrow + 8 * t);
  const u32x4 wlo0 = *(const u32x4*)(hrow + 8 * (t > 0 ? t - 1 : 0));
  const float Dd = bf2f(ssmD[d]);
  *(u32x4*)(kvs + ((t >> 2) + 32) * CV_KVSTR + (t & 3) * 16) = kvv;
  if (t < 128) {
    u32x4 z = {0u, 0u, 0u, 0u};
    *(u32x4*)(kvs + (t >> 2) * CV_KVSTR + (t & 3) * 16) = z;
  }
  {
    const int E = t;
    u32x4 wlo = wlo0;
    if (E == 0) wlo = (u32x4){0u, 0u, 0u, 0u};
    unsigned z[8];
#pragma unroll
    for (int i = 0; i < 4; ++i) {
      const unsigned a = whi[3 - i];
      z[i] = (a >> 16) | (a << 16);
      const unsigned b = wlo[3 - i];
      z[4 + i] = (b >> 16) | (b << 16);
    }
    const int M = 255 - E;
#pragma unroll
    for (int p = 0; p < 8; ++p) {
      const int o = 7 - p;
      u32x4 v;
      if ((o & 1) == 0) {
        v[0] = z[o / 2];
        v[1] = z[o / 2 + 1];
        v[2] = z[o / 2 + 2];
        v[3] = z[o / 2 + 3];
      } else {
        v[0] = (z[o / 2] >> 16) | (z[o / 2 + 1] << 16);
        v[1] = (z[o / 2 + 1] >> 16) | (z[o / 2 + 2] << 16);
        v[2] = (z[o / 2 + 2] >> 16) | (z[o / 2 + 3] << 16);
        v[3] = (z[o / 2 + 3] >> 16) | (z[o / 2 + 4] << 16);
      }
      *(u32x4*)(hs + p * CV_HSTR + 16 * M) = v;
    }
    if (t < 24) {
      u32x4 zz = {0u, 0u, 0u, 0u};
      *(u32x4*)(hs + (t & 7) * CV_HSTR + 16 * (256 + (t >> 3))) = zz;
    }
  }
  __syncthreads();
  STAMP(1, 1);
  f32x16 acc0, acc1, acc2;
#pragma unroll
  for (int e = 0; e < 16; ++e) {
    acc0[e] = 0.f;
    acc1[e] = 0.f;
    acc2[e] = 0.f;
  }
  const char* abase = hs + (r & 7) * CV_HSTR + 16 * (255 - (r >> 3) + hh) - 64 * wv;
  const char* bbase = kvs + (r + 32 - wv) * CV_KVSTR + 16 * hh;
  {
    bf16x8 fa[2][2], fb[2][2];
    auto ldc = [&](int step, bf16x8(&a)[2], bf16x8(&b)[2]) {
      const int i = step >> 1, ks = step & 1;
      a[0] = *(const bf16x8*)(abase - 256 * i + 32 * ks);
      a[1] = *(const bf16x8*)(abase - 256 * i - 2048 + 32 * ks);
      b[1] = *(const bf16x8*)(bbase + (32 - 4 * i) * CV_KVSTR + 32 * ks);
      b[0] = *(const bf16x8*)(bbase - (4 * i) * CV_KVSTR + 32 * ks);
    };
    ldc(0, fa[0], fb[0]);
#pragma unroll
    for (int step = 0; step < 16; ++step) {
      if (step < 15) ldc(step + 1, fa[(step + 1) & 1], fb[(step + 1) & 1]);
      __builtin_amdgcn_sched_barrier(0);
      acc1 = MFMA32(fa[step & 1][0], fb[step & 1][1], acc1);
      acc0 = MFMA32(fa[step & 1][0], fb[step & 1][0], acc0);
      acc2 = MFMA32(fa[step & 1][1], fb[step & 1][0], acc2);
      __builtin_amdgcn_sched_barrier(0);
    }
  }
  acc1 += acc2;
  STAMP(1, 2);
  __syncthreads();
  STAMP(1, 6);
  float* red = (float*)smem;
  {
    float* rw = red + wv * (64 * CV_REDSTR);
#pragma unroll
    for (int g = 0; g < 4; ++g) {
      f32x4 v0 = {acc0[4 * g], acc0[4 * g + 1], acc0[4 * g + 2], acc0[4 * g + 3]};
      f32x4 v1 = {acc1[4 * g], acc1[4 * g + 1], acc1[4 * g + 2], acc1[4 * g + 3]};
      *(f32x4*)(rw + r * CV_REDSTR + 8 * g + 4 * hh) = v0;
      *(f32x4*)(rw + (32 + r) * CV_REDSTR + 8 * g + 4 * hh) = v1;
    }
  }
  __syncthreads();
  STAMP(1, 7);
  {
    const int a = t >> 2, b0 = 8 * (t & 3);
    f32x4 s0 = {0.f, 0.f, 0.f, 0.f}, s1 = {0.f, 0.f, 0.f, 0.f};
#pragma unroll
    for (int w = 0; w < 4; ++w) {
      const float* rp = red + w * (64 * CV_REDSTR) + a * CV_REDSTR + b0;
      s0 += *(const f32x4*)(rp);
      s1 += *(const f32x4*)(rp + 4);
    }
    float yy[8] = {s0[0], s0[1], s0[2], s0[3], s1[0], s1[1], s1[2], s1[3]};
    u32x4 o;
#pragma unroll
    for (int i = 0; i < 4; ++i) {
      const float y0 = rbf(yy[2 * i]) + rbf(bflo(kvv[i]) * Dd);
      const float y1 = rbf(yy[2 * i + 1]) + rbf(bfhi(kvv[i]) * Dd);
      o[i] = pack2(y0, y1);
    }
    wst16(ws_rsrc(ws), WS_Y2T + (unsigned)(d * SEQ + 8 * t) * 2, o);
  }
  STAMP(1, 4);
}

__global__ __launch_bounds__(512, 4) void k2_kernel(const u16* __restrict__ ssmk, const u16* __restrict__ ssmD,
                                                    char* __restrict__ ws) {
  __shared__ __attribute__((aligned(16))) char smem[2 * CV_SMEM];
  const int xcd = blockIdx.x & 7, bidx = (blockIdx.x >> 3) ^ 32;
  if (bidx >= 32) {
    const int half = threadIdx.x >> 8;
    conv_block(ssmk, ssmD, ws, smem + half * CV_SMEM, xcd * 64 + (bidx - 32) * 2 + half);
  } else {
    if (xcd == 0)
      attn_block<true>(ws, smem, bidx, 0);
    else
      attn_block<false>(ws, smem, bidx, xcd * 256);
  }
}

#define K3_STAGE 32768
#define K3_CSTR 68
#define K3_YSTR 136

__global__ __launch_bounds__(256) void k3_kernel(const u16* __restrict__ x, const u16* __restrict__ wo,
                                                 const char* __restrict__ ws, u16* __restrict__ out) {
  __shared__ __attribute__((aligned(16))) char smem[4 * K3_STAGE];
  const int t = threadIdx.x;
  const int l = t & 63, wv = t >> 6;
  const int r = l & 31, hh = l >> 5;
  const int wm = wv & 1, wn = wv >> 1;
  const int tm = (blockIdx.x & 7) * 4 + ((blockIdx.x >> 3) & 3), tn = blockIdx.x >> 5;
  const int m0 = tm * 64, n0 = tn * 64;
  const u16* o = (const u16*)(ws + WS_O);
  STAMP(2, 0);

  const u16* y2T = (const u16*)(ws + WS_Y2T);
  const float* gp = (const float*)(ws + WS_G);
  u32x4 yv[2], xq[2];
  float gv[2];
#pragma unroll
  for (int i = 0; i < 2; ++i) {
    const int idx = t + 256 * i;
    const int rr = idx >> 3, ch = idx & 7;
    yv[i] = *(const u32x4*)(y2T + (size_t)(n0 + rr) * SEQ + m0 + ch * 8);
    xq[i] = *(const u32x4*)(x + (size_t)(m0 + rr) * DM + n0 + ch * 8);
    gv[i] = gp[m0 + rr];
  }
  {
    char* ldsw = smem + wv * 4096 + l * 16;
#pragma unroll
    for (int st = 0; st < 4; ++st) {
#pragma unroll
      for (int i = 0; i < 4; ++i) {
        const int ar = 16 * wv + 4 * i + (l >> 4);
        GLDS16(o + (size_t)(m0 + ar) * DM + st * 128 + (((l & 15) ^ (ar & 15)) * 8), ldsw + st * K3_STAGE + i * 1024);
      }
#pragma unroll
      for (int i = 0; i < 4; ++i) {
        const int br = 32 * wv + 8 * i + (l >> 3);
        GLDS16(wo + (size_t)(st * 128 + br) * DM + n0 + (((l & 7) ^ (((br >> 1) & 1) << 2)) * 8),
               ldsw + st * K3_STAGE + 16384 + i * 1024);
      }
    }
  }
  f32x16 acc;
#pragma unroll
  for (int e = 0; e < 16; ++e) acc[e] = 0.f;
  const int blk = (l >> 4) & 1, q4 = (l & 15) >> 2, p4 = l & 3;
  const int arow = wm * 32 + r;
  const int a_off = arow * 256;
  const int a_sw = arow & 15;
  const int b_off = 16384 + (8 * hh + q4) * 128 + (((wn * 4 + 2 * blk + (p4 >> 1)) ^ (((q4 >> 1) & 1) << 2)) * 16) +
                    (p4 & 1) * 8;
#pragma unroll
  for (int st = 0; st < 4; ++st) {
    if (st == 0) wait_vm<24>();
    else if (st == 1) wait_vm<16>();
    else if (st == 2) wait_vm<8>();
    else wait_vm<0>();
    RAWBAR();
    const char* sb = smem + st * K3_STAGE;
    bf16x8 af[8], bfr[8];
#pragma unroll
    for (int ks = 0; ks < 8; ++ks) {
      af[ks] = *(const bf16x8*)(sb + a_off + (((2 * ks + hh) ^ a_sw) * 16));
      s16x4 lo = tr_read(sb + b_off + (16 * ks) * 128);
      s16x4 hi = tr_read(sb + b_off + (16 * ks + 4) * 128);
      bfr[ks] = cat_frag(lo, hi);
    }
    __builtin_amdgcn_sched_barrier(0);
#pragma unroll
    for (int ks = 0; ks < 8; ++ks) acc = MFMA32(af[ks], bfr[ks], acc);
    __builtin_amdgcn_sched_barrier(0);
  }
  __syncthreads();
  STAMP(2, 5);
  float* Cs = (float*)smem;
  char* Ys = smem + 64 * K3_CSTR * 4;
#pragma unroll
  for (int e = 0; e < 16; ++e) {
    const int row = wm * 32 + (e & 3) + 8 * (e >> 2) + 4 * hh;
    Cs[row * K3_CSTR + wn * 32 + r] = acc[e];
  }
#pragma unroll
  for (int i = 0; i < 2; ++i) {
    const int idx = t + 256 * i;
    const int dr = idx >> 3, ch = idx & 7;
    u32x2 a = {yv[i][0], yv[i][1]}, b = {yv[i][2], yv[i][3]};
    *(u32x2*)(Ys + dr * K3_YSTR + ch * 16) = a;
    *(u32x2*)(Ys + dr * K3_YSTR + ch * 16 + 8) = b;
  }
  __syncthreads();
#pragma unroll
  for (int i = 0; i < 2; ++i) {
    const int idx = t + 256 * i;
    const int row = idx >> 3, ch = idx & 7;
    const int s = m0 + row;
    const float g = gv[i];
    const float omg = rbf(1.f - g);
    const f32x4 c0 = *(const f32x4*)(Cs + row * K3_CSTR + ch * 8);
    const f32x4 c1 = *(const f32x4*)(Cs + row * K3_CSTR + ch * 8 + 4);
    float bb[8] = {c0[0], c0[1], c0[2], c0[3], c1[0], c1[1], c1[2], c1[3]};
    float res[8];
#pragma unroll
    for (int j = 0; j < 8; ++j) {
      const float y2 = bf2f(*(const u16*)(Ys + (ch * 8 + j) * K3_YSTR + row * 2));
      const float qv = (j & 1) ? bfhi(xq[i][j >> 1]) : bflo(xq[i][j >> 1]);
      const float a = rbf(qv * y2);
      const float ga = rbf(g * a);
      const float gb = rbf(omg * rbf(bb[j]));
      res[j] = ga + gb;
    }
    u32x4 ov;
    ov[0] = pack2(res[0], res[1]);
    ov[1] = pack2(res[2], res[3]);
    ov[2] = pack2(res[4], res[5]);
    ov[3] = pack2(res[6], res[7]);
    *(u32x4*)(out + (size_t)s * DM + n0 + ch * 8) = ov;
  }
  STAMP(2, 6);
}

extern "C" void kernel_launch(void* const* d_in, const int* in_sizes, int n_in, void* d_out, int out_size,
                              void* d_ws, size_t ws_size, hipStream_t stream) {
  const u16* A_q = (const u16*)d_in[0];
  const u16* A_k = (const u16*)d_in[1];
  const u16* A_v = (const u16*)d_in[2];
  const u16* A_shift = (const u16*)d_in[3];
  const u16* A_ssmk = (const u16*)d_in[4];
  const u16* A_ssmD = (const u16*)d_in[5];
  const u16* B_wqkv = (const u16*)d_in[7];
  const u16* B_wo = (const u16*)d_in[8];
  const u16* gate_w = (const u16*)d_in[9];
  char* ws = (char*)d_ws;
  k1_kernel<<<256, 512, 0, stream>>>(A_q, A_k, A_v, A_shift, B_wqkv, gate_w, ws);
  k2_kernel<<<512, 512, 0, stream>>>(A_ssmk, A_ssmD, ws);
  k3_kernel<<<256, 256, 0, stream>>>(A_q, B_wo, ws, (u16*)d_out);
}
```

```cpp
#include <hip/hip_runtime.h>
#include <hip/hip_bf16.h>
#include <stdint.h>

#define DI __device__ __forceinline__
typedef __attribute__((ext_vector_type(8))) __bf16 bf16x8;
typedef __attribute__((ext_vector_type(4))) __bf16 bf16x4;
typedef __attribute__((ext_vector_type(2))) __bf16 bf16x2;
typedef __attribute__((ext_vector_type(4))) short s16x4;
typedef __attribute__((ext_vector_type(16))) float f32x16;
typedef __attribute__((ext_vector_type(4))) float f32x4;
typedef __attribute__((ext_vector_type(2))) float f32x2;
typedef __attribute__((ext_vector_type(4))) unsigned u32x4;
typedef __attribute__((ext_vector_type(2))) unsigned u32x2;
typedef unsigned short u16;

#define SEQ 2048
#define DM 512

#define MFMA32(a, b, c) __builtin_amdgcn_mfma_f32_32x32x16_bf16((a), (b), (c), 0, 0, 0)

DI float bf2f(u16 v) { return __uint_as_float(((unsigned)v) << 16); }
DI float bflo(unsigned v) { return __uint_as_float(v << 16); }
DI float bfhi(unsigned v) { return __uint_as_float(v & 0xffff0000u); }
DI unsigned pack2(float a, float b) {
  f32x2 f = {a, b};
  bf16x2 r = __builtin_convertvector(f, bf16x2);
  return __builtin_bit_cast(unsigned, r);
}
DI float rbf(float a) {
  return (float)((__bf16)a);
}
DI s16x4 tr_read(const char* p) {
  typedef __attribute__((address_space(3))) s16x4* lp;
  return __builtin_amdgcn_ds_read_tr16_b64_v4i16((lp)(p));
}
DI bf16x8 cat_frag(s16x4 lo, s16x4 hi) {
  typedef __attribute__((ext_vector_type(8))) short s16x8;
  s16x8 v = __builtin_shufflevector(lo, hi, 0, 1, 2, 3, 4, 5, 6, 7);
  return __builtin_bit_cast(bf16x8, v);
}

#define STAMP(k, i) do {} while (0)
#define WS_Q 0
#define WS_K (2u << 20)
#define WS_VT (4u << 20)
#define WS_KVT (6u << 20)
#define WS_Y2T (8u << 20)
#define WS_O (10u << 20)
#define WS_G (12u << 20)

#define WT_AUX 0
typedef __amdgpu_buffer_rsrc_t rsrc_t;
DI rsrc_t ws_rsrc(const char* ws) { return __builtin_amdgcn_make_buffer_rsrc((void*)ws, (short)0, 16 << 20, 0x00020000); }
DI void wst16(rsrc_t rs, unsigned off, u32x4 v) { __builtin_amdgcn_raw_buffer_store_b128(v, rs, (int)off, 0, WT_AUX); }
DI void wst8(rsrc_t rs, unsigned off, u32x2 v) { __builtin_amdgcn_raw_buffer_store_b64(v, rs, (int)off, 0, WT_AUX); }

#define GLDS16(g, l) __builtin_amdgcn_global_load_lds((const unsigned*)(g), (unsigned*)(l), 16, 0, 0)
#define WAIT_V(n) asm volatile("s_waitcnt vmcnt(" #n ")" ::: "memory")
#define RAWBAR() do { asm volatile("s_waitcnt lgkmcnt(0)" ::: "memory"); __builtin_amdgcn_s_barrier(); } while (0)
#define K1_STAGE 32768

template <int N>
DI void wait_vm() {
  if constexpr (N == 0) WAIT_V(0);
  else if constexpr (N == 4) WAIT_V(4);
  else if constexpr (N == 8) WAIT_V(8);
  else if constexpr (N == 12) WAIT_V(12);
  else if constexpr (N == 16) WAIT_V(16);
  else if constexpr (N == 24) WAIT_V(24);
}

#define QSCALE (0.25f * 1.44269504088896340736f)

template <int MODE>
DI void k1_gemm(const u16* __restrict__ x, const u16* __restrict__ w, char* __restrict__ ws, char* smem, int tm,
                int tn) {
  const int t = threadIdx.x;
  const int l = t & 63, wv = t >> 6;
  const int r = l & 31, hh = l >> 5;
  const int wm = wv & 1, wn = wv >> 1;
  const int m0 = tm * 128, n0 = tn * 128;

  const u16* xg[2];
  const u16* wg[2];
#pragma unroll
  for (int i = 0; i < 2; ++i) {
    const int ar = 16 * wv + 8 * i + (l >> 3);
    xg[i] = x + (size_t)(m0 + ar) * DM + (((l & 7) ^ ((ar >> 1) & 7)) * 8);
    const int br = 8 * wv + 4 * i + (l >> 4);
    wg[i] = w + (size_t)br * 1536 + n0 + (((l & 15) ^ ((br & 3) << 2)) * 8);
  }
  char* ldsw = smem + wv * 2048 + l * 16;

  const int rot = 0;
  auto issue = [&](int kt) {
    char* st = ldsw + (kt & 3) * K1_STAGE;
    const int kk = (kt + rot) & 7;
#pragma unroll
    for (int i = 0; i < 2; ++i) GLDS16(xg[i] + kk * 64, st + i * 1024);
#pragma unroll
    for (int i = 0; i < 2; ++i) GLDS16(wg[i] + (size_t)kk * 64 * 1536, st + 16384 + i * 1024);
  };

  f32x16 acc[2];
#pragma unroll
  for (int i = 0; i < 2; ++i)
#pragma unroll
    for (int e = 0; e < 16; ++e) acc[i][e] = 0.f;

  const int blk = (l >> 4) & 1, q4 = (l & 15) >> 2, p4 = l & 3;
  const int a_row_off = (wm * 64 + r) * 128;
  const int a_sw = (r >> 1) & 7;
  const int b_base = 16384 + (8 * hh + q4) * 256 + (p4 & 1) * 8 + (((wn * 4 + 2 * blk + (p4 >> 1)) ^ (q4 << 2)) * 16);

  STAMP(0, 0);
  issue(0);
  issue(1);
  issue(2);
  issue(3);
  bf16x8 fa[2][4][2], fb[2][4];
  auto ldfr = [&](int kt, bf16x8(&a)[4][2], bf16x8(&b)[4]) {
    const char* st = smem + (kt & 3) * K1_STAGE;
#pragma unroll
    for (int ks = 0; ks < 4; ++ks) {
#pragma unroll
      for (int i = 0; i < 2; ++i)
        a[ks][i] = *(const bf16x8*)(st + a_row_off + i * 32 * 128 + (((2 * ks + hh) ^ a_sw) * 16));
      s16x4 lo = tr_read(st + b_base + (16 * ks) * 256);
      s16x4 hi = tr_read(st + b_base + (16 * ks + 4) * 256);
      b[ks] = cat_frag(lo, hi);
    }
  };
  wait_vm<12>();
  RAWBAR();
  ldfr(0, fa[0], fb[0]);
#pragma unroll
  for (int kt = 0; kt < 8; ++kt) {
    if (kt < 7) {
      if (kt <= 4) wait_vm<8>();
      else if (kt == 5) wait_vm<4>();
      else wait_vm<0>();
      RAWBAR();
      ldfr(kt + 1, fa[(kt + 1) & 1], fb[(kt + 1) & 1]);
    }
    __builtin_amdgcn_sched_barrier(0);
#pragma unroll
    for (int ks = 0; ks < 4; ++ks) {
#pragma unroll
      for (int i = 0; i < 2; ++i) {
        if (MODE == 2)
          acc[i] = MFMA32(fa[kt & 1][ks][i], fb[kt & 1][ks], acc[i]);
        else
          acc[i] = MFMA32(fb[kt & 1][ks], fa[kt & 1][ks][i], acc[i]);
      }
      __builtin_amdgcn_sched_barrier(0);
      if (kt + 4 <= 7) {
        char* st4 = ldsw + (kt & 3) * K1_STAGE;
        const int kk = (kt + 4 + rot) & 7;
        if (ks < 2)
          GLDS16(xg[ks] + kk * 64, st4 + ks * 1024);
        else
          GLDS16(wg[ks - 2] + (size_t)kk * 64 * 1536, st4 + 16384 + (ks - 2) * 1024);
      }
      __builtin_amdgcn_sched_barrier(0);
    }
  }

  STAMP(0, 9);
  const rsrc_t rs = ws_rsrc(ws);
  if (MODE == 2) {
#pragma unroll
    for (int i = 0; i < 2; ++i) {
      const int n = (n0 - 1024) + wn * 32 + r;
      const int tok0 = m0 + wm * 64 + i * 32;
      const unsigned dst = WS_VT + (unsigned)(n * SEQ + tok0 + 8 * hh) * 2;
#pragma unroll
      for (int bb = 0; bb < 2; ++bb) {
        u32x4 v;
        v[0] = pack2(acc[i][8 * bb + 0], acc[i][8 * bb + 1]);
        v[1] = pack2(acc[i][8 * bb + 2], acc[i][8 * bb + 3]);
        v[2] = pack2(acc[i][8 * bb + 4], acc[i][8 * bb + 5]);
        v[3] = pack2(acc[i][8 * bb + 6], acc[i][8 * bb + 7]);
        wst16(rs, dst + 32 * bb, v);
      }
    }
  } else {
    const unsigned base = (MODE == 1 ? WS_K : WS_Q);
    const int nn0 = n0 & 511;
#pragma unroll
    for (int i = 0; i < 2; ++i) {
      const int tok = m0 + wm * 64 + i * 32 + r;
      const int nb = nn0 + wn * 32;
      const int head = nb >> 4;
#pragma unroll
      for (int g = 0; g < 4; ++g) {
        u32x2 v;
        const float sc = (MODE == 0) ? QSCALE : 1.f;
        v[0] = pack2(acc[i][4 * g + 0] * sc, acc[i][4 * g + 1] * sc);
        v[1] = pack2(acc[i][4 * g + 2] * sc, acc[i][4 * g + 3] * sc);
        wst8(rs, base + (unsigned)(((head + (g >> 1)) * SEQ + tok) * 16 + 8 * (g & 1) + 4 * hh) * 2, v);
      }
    }
  }
  STAMP(0, 10);
}

__global__ __launch_bounds__(512) void k1_kernel(const u16* __restrict__ x, const u16* __restrict__ kin,
                                                 const u16* __restrict__ vin, const u16* __restrict__ shiftk,
                                                 const u16* __restrict__ wqkv, const u16* __restrict__ gatew,
                                                 const u16* __restrict__ ssmk, char* __restrict__ ws) {
  __shared__ __attribute__((aligned(16))) char smem[4 * K1_STAGE];
  const int xcd = blockIdx.x & 7, bidx = blockIdx.x >> 3;
  if (bidx < 24) {
    const int tm = 2 * xcd + (bidx & 1), tn = bidx >> 1;
    if (tn >= 8)
      k1_gemm<2>(x, wqkv, ws, smem, tm, tn);
    else if (tn >= 4)
      k1_gemm<1>(x, wqkv, ws, smem, tm, tn);
    else
      k1_gemm<0>(x, wqkv, ws, smem, tm, tn);
    return;
  }
  const int l = threadIdx.x & 63, wv = threadIdx.x >> 6;
  const int t = threadIdx.x & 255, h2 = threadIdx.x >> 8;
  const int pb = xcd * 8 + (bidx - 24);
  STAMP(0, 0);
  u32x4 pf[4];
#pragma unroll
  for (int i = 0; i < 4; ++i) pf[i] = *(const u32x4*)(ssmk + (size_t)pb * 16384 + (i * 512 + threadIdx.x) * 8);
  const int wid = pb * 8 + wv;
  const u32x4 gw = *(const u32x4*)(gatew + l * 8);
  u32x4 xv[4];
#pragma unroll
  for (int it = 0; it < 4; ++it) xv[it] = *(const u32x4*)(x + (size_t)(wid * 4 + it) * DM + l * 8);
  {
    const int ch = t & 15, rg = t >> 4;
    u32x4 kr[7], vr[4], sk[4];
    const int tile = pb * 2 + h2;
    const int s0 = (tile >> 2) * 64, d0 = (tile & 3) * 128;
    {
      const int sb = s0 + rg * 4;
      const int dd = d0 + ch * 8;
#pragma unroll
      for (int i = 0; i < 7; ++i) {
        const int s = sb - 3 + i;
        u32x4 z = {0u, 0u, 0u, 0u};
        kr[i] = (s >= 0) ? *(const u32x4*)(kin + (size_t)s * DM + dd) : z;
      }
#pragma unroll
      for (int i = 0; i < 4; ++i) vr[i] = *(const u32x4*)(vin + (size_t)(sb + i) * DM + dd);
#pragma unroll
      for (int i = 0; i < 4; ++i) sk[i] = *(const u32x4*)(shiftk + (size_t)dd * 4 + i * 8);
    }
    char* sm = smem + h2 * 17408;
#pragma unroll
    for (int c = 0; c < 8; ++c) {
      const unsigned s01 = sk[c >> 1][(c & 1) * 2], s23 = sk[c >> 1][(c & 1) * 2 + 1];
      const float sk0 = bflo(s01), sk1 = bfhi(s01), sk2 = bflo(s23), sk3 = bfhi(s23);
      float kk[7];
#pragma unroll
      for (int i = 0; i < 7; ++i) kk[i] = (c & 1) ? bfhi(kr[i][c >> 1]) : bflo(kr[i][c >> 1]);
      unsigned o[2];
#pragma unroll
      for (int i = 0; i < 4; i += 2) {
        const unsigned p3 = pack2(kk[i] * sk3, kk[i + 1] * sk3);
        const unsigned p2 = pack2(kk[i + 1] * sk2, kk[i + 2] * sk2);
        const unsigned p1 = pack2(kk[i + 2] * sk1, kk[i + 3] * sk1);
        const unsigned p0 = pack2(kk[i + 3] * sk0, kk[i + 4] * sk0);
        unsigned a = pack2(bflo(p3) + bflo(p2), bfhi(p3) + bfhi(p2));
        a = pack2(bflo(a) + bflo(p1), bfhi(a) + bfhi(p1));
        a = pack2(bflo(a) + bflo(p0), bfhi(a) + bfhi(p0));
        const float v0 = (c & 1) ? bfhi(vr[i][c >> 1]) : bflo(vr[i][c >> 1]);
        const float v1 = (c & 1) ? bfhi(vr[i + 1][c >> 1]) : bflo(vr[i + 1][c >> 1]);
        o[i >> 1] = pack2(bflo(a) * v0, bfhi(a) * v1);
      }
      *(u32x2*)(sm + (ch * 8 + c) * 136 + rg * 8) = (u32x2){o[0], o[1]};
    }
    __syncthreads();
    const rsrc_t rs = ws_rsrc(ws);
#pragma unroll
    for (int i = 0; i < 4; ++i) {
      const int idx = t + 256 * i;
      const int dr = idx >> 3, c8 = idx & 7;
      const u32x2 a = *(const u32x2*)(sm + dr * 136 + c8 * 16);
      const u32x2 b = *(const u32x2*)(sm + dr * 136 + c8 * 16 + 8);
      u32x4 v = {a[0], a[1], b[0], b[1]};
      wst16(rs, WS_KVT + (unsigned)((d0 + dr) * SEQ + s0 + c8 * 8) * 2, v);
    }
  }
  STAMP(0, 11);
  {
    float* g = (float*)(ws + WS_G);
    float sums[4];
#pragma unroll
    for (int it = 0; it < 4; ++it) {
      float sum = 0.f;
#pragma unroll
      for (int i = 0; i < 4; ++i) {
        const unsigned pr = pack2(bflo(xv[it][i]) * bflo(gw[i]), bfhi(xv[it][i]) * bfhi(gw[i]));
        sum += bflo(pr);
        sum += bfhi(pr);
      }
      sums[it] = sum;
    }
#pragma unroll
    for (int o = 32; o >= 1; o >>= 1)
#pragma unroll
      for (int it = 0; it < 4; ++it) sums[it] += __shfl_xor(sums[it], o);
    if (l < 4) {
      float sv = sums[0];
#pragma unroll
      for (int it = 1; it < 4; ++it) sv = (l == it) ? sums[it] : sv;
      const float z = rbf(sv);
      g[wid * 4 + l] = rbf(1.f / (1.f + __expf(-z)));
    }
  }
#pragma unroll
  for (int i = 0; i < 4; ++i) asm volatile("" ::"v"(pf[i]));
  STAMP(0, 12);
}

#define CV_KVSTR 80
#define CV_HSTR 4160
#define CV_HBYTES (8 * CV_HSTR)
#define CV_REDSTR 36
#define CV_SMEM 40960

template <bool FIRST>
DI void attn_block(const char* __restrict__ ws, char* smem, int head, int qb) {
  const int l = threadIdx.x & 63, wv = threadIdx.x >> 6;
  const int r = l & 31, hh = l >> 5;
  const u16* Qh = (const u16*)(ws + WS_Q) + (size_t)head * SEQ * 16;
  const u16* Kh = (const u16*)(ws + WS_K) + (size_t)head * SEQ * 16;
  const u16* Vh = (const u16*)(ws + WS_VT) + (size_t)head * 16 * SEQ;
  u16* O = (u16*)(ws + WS_O);
  const int q0 = qb + 32 * wv;
  const int kt0 = FIRST ? (8 - wv) : 0;
  STAMP(1, 0);
  char* ks_ = smem;
  char* vs_ = smem + 16384;
  char* cs_ = smem + 32768;
  {
    const int key = l >> 1;
    const u16* ksrc = Kh + (size_t)key * 16 + (((l & 1) ^ ((key >> 3) & 1)) * 8);
    const int dd = l >> 2;
    const u16* vsrc = Vh + (size_t)dd * SEQ + (((l & 3) ^ ((dd >> 2) & 3)) * 8);
#pragma unroll
    for (int i = 0; i < 2; ++i) {
      const int u = wv + 8 * i;
      int key0 = qb - 256 + 32 * u;
      key0 = key0 < 0 ? 0 : key0;
      GLDS16(ksrc + (size_t)key0 * 16, ks_ + u * 1024 + l * 16);
      GLDS16(vsrc + key0, vs_ + u * 1024 + l * 16);
    }
  }
  const bf16x8 qf = *(const bf16x8*)(Qh + (size_t)(q0 + r) * 16 + 8 * hh);
  if (threadIdx.x < 2) {
    const unsigned o = threadIdx.x == 0 ? 0x3f803f80u : 0u;
    *(u32x4*)(cs_ + threadIdx.x * 16) = (u32x4){o, o, o, o};
  }
  WAIT_V(0);
  RAWBAR();
  STAMP(1, 1);
  const char* krd = ks_ + (32 * wv + r) * 32 + ((hh ^ ((r >> 3) & 1)) * 16);
  f32x16 zero;
#pragma unroll
  for (int e = 0; e < 16; ++e) zero[e] = 0.f;
  const float NEG = -3.0e38f;
  auto mask = [&](int kt, f32x16& s) {
    if (kt == 0) {
#pragma unroll
      for (int e = 0; e < 16; ++e) s[e] = ((e & 3) + 8 * (e >> 2) + 4 * hh > r) ? s[e] : NEG;
    }
    if (kt == 8) {
#pragma unroll
      for (int e = 0; e < 16; ++e) s[e] = ((e & 3) + 8 * (e >> 2) + 4 * hh <= r) ? s[e] : NEG;
    }
  };
  float mx = NEG;
  {
    bf16x8 kf[9];
#pragma unroll
    for (int kt = 0; kt < 9; ++kt) kf[kt] = *(const bf16x8*)(krd + kt * 1024);
    f32x16 s[2];
    s[0] = MFMA32(kf[0], qf, zero);
#pragma unroll
    for (int kt = 0; kt < 9; ++kt) {
      if (kt < 8) s[(kt + 1) & 1] = MFMA32(kf[kt + 1], qf, zero);
      f32x16& c = s[kt & 1];
      mask(kt, c);
      float m = NEG;
#pragma unroll
      for (int e = 0; e < 16; e += 2) m = fmaxf(fmaxf(c[e], c[e + 1]), m);
      if (FIRST) m = (kt >= kt0) ? m : NEG;
      mx = fmaxf(mx, m);
    }
  }
  mx = fmaxf(mx, __shfl_xor(mx, 32));
  f32x16 negm;
#pragma unroll
  for (int e = 0; e < 16; ++e) negm[e] = -mx;
  STAMP(1, 2);
  const int vsw = (r >> 2) & 3;
  const bool isd = r < 16;
  const int cad = 32768 + ((r == 16 || r == 20) ? 0 : 16);
  int va0 = isd ? 16384 + wv * 1024 + r * 64 + ((hh ^ vsw) * 16) : cad;
  int va1 = isd ? 16384 + wv * 1024 + r * 64 + (((2 + hh) ^ vsw) * 16) : cad;
  const int vstep = isd ? 1024 : 0;
  f32x16 oacc = zero;
  {
    f32x16 s[2];
    bf16x8 kf = *(const bf16x8*)(krd);
    s[0] = MFMA32(kf, qf, negm);
#pragma unroll
    for (int kt = 0; kt < 9; ++kt) {
      const bf16x8 vf0 = *(const bf16x8*)(smem + va0);
      const bf16x8 vf1 = *(const bf16x8*)(smem + va1);
      va0 += vstep;
      va1 += vstep;
      if (kt < 8) {
        kf = *(const bf16x8*)(krd + (kt + 1) * 1024);
        s[(kt + 1) & 1] = MFMA32(kf, qf, negm);
      }
      f32x16& c = s[kt & 1];
      mask(kt, c);
      if (!FIRST || kt >= kt0) {
        u32x4 pp[2];
#pragma unroll
        for (int e = 0; e < 16; e += 2) {
          pp[e >> 3][(e >> 1) & 3] = pack2(__builtin_amdgcn_exp2f(c[e]), __builtin_amdgcn_exp2f(c[e + 1]));
        }
        oacc = MFMA32(vf0, __builtin_bit_cast(bf16x8, pp[0]), oacc);
        oacc = MFMA32(vf1, __builtin_bit_cast(bf16x8, pp[1]), oacc);
      }
    }
  }
  STAMP(1, 3);
  const float inv = 1.f / oacc[8];
  u32x2 v0 = {pack2(oacc[0] * inv, oacc[1] * inv), pack2(oacc[2] * inv, oacc[3] * inv)};
  u32x2 v1 = {pack2(oacc[4] * inv, oacc[5] * inv), pack2(oacc[6] * inv, oacc[7] * inv)};
  const rsrc_t rs = ws_rsrc(ws);
  const unsigned op = WS_O + (unsigned)((q0 + r) * DM + head * 16 + 4 * hh) * 2;
  wst8(rs, op, v0);
  wst8(rs, op + 16, v1);
  STAMP(1, 4);
}

DI void conv_block(const u16* __restrict__ ssmk, const u16* __restrict__ ssmD, char* __restrict__ ws, char* smem,
                   int d) {
  const int t = threadIdx.x & 255;
  const int l = t & 63, wv = t >> 6;
  const int r = l & 31, hh = l >> 5;
  char* hs = smem;
  char* kvs = smem + CV_HBYTES;
  const u16* kvT = (const u16*)(ws + WS_KVT) + (size_t)d * SEQ;
  const u16* hrow = ssmk + (size_t)d * SEQ;
  STAMP(1, 0);
  const u32x4 kvv = *(const u32x4*)(kvT + 8 * t);
  const u32x4 whi = *(const u32x4*)(hrow + 8 * t);
  const u32x4 wlo0 = *(const u32x4*)(hrow + 8 * (t > 0 ? t - 1 : 0));
  const float Dd = bf2f(ssmD[d]);
  *(u32x4*)(kvs + ((t >> 2) + 32) * CV_KVSTR + (t & 3) * 16) = kvv;
  if (t < 128) {
    u32x4 z = {0u, 0u, 0u, 0u};
    *(u32x4*)(kvs + (t >> 2) * CV_KVSTR + (t & 3) * 16) = z;
  }
  {
    const int E = t;
    u32x4 wlo = wlo0;
    if (E == 0) wlo = (u32x4){0u, 0u, 0u, 0u};
    unsigned z[8];
#pragma unroll
    for (int i = 0; i < 4; ++i) {
      const unsigned a = whi[3 - i];
      z[i] = (a >> 16) | (a << 16);
      const unsigned b = wlo[3 - i];
      z[4 + i] = (b >> 16) | (b << 16);
    }
    const int M = 255 - E;
#pragma unroll
    for (int p = 0; p < 8; ++p) {
      const int o = 7 - p;
      u32x4 v;
      if ((o & 1) == 0) {
        v[0] = z[o / 2];
        v[1] = z[o / 2 + 1];
        v[2] = z[o / 2 + 2];
        v[3] = z[o / 2 + 3];
      } else {
        v[0] = (z[o / 2] >> 16) | (z[o / 2 + 1] << 16);
        v[1] = (z[o / 2 + 1] >> 16) | (z[o / 2 + 2] << 16);
        v[2] = (z[o / 2 + 2] >> 16) | (z[o / 2 + 3] << 16);
        v[3] = (z[o / 2 + 3] >> 16) | (z[o / 2 + 4] << 16);
      }
      *(u32x4*)(hs + p * CV_HSTR + 16 * M) = v;
    }
    if (t < 24) {
      u32x4 zz = {0u, 0u, 0u, 0u};
      *(u32x4*)(hs + (t & 7) * CV_HSTR + 16 * (256 + (t >> 3))) = zz;
    }
  }
  __syncthreads();
  STAMP(1, 1);
  f32x16 acc0, acc1, acc2;
#pragma unroll
  for (int e = 0; e < 16; ++e) {
    acc0[e] = 0.f;
    acc1[e] = 0.f;
    acc2[e] = 0.f;
  }
  const char* abase = hs + (r & 7) * CV_HSTR + 16 * (255 - (r >> 3) + hh) - 64 * wv;
  const char* bbase = kvs + (r + 32 - wv) * CV_KVSTR + 16 * hh;
  {
    bf16x8 fa[2][2], fb[2][2];
    auto ldc = [&](int step, bf16x8(&a)[2], bf16x8(&b)[2]) {
      const int i = step >> 1, ks = step & 1;
      a[0] = *(const bf16x8*)(abase - 256 * i + 32 * ks);
      a[1] = *(const bf16x8*)(abase - 256 * i - 2048 + 32 * ks);
      b[1] = *(const bf16x8*)(bbase + (32 - 4 * i) * CV_KVSTR + 32 * ks);
      b[0] = *(const bf16x8*)(bbase - (4 * i) * CV_KVSTR + 32 * ks);
    };
    ldc(0, fa[0], fb[0]);
#pragma unroll
    for (int step = 0; step < 16; ++step) {
      if (step < 15) ldc(step + 1, fa[(step + 1) & 1], fb[(step + 1) & 1]);
      __builtin_amdgcn_sched_barrier(0);
      acc1 = MFMA32(fa[step & 1][0], fb[step & 1][1], acc1);
      acc0 = MFMA32(fa[step & 1][0], fb[step & 1][0], acc0);
      acc2 = MFMA32(fa[step & 1][1], fb[step & 1][0], acc2);
      __builtin_amdgcn_sched_barrier(0);
    }
  }
  acc1 += acc2;
  STAMP(1, 2);
  __syncthreads();
  STAMP(1, 6);
  float* red = (float*)smem;
  {
    float* rw = red + wv * (64 * CV_REDSTR);
#pragma unroll
    for (int g = 0; g < 4; ++g) {
      f32x4 v0 = {acc0[4 * g], acc0[4 * g + 1], acc0[4 * g + 2], acc0[4 * g + 3]};
      f32x4 v1 = {acc1[4 * g], acc1[4 * g + 1], acc1[4 * g + 2], acc1[4 * g + 3]};
      *(f32x4*)(rw + r * CV_REDSTR + 8 * g + 4 * hh) = v0;
      *(f32x4*)(rw + (32 + r) * CV_REDSTR + 8 * g + 4 * hh) = v1;
    }
  }
  __syncthreads();
  STAMP(1, 7);
  {
    const int a = t >> 2, b0 = 8 * (t & 3);
    f32x4 s0 = {0.f, 0.f, 0.f, 0.f}, s1 = {0.f, 0.f, 0.f, 0.f};
#pragma unroll
    for (int w = 0; w < 4; ++w) {
      const float* rp = red + w * (64 * CV_REDSTR) + a * CV_REDSTR + b0;
      s0 += *(const f32x4*)(rp);
      s1 += *(const f32x4*)(rp + 4);
    }
    float yy[8] = {s0[0], s0[1], s0[2], s0[3], s1[0], s1[1], s1[2], s1[3]};
    u32x4 o;
#pragma unroll
    for (int i = 0; i < 4; ++i) {
      const float y0 = rbf(yy[2 * i]) + rbf(bflo(kvv[i]) * Dd);
      const float y1 = rbf(yy[2 * i + 1]) + rbf(bfhi(kvv[i]) * Dd);
      o[i] = pack2(y0, y1);
    }
    wst16(ws_rsrc(ws), WS_Y2T + (unsigned)(d * SEQ + 8 * t) * 2, o);
  }
  STAMP(1, 4);
}

__global__ __launch_bounds__(512, 4) void k2_kernel(const u16* __restrict__ ssmk, const u16* __restrict__ ssmD,
                                                    const u16* __restrict__ wo, char* __restrict__ ws) {
  __shared__ __attribute__((aligned(16))) char smem[2 * CV_SMEM];
  const int xcd = blockIdx.x & 7, bidx = (blockIdx.x >> 3) ^ 32;
  if (bidx >= 32) {
    const u32x4 pf0 = *(const u32x4*)(wo + (size_t)(bidx - 32) * 8192 + threadIdx.x * 8);
    const u32x4 pf1 = *(const u32x4*)(wo + (size_t)(bidx - 32) * 8192 + 4096 + threadIdx.x * 8);
    const int half = threadIdx.x >> 8;
    conv_block(ssmk, ssmD, ws, smem + half * CV_SMEM, xcd * 64 + (bidx - 32) * 2 + half);
    asm volatile("" ::"v"(pf0));
    asm volatile("" ::"v"(pf1));
  } else {
    if (xcd == 0)
      attn_block<true>(ws, smem, bidx, 0);
    else
      attn_block<false>(ws, smem, bidx, xcd * 256);
  }
}

#define K3_STAGE 32768
#define K3_CSTR 68
#define K3_YSTR 136

DI void k3_body(const u16* __restrict__ x, const u16* __restrict__ wo, const char* __restrict__ ws,
                u16* __restrict__ out, char* smem) {
  const int t = threadIdx.x;
  const int l = t & 63, wv = t >> 6;
  const int r = l & 31, hh = l >> 5;
  const int wm = wv & 1, wn = (wv >> 1) & 1, kh = wv >> 2;
  const int tm = (blockIdx.x & 7) * 4 + ((blockIdx.x >> 3) & 3), tn = blockIdx.x >> 5;
  const int m0 = tm * 64, n0 = tn * 64;
  const u16* o = (const u16*)(ws + WS_O);
  STAMP(2, 0);

  const u16* y2T = (const u16*)(ws + WS_Y2T);
  const float* gp = (const float*)(ws + WS_G);
  const int rr = t >> 3, ch = t & 7;
  const u32x4 yv = *(const u32x4*)(y2T + (size_t)(n0 + rr) * SEQ + m0 + ch * 8);
  const u32x4 xq = *(const u32x4*)(x + (size_t)(m0 + rr) * DM + n0 + ch * 8);
  const float g = gp[m0 + rr];
  __builtin_amdgcn_sched_barrier(0);
  {
    char* ldsw = smem + wv * 2048 + l * 16;
#pragma unroll
    for (int s4 = 0; s4 < 4; ++s4) {
      const int st = s4;
#pragma unroll
      for (int i = 0; i < 2; ++i) {
        const int ar = 8 * wv + 4 * i + (l >> 4);
        GLDS16(o + (size_t)(m0 + ar) * DM + st * 128 + (((l & 15) ^ (ar & 15)) * 8), ldsw + st * K3_STAGE + i * 1024);
      }
#pragma unroll
      for (int i = 0; i < 2; ++i) {
        const int br = 16 * wv + 8 * i + (l >> 3);
        GLDS16(wo + (size_t)(st * 128 + br) * DM + n0 + (((l & 7) ^ (((br >> 1) & 1) << 2)) * 8),
               ldsw + st * K3_STAGE + 16384 + i * 1024);
      }
    }
  }

  f32x16 acc;
#pragma unroll
  for (int e = 0; e < 16; ++e) acc[e] = 0.f;
  const int blk = (l >> 4) & 1, q4 = (l & 15) >> 2, p4 = l & 3;
  const int arow = wm * 32 + r;
  const int a_off = arow * 256;
  const int a_sw = arow & 15;
  const int b_off = 16384 + (8 * hh + q4) * 128 + (((wn * 4 + 2 * blk + (p4 >> 1)) ^ (((q4 >> 1) & 1) << 2)) * 16) +
                    (p4 & 1) * 8;
  const int a_x = (8 * kh + hh) ^ a_sw;
  const int b_k = b_off + 64 * kh * 128;
#pragma unroll
  for (int st = 0; st < 4; ++st) {
    if (st == 0) WAIT_V(12);
    else if (st == 1) WAIT_V(8);
    else if (st == 2) WAIT_V(4);
    else WAIT_V(0);
    RAWBAR();
    const char* sb = smem + st * K3_STAGE;
    bf16x8 af[4], bfr[4];
#pragma unroll
    for (int ks = 0; ks < 4; ++ks) {
      af[ks] = *(const bf16x8*)(sb + a_off + ((a_x ^ (2 * ks)) * 16));
      s16x4 lo = tr_read(sb + b_k + (16 * ks) * 128);
      s16x4 hi = tr_read(sb + b_k + (16 * ks + 4) * 128);
      bfr[ks] = cat_frag(lo, hi);
    }
    __builtin_amdgcn_sched_barrier(0);
#pragma unroll
    for (int ks = 0; ks < 4; ++ks) acc = MFMA32(af[ks], bfr[ks], acc);
    __builtin_amdgcn_sched_barrier(0);
  }
  __syncthreads();
  STAMP(2, 5);
  float* Cs = (float*)smem;
  char* Ys = smem + 2 * 64 * K3_CSTR * 4;
#pragma unroll
  for (int e = 0; e < 16; ++e) {
    const int row = wm * 32 + (e & 3) + 8 * (e >> 2) + 4 * hh;
    Cs[kh * 64 * K3_CSTR + row * K3_CSTR + wn * 32 + r] = acc[e];
  }
  {
    u32x2 a = {yv[0], yv[1]}, b = {yv[2], yv[3]};
    *(u32x2*)(Ys + rr * K3_YSTR + ch * 16) = a;
    *(u32x2*)(Ys + rr * K3_YSTR + ch * 16 + 8) = b;
  }
  __syncthreads();
  {
    const int row = rr;
    const int s = m0 + row;
    const float omg = rbf(1.f - g);
    const f32x4 c0 = *(const f32x4*)(Cs + row * K3_CSTR + ch * 8) + *(const f32x4*)(Cs + (64 + row) * K3_CSTR + ch * 8);
    const f32x4 c1 =
        *(const f32x4*)(Cs + row * K3_CSTR + ch * 8 + 4) + *(const f32x4*)(Cs + (64 + row) * K3_CSTR + ch * 8 + 4);
    float bb[8] = {c0[0], c0[1], c0[2], c0[3], c1[0], c1[1], c1[2], c1[3]};
    float res[8];
#pragma unroll
    for (int j = 0; j < 8; ++j) {
      const float y2 = bf2f(*(const u16*)(Ys + (ch * 8 + j) * K3_YSTR + row * 2));
      const float qv = (j & 1) ? bfhi(xq[j >> 1]) : bflo(xq[j >> 1]);
      const float a = rbf(qv * y2);
      const float ga = rbf(g * a);
      const float gb = rbf(omg * rbf(bb[j]));
      res[j] = ga + gb;
    }
    u32x4 ov;
    ov[0] = pack2(res[0], res[1]);
    ov[1] = pack2(res[2], res[3]);
    ov[2] = pack2(res[4], res[5]);
    ov[3] = pack2(res[6], res[7]);
    *(u32x4*)(out + (size_t)s * DM + n0 + ch * 8) = ov;
  }
  STAMP(2, 6);
}

__global__ __launch_bounds__(512) void k3_kernel(const u16* __restrict__ x, const u16* __restrict__ wo,
                                                 const char* __restrict__ ws, u16* __restrict__ out) {
  __shared__ __attribute__((aligned(16))) char smem[4 * K3_STAGE];
  k3_body(x, wo, ws, out, smem);
}

extern "C" void kernel_launch(void* const* d_in, const int* in_sizes, int n_in, void* d_out, int out_size,
                              void* d_ws, size_t ws_size, hipStream_t stream) {
  const u16* A_q = (const u16*)d_in[0];
  const u16* A_k = (const u16*)d_in[1];
  const u16* A_v = (const u16*)d_in[2];
  const u16* A_shift = (const u16*)d_in[3];
  const u16* A_ssmk = (const u16*)d_in[4];
  const u16* A_ssmD = (const u16*)d_in[5];
  const u16* B_wqkv = (const u16*)d_in[7];
  const u16* B_wo = (const u16*)d_in[8];
  const u16* gate_w = (const u16*)d_in[9];
  char* ws = (char*)d_ws;
  k1_kernel<<<256, 512, 0, stream>>>(A_q, A_k, A_v, A_shift, B_wqkv, gate_w, A_ssmk, ws);
  k2_kernel<<<512, 512, 0, stream>>>(A_ssmk, A_ssmD, B_wo, ws);
  k3_kernel<<<256, 512, 0, stream>>>(A_q, B_wo, ws, (u16*)d_out);
}
```

```cpp
#include <hip/hip_runtime.h>
#include <hip/hip_bf16.h>
#include <stdint.h>

#define DI __device__ __forceinline__
typedef __attribute__((ext_vector_type(8))) __bf16 bf16x8;
typedef __attribute__((ext_vector_type(4))) __bf16 bf16x4;
typedef __attribute__((ext_vector_type(2))) __bf16 bf16x2;
typedef __attribute__((ext_vector_type(4))) short s16x4;
typedef __attribute__((ext_vector_type(16))) float f32x16;
typedef __attribute__((ext_vector_type(4))) float f32x4;
typedef __attribute__((ext_vector_type(2))) float f32x2;
typedef __attribute__((ext_vector_type(4))) unsigned u32x4;
typedef __attribute__((ext_vector_type(2))) unsigned u32x2;
typedef unsigned short u16;

#define SEQ 2048
#define DM 512

#define MFMA32(a, b, c) __builtin_amdgcn_mfma_f32_32x32x16_bf16((a), (b), (c), 0, 0, 0)

DI float bf2f(u16 v) { return __uint_as_float(((unsigned)v) << 16); }
DI float bflo(unsigned v) { return __uint_as_float(v << 16); }
DI float bfhi(unsigned v) { return __uint_as_float(v & 0xffff0000u); }
DI unsigned pack2(float a, float b) {
  f32x2 f = {a, b};
  bf16x2 r = __builtin_convertvector(f, bf16x2);
  return __builtin_bit_cast(unsigned, r);
}
DI float rbf(float a) {
  return (float)((__bf16)a);
}
DI s16x4 tr_read(const char* p) {
  typedef __attribute__((address_space(3))) s16x4* lp;
  return __builtin_amdgcn_ds_read_tr16_b64_v4i16((lp)(p));
}
DI bf16x8 cat_frag(s16x4 lo, s16x4 hi) {
  typedef __attribute__((ext_vector_type(8))) short s16x8;
  s16x8 v = __builtin_shufflevector(lo, hi, 0, 1, 2, 3, 4, 5, 6, 7);
  return __builtin_bit_cast(bf16x8, v);
}

#define STAMP(k, i) do {} while (0)
#define WS_Q 0
#define WS_K (2u << 20)
#define WS_VT (4u << 20)
#define WS_KVT (6u << 20)
#define WS_Y2T (8u << 20)
#define WS_O (10u << 20)
#define WS_G (12u << 20)

#define WT_AUX 0
typedef __amdgpu_buffer_rsrc_t rsrc_t;
DI rsrc_t ws_rsrc(const char* ws) { return __builtin_amdgcn_make_buffer_rsrc((void*)ws, (short)0, 16 << 20, 0x00020000); }
DI void wst16(rsrc_t rs, unsigned off, u32x4 v) { __builtin_amdgcn_raw_buffer_store_b128(v, rs, (int)off, 0, WT_AUX); }
DI void wst8(rsrc_t rs, unsigned off, u32x2 v) { __builtin_amdgcn_raw_buffer_store_b64(v, rs, (int)off, 0, WT_AUX); }

#define GLDS16(g, l) __builtin_amdgcn_global_load_lds((const unsigned*)(g), (unsigned*)(l), 16, 0, 0)
#define WAIT_V(n) asm volatile("s_waitcnt vmcnt(" #n ")" ::: "memory")
#define RAWBAR() do { asm volatile("s_waitcnt lgkmcnt(0)" ::: "memory"); __builtin_amdgcn_s_barrier(); } while (0)
#define K1_STAGE 32768

template <int N>
DI void wait_vm() {
  if constexpr (N == 0) WAIT_V(0);
  else if constexpr (N == 4) WAIT_V(4);
  else if constexpr (N == 8) WAIT_V(8);
  else if constexpr (N == 12) WAIT_V(12);
  else if constexpr (N == 16) WAIT_V(16);
  else if constexpr (N == 24) WAIT_V(24);
}

#define QSCALE (0.25f * 1.44269504088896340736f)

template <int MODE>
DI void k1_gemm(const u16* __restrict__ x, const u16* __restrict__ w, char* __restrict__ ws, char* smem, int tm,
                int tn) {
  const int t = threadIdx.x;
  const int l = t & 63, wv = t >> 6;
  const int r = l & 31, hh = l >> 5;
  const int wm = wv & 1, wn = wv >> 1;
  const int m0 = tm * 128, n0 = tn * 128;

  const u16* xg[2];
  const u16* wg[2];
#pragma unroll
  for (int i = 0; i < 2; ++i) {
    const int ar = 16 * wv + 8 * i + (l >> 3);
    xg[i] = x + (size_t)(m0 + ar) * DM + (((l & 7) ^ ((ar >> 1) & 7)) * 8);
    const int br = 8 * wv + 4 * i + (l >> 4);
    wg[i] = w + (size_t)br * 1536 + n0 + (((l & 15) ^ ((br & 3) << 2)) * 8);
  }
  char* ldsw = smem + wv * 2048 + l * 16;

  const int rot = 0;
  auto issue = [&](int kt) {
    char* st = ldsw + (kt & 3) * K1_STAGE;
    const int kk = (kt + rot) & 7;
#pragma unroll
    for (int i = 0; i < 2; ++i) GLDS16(xg[i] + kk * 64, st + i * 1024);
#pragma unroll
    for (int i = 0; i < 2; ++i) GLDS16(wg[i] + (size_t)kk * 64 * 1536, st + 16384 + i * 1024);
  };

  f32x16 acc[2];
#pragma unroll
  for (int i = 0; i < 2; ++i)
#pragma unroll
    for (int e = 0; e < 16; ++e) acc[i][e] = 0.f;

  const int blk = (l >> 4) & 1, q4 = (l & 15) >> 2, p4 = l & 3;
  const int a_row_off = (wm * 64 + r) * 128;
  const int a_sw = (r >> 1) & 7;
  const int b_base = 16384 + (8 * hh + q4) * 256 + (p4 & 1) * 8 + (((wn * 4 + 2 * blk + (p4 >> 1)) ^ (q4 << 2)) * 16);

  STAMP(0, 0);
  issue(0);
  issue(1);
  issue(2);
  issue(3);
  bf16x8 fa[2][4][2], fb[2][4];
  auto ldfr = [&](int kt, bf16x8(&a)[4][2], bf16x8(&b)[4]) {
    const char* st = smem + (kt & 3) * K1_STAGE;
#pragma unroll
    for (int ks = 0; ks < 4; ++ks) {
#pragma unroll
      for (int i = 0; i < 2; ++i)
        a[ks][i] = *(const bf16x8*)(st + a_row_off + i * 32 * 128 + (((2 * ks + hh) ^ a_sw) * 16));
      s16x4 lo = tr_read(st + b_base + (16 * ks) * 256);
      s16x4 hi = tr_read(st + b_base + (16 * ks + 4) * 256);
      b[ks] = cat_frag(lo, hi);
    }
  };
  wait_vm<12>();
  RAWBAR();
  ldfr(0, fa[0], fb[0]);
#pragma unroll
  for (int kt = 0; kt < 8; ++kt) {
    if (kt < 7) {
      if (kt <= 4) wait_vm<8>();
      else if (kt == 5) wait_vm<4>();
      else wait_vm<0>();
      RAWBAR();
      ldfr(kt + 1, fa[(kt + 1) & 1], fb[(kt + 1) & 1]);
    }
    __builtin_amdgcn_sched_barrier(0);
#pragma unroll
    for (int ks = 0; ks < 4; ++ks) {
#pragma unroll
      for (int i = 0; i < 2; ++i) {
        if (MODE == 2)
          acc[i] = MFMA32(fa[kt & 1][ks][i], fb[kt & 1][ks], acc[i]);
        else
          acc[i] = MFMA32(fb[kt & 1][ks], fa[kt & 1][ks][i], acc[i]);
      }
      __builtin_amdgcn_sched_barrier(0);
      if (kt + 4 <= 7) {
        char* st4 = ldsw + (kt & 3) * K1_STAGE;
        const int kk = (kt + 4 + rot) & 7;
        if (ks < 2)
          GLDS16(xg[ks] + kk * 64, st4 + ks * 1024);
        else
          GLDS16(wg[ks - 2] + (size_t)kk * 64 * 1536, st4 + 16384 + (ks - 2) * 1024);
      }
      __builtin_amdgcn_sched_barrier(0);
    }
  }

  STAMP(0, 9);
  const rsrc_t rs = ws_rsrc(ws);
  if (MODE == 2) {
#pragma unroll
    for (int i = 0; i < 2; ++i) {
      const int n = (n0 - 1024) + wn * 32 + r;
      const int tok0 = m0 + wm * 64 + i * 32;
      const unsigned dst = WS_VT + (unsigned)(n * SEQ + tok0 + 8 * hh) * 2;
#pragma unroll
      for (int bb = 0; bb < 2; ++bb) {
        u32x4 v;
        v[0] = pack2(acc[i][8 * bb + 0], acc[i][8 * bb + 1]);
        v[1] = pack2(acc[i][8 * bb + 2], acc[i][8 * bb + 3]);
        v[2] = pack2(acc[i][8 * bb + 4], acc[i][8 * bb + 5]);
        v[3] = pack2(acc[i][8 * bb + 6], acc[i][8 * bb + 7]);
        wst16(rs, dst + 32 * bb, v);
      }
    }
  } else {
    const unsigned base = (MODE == 1 ? WS_K : WS_Q);
    const int nn0 = n0 & 511;
#pragma unroll
    for (int i = 0; i < 2; ++i) {
      const int tok = m0 + wm * 64 + i * 32 + r;
      const int nb = nn0 + wn * 32;
      const int head = nb >> 4;
#pragma unroll
      for (int g = 0; g < 4; ++g) {
        u32x2 v;
        const float sc = (MODE == 0) ? QSCALE : 1.f;
        v[0] = pack2(acc[i][4 * g + 0] * sc, acc[i][4 * g + 1] * sc);
        v[1] = pack2(acc[i][4 * g + 2] * sc, acc[i][4 * g + 3] * sc);
        wst8(rs, base + (unsigned)(((head + (g >> 1)) * SEQ + tok) * 16 + 8 * (g & 1) + 4 * hh) * 2, v);
      }
    }
  }
  STAMP(0, 10);
}

__global__ __launch_bounds__(512) void k1_kernel(const u16* __restrict__ x, const u16* __restrict__ kin,
                                                 const u16* __restrict__ vin, const u16* __restrict__ shiftk,
                                                 const u16* __restrict__ wqkv, const u16* __restrict__ gatew,
                                                 const u16* __restrict__ ssmk, char* __restrict__ ws) {
  __shared__ __attribute__((aligned(16))) char smem[4 * K1_STAGE];
  const int xcd = blockIdx.x & 7, bidx = blockIdx.x >> 3;
  if (bidx < 24) {
    const int tm = 2 * xcd + (bidx & 1), tn = bidx >> 1;
    if (tn >= 8)
      k1_gemm<2>(x, wqkv, ws, smem, tm, tn);
    else if (tn >= 4)
      k1_gemm<1>(x, wqkv, ws, smem, tm, tn);
    else
      k1_gemm<0>(x, wqkv, ws, smem, tm, tn);
    return;
  }
  const int l = threadIdx.x & 63, wv = threadIdx.x >> 6;
  const int t = threadIdx.x & 255, h2 = threadIdx.x >> 8;
  const int pb = xcd * 8 + (bidx - 24);
  STAMP(0, 0);
  u32x4 pf[4];
#pragma unroll
  for (int i = 0; i < 4; ++i) pf[i] = *(const u32x4*)(ssmk + (size_t)pb * 16384 + (i * 512 + threadIdx.x) * 8);
  const int wid = pb * 8 + wv;
  const u32x4 gw = *(const u32x4*)(gatew + l * 8);
  u32x4 xv[4];
#pragma unroll
  for (int it = 0; it < 4; ++it) xv[it] = *(const u32x4*)(x + (size_t)(wid * 4 + it) * DM + l * 8);
  {
    const int ch = t & 15, rg = t >> 4;
    u32x4 kr[7], vr[4], sk[4];
    const int tile = pb * 2 + h2;
    const int s0 = (tile >> 2) * 64, d0 = (tile & 3) * 128;
    {
      const int sb = s0 + rg * 4;
      const int dd = d0 + ch * 8;
#pragma unroll
      for (int i = 0; i < 7; ++i) {
        const int s = sb - 3 + i;
        u32x4 z = {0u, 0u, 0u, 0u};
        kr[i] = (s >= 0) ? *(const u32x4*)(kin + (size_t)s * DM + dd) : z;
      }
#pragma unroll
      for (int i = 0; i < 4; ++i) vr[i] = *(const u32x4*)(vin + (size_t)(sb + i) * DM + dd);
#pragma unroll
      for (int i = 0; i < 4; ++i) sk[i] = *(const u32x4*)(shiftk + (size_t)dd * 4 + i * 8);
    }
    char* sm = smem + h2 * 17408;
#pragma unroll
    for (int c = 0; c < 8; ++c) {
      const unsigned s01 = sk[c >> 1][(c & 1) * 2], s23 = sk[c >> 1][(c & 1) * 2 + 1];
      const float sk0 = bflo(s01), sk1 = bfhi(s01), sk2 = bflo(s23), sk3 = bfhi(s23);
      float kk[7];
#pragma unroll
      for (int i = 0; i < 7; ++i) kk[i] = (c & 1) ? bfhi(kr[i][c >> 1]) : bflo(kr[i][c >> 1]);
      unsigned o[2];
#pragma unroll
      for (int i = 0; i < 4; i += 2) {
        const unsigned p3 = pack2(kk[i] * sk3, kk[i + 1] * sk3);
        const unsigned p2 = pack2(kk[i + 1] * sk2, kk[i + 2] * sk2);
        const unsigned p1 = pack2(kk[i + 2] * sk1, kk[i + 3] * sk1);
        const unsigned p0 = pack2(kk[i + 3] * sk0, kk[i + 4] * sk0);
        unsigned a = pack2(bflo(p3) + bflo(p2), bfhi(p3) + bfhi(p2));
        a = pack2(bflo(a) + bflo(p1), bfhi(a) + bfhi(p1));
        a = pack2(bflo(a) + bflo(p0), bfhi(a) + bfhi(p0));
        const float v0 = (c & 1) ? bfhi(vr[i][c >> 1]) : bflo(vr[i][c >> 1]);
        const float v1 = (c & 1) ? bfhi(vr[i + 1][c >> 1]) : bflo(vr[i + 1][c >> 1]);
        o[i >> 1] = pack2(bflo(a) * v0, bfhi(a) * v1);
      }
      *(u32x2*)(sm + (ch * 8 + c) * 136 + rg * 8) = (u32x2){o[0], o[1]};
    }
    __syncthreads();
    const rsrc_t rs = ws_rsrc(ws);
#pragma unroll
    for (int i = 0; i < 4; ++i) {
      const int idx = t + 256 * i;
      const int dr = idx >> 3, c8 = idx & 7;
      const u32x2 a = *(const u32x2*)(sm + dr * 136 + c8 * 16);
      const u32x2 b = *(const u32x2*)(sm + dr * 136 + c8 * 16 + 8);
      u32x4 v = {a[0], a[1], b[0], b[1]};
      wst16(rs, WS_KVT + (unsigned)((d0 + dr) * SEQ + s0 + c8 * 8) * 2, v);
    }
  }
  STAMP(0, 11);
  {
    float* g = (float*)(ws + WS_G);
    float sums[4];
#pragma unroll
    for (int it = 0; it < 4; ++it) {
      float sum = 0.f;
#pragma unroll
      for (int i = 0; i < 4; ++i) {
        const unsigned pr = pack2(bflo(xv[it][i]) * bflo(gw[i]), bfhi(xv[it][i]) * bfhi(gw[i]));
        sum += bflo(pr);
        sum += bfhi(pr);
      }
      sums[it] = sum;
    }
#pragma unroll
    for (int o = 32; o >= 1; o >>= 1)
#pragma unroll
      for (int it = 0; it < 4; ++it) sums[it] += __shfl_xor(sums[it], o);
    if (l < 4) {
      float sv = sums[0];
#pragma unroll
      for (int it = 1; it < 4; ++it) sv = (l == it) ? sums[it] : sv;
      const float z = rbf(sv);
      g[wid * 4 + l] = rbf(1.f / (1.f + __expf(-z)));
    }
  }
#pragma unroll
  for (int i = 0; i < 4; ++i) asm volatile("" ::"v"(pf[i]));
  STAMP(0, 12);
}

#define CV_KVSTR 80
#define CV_HSTR 4160
#define CV_HBYTES (8 * CV_HSTR)
#define CV_REDSTR 36
#define CV_SMEM 40960

#define AT_OFF (2 * CV_SMEM)
#define K2_SMEM (AT_OFF + 32768 + 64)

struct ConvFr {
  bf16x8 a0, a1, b0, b1;
};

template <bool FIRST>
DI void k2_body(const u16* __restrict__ ssmk, const u16* __restrict__ ssmD, char* __restrict__ ws, char* smem,
                int head, int qb, int dpair) {
  const int l = threadIdx.x & 63, wv = threadIdx.x >> 6;
  const int r = l & 31, hh = l >> 5;
  const int t = threadIdx.x & 255, half = threadIdx.x >> 8, cw = wv & 3;
  const int d = dpair * 2 + half;
  STAMP(1, 0);
  const u16* kvT = (const u16*)(ws + WS_KVT) + (size_t)d * SEQ;
  const u16* hrow = ssmk + (size_t)d * SEQ;
  const u32x4 kvv = *(const u32x4*)(kvT + 8 * t);
  const u32x4 whi = *(const u32x4*)(hrow + 8 * t);
  const u32x4 wlo0 = *(const u32x4*)(hrow + 8 * (t > 0 ? t - 1 : 0));
  const float Dd = bf2f(ssmD[d]);
  const u16* Qh = (const u16*)(ws + WS_Q) + (size_t)head * SEQ * 16;
  const u16* Kh = (const u16*)(ws + WS_K) + (size_t)head * SEQ * 16;
  const u16* Vh = (const u16*)(ws + WS_VT) + (size_t)head * 16 * SEQ;
  const int q0 = qb + 32 * wv;
  const int kt0 = FIRST ? (8 - wv) : 0;
  char* ks_ = smem + AT_OFF;
  char* vs_ = smem + AT_OFF + 16384;
  char* cs_ = smem + AT_OFF + 32768;
  {
    const int key = l >> 1;
    const u16* ksrc = Kh + (size_t)key * 16 + (((l & 1) ^ ((key >> 3) & 1)) * 8);
    const int dd = l >> 2;
    const u16* vsrc = Vh + (size_t)dd * SEQ + (((l & 3) ^ ((dd >> 2) & 3)) * 8);
#pragma unroll
    for (int i = 0; i < 2; ++i) {
      const int u = wv + 8 * i;
      int key0 = qb - 256 + 32 * u;
      key0 = key0 < 0 ? 0 : key0;
      GLDS16(ksrc + (size_t)key0 * 16, ks_ + u * 1024 + l * 16);
      GLDS16(vsrc + key0, vs_ + u * 1024 + l * 16);
    }
  }
  const bf16x8 qf = *(const bf16x8*)(Qh + (size_t)(q0 + r) * 16 + 8 * hh);
  if (threadIdx.x < 2) {
    const unsigned o = threadIdx.x == 0 ? 0x3f803f80u : 0u;
    *(u32x4*)(cs_ + threadIdx.x * 16) = (u32x4){o, o, o, o};
  }
  char* hs = smem + half * CV_SMEM;
  char* kvs = hs + CV_HBYTES;
  *(u32x4*)(kvs + ((t >> 2) + 32) * CV_KVSTR + (t & 3) * 16) = kvv;
  if (t < 128) {
    u32x4 z = {0u, 0u, 0u, 0u};
    *(u32x4*)(kvs + (t >> 2) * CV_KVSTR + (t & 3) * 16) = z;
  }
  {
    const int E = t;
    u32x4 wlo = wlo0;
    if (E == 0) wlo = (u32x4){0u, 0u, 0u, 0u};
    unsigned z[8];
#pragma unroll
    for (int i = 0; i < 4; ++i) {
      const unsigned a = whi[3 - i];
      z[i] = (a >> 16) | (a << 16);
      const unsigned b = wlo[3 - i];
      z[4 + i] = (b >> 16) | (b << 16);
    }
    const int M = 255 - E;
#pragma unroll
    for (int p = 0; p < 8; ++p) {
      const int o = 7 - p;
      u32x4 v;
      if ((o & 1) == 0) {
        v[0] = z[o / 2];
        v[1] = z[o / 2 + 1];
        v[2] = z[o / 2 + 2];
        v[3] = z[o / 2 + 3];
      } else {
        v[0] = (z[o / 2] >> 16) | (z[o / 2 + 1] << 16);
        v[1] = (z[o / 2 + 1] >> 16) | (z[o / 2 + 2] << 16);
        v[2] = (z[o / 2 + 2] >> 16) | (z[o / 2 + 3] << 16);
        v[3] = (z[o / 2 + 3] >> 16) | (z[o / 2 + 4] << 16);
      }
      *(u32x4*)(hs + p * CV_HSTR + 16 * M) = v;
    }
    if (t < 24) {
      u32x4 zz = {0u, 0u, 0u, 0u};
      *(u32x4*)(hs + (t & 7) * CV_HSTR + 16 * (256 + (t >> 3))) = zz;
    }
  }
  WAIT_V(0);
  RAWBAR();
  STAMP(1, 1);

  f32x16 zero;
#pragma unroll
  for (int e = 0; e < 16; ++e) zero[e] = 0.f;
  f32x16 acc0 = zero, acc1 = zero, acc2 = zero;
  const char* abase = hs + (r & 7) * CV_HSTR + 16 * (255 - (r >> 3) + hh) - 64 * cw;
  const char* bbase = kvs + (r + 32 - cw) * CV_KVSTR + 16 * hh;
  auto cld = [&](int step) {
    const int i = step >> 1, ks = step & 1;
    ConvFr f;
    f.a0 = *(const bf16x8*)(abase - 256 * i + 32 * ks);
    f.a1 = *(const bf16x8*)(abase - 256 * i - 2048 + 32 * ks);
    f.b1 = *(const bf16x8*)(bbase + (32 - 4 * i) * CV_KVSTR + 32 * ks);
    f.b0 = *(const bf16x8*)(bbase - (4 * i) * CV_KVSTR + 32 * ks);
    return f;
  };
  auto cmm = [&](const ConvFr& f) {
    acc1 = MFMA32(f.a0, f.b1, acc1);
    acc0 = MFMA32(f.a0, f.b0, acc0);
    acc2 = MFMA32(f.a1, f.b0, acc2);
  };

  const char* krd = ks_ + (32 * wv + r) * 32 + ((hh ^ ((r >> 3) & 1)) * 16);
  const float NEG = -3.0e38f;
  auto mask = [&](int kt, f32x16& s) {
    if (kt == 0) {
#pragma unroll
      for (int e = 0; e < 16; ++e) s[e] = ((e & 3) + 8 * (e >> 2) + 4 * hh > r) ? s[e] : NEG;
    }
    if (kt == 8) {
#pragma unroll
      for (int e = 0; e < 16; ++e) s[e] = ((e & 3) + 8 * (e >> 2) + 4 * hh <= r) ? s[e] : NEG;
    }
    if (FIRST && kt < 8) {
#pragma unroll
      for (int e = 0; e < 16; ++e) s[e] = (kt >= kt0) ? s[e] : NEG;
    }
  };
  bf16x8 kf[9];
#pragma unroll
  for (int kt = 0; kt < 9; ++kt) kf[kt] = *(const bf16x8*)(krd + kt * 1024);
  ConvFr cf = cld(0);
  float mx = NEG;
  {
    f32x16 s[2];
    s[0] = MFMA32(kf[0], qf, zero);
#pragma unroll
    for (int kt = 0; kt < 9; ++kt) {
      ConvFr cn;
      if (kt < 7) cn = cld(kt + 1);
      if (kt < 8) s[(kt + 1) & 1] = MFMA32(kf[kt + 1], qf, zero);
      if (kt < 7) cmm(cf);
      f32x16& c = s[kt & 1];
      mask(kt, c);
#pragma unroll
      for (int e = 0; e < 16; e += 2) mx = fmaxf(fmaxf(c[e], c[e + 1]), mx);
      if (kt < 7) cf = cn;
      __builtin_amdgcn_sched_barrier(0);
    }
  }
  mx = fmaxf(mx, __shfl_xor(mx, 32));
  f32x16 negm;
#pragma unroll
  for (int e = 0; e < 16; ++e) negm[e] = -mx;
  STAMP(1, 2);
  const int vsw = (r >> 2) & 3;
  const bool isd = r < 16;
  const int cad = AT_OFF + 32768 + ((r == 16 || r == 20) ? 0 : 16);
  int va0 = isd ? AT_OFF + 16384 + wv * 1024 + r * 64 + ((hh ^ vsw) * 16) : cad;
  int va1 = isd ? AT_OFF + 16384 + wv * 1024 + r * 64 + (((2 + hh) ^ vsw) * 16) : cad;
  const int vstep = isd ? 1024 : 0;
  f32x16 oacc = zero;
  {
    f32x16 s[2];
    s[0] = MFMA32(kf[0], qf, negm);
#pragma unroll
    for (int kt = 0; kt < 9; ++kt) {
      const bf16x8 vf0 = *(const bf16x8*)(smem + va0);
      const bf16x8 vf1 = *(const bf16x8*)(smem + va1);
      va0 += vstep;
      va1 += vstep;
      ConvFr cn;
      if (kt < 8) cn = cld(kt + 8);
      if (kt < 8) s[(kt + 1) & 1] = MFMA32(kf[kt + 1], qf, negm);
      cmm(cf);
      f32x16& c = s[kt & 1];
      mask(kt, c);
      u32x4 pp[2];
#pragma unroll
      for (int e = 0; e < 16; e += 2)
        pp[e >> 3][(e >> 1) & 3] = pack2(__builtin_amdgcn_exp2f(c[e]), __builtin_amdgcn_exp2f(c[e + 1]));
      oacc = MFMA32(vf0, __builtin_bit_cast(bf16x8, pp[0]), oacc);
      oacc = MFMA32(vf1, __builtin_bit_cast(bf16x8, pp[1]), oacc);
      if (kt < 8) cf = cn;
      __builtin_amdgcn_sched_barrier(0);
    }
  }
  STAMP(1, 3);
  const rsrc_t rs = ws_rsrc(ws);
  {
    const float inv = 1.f / oacc[8];
    u32x2 v0 = {pack2(oacc[0] * inv, oacc[1] * inv), pack2(oacc[2] * inv, oacc[3] * inv)};
    u32x2 v1 = {pack2(oacc[4] * inv, oacc[5] * inv), pack2(oacc[6] * inv, oacc[7] * inv)};
    const unsigned op = WS_O + (unsigned)((q0 + r) * DM + head * 16 + 4 * hh) * 2;
    wst8(rs, op, v0);
    wst8(rs, op + 16, v1);
  }
  acc1 += acc2;
  __syncthreads();
  STAMP(1, 6);
  float* red = (float*)hs;
  {
    float* rw = red + cw * (64 * CV_REDSTR);
#pragma unroll
    for (int g = 0; g < 4; ++g) {
      f32x4 v0 = {acc0[4 * g], acc0[4 * g + 1], acc0[4 * g + 2], acc0[4 * g + 3]};
      f32x4 v1 = {acc1[4 * g], acc1[4 * g + 1], acc1[4 * g + 2], acc1[4 * g + 3]};
      *(f32x4*)(rw + r * CV_REDSTR + 8 * g + 4 * hh) = v0;
      *(f32x4*)(rw + (32 + r) * CV_REDSTR + 8 * g + 4 * hh) = v1;
    }
  }
  __syncthreads();
  STAMP(1, 7);
  {
    const int a = t >> 2, b0 = 8 * (t & 3);
    f32x4 s0 = {0.f, 0.f, 0.f, 0.f}, s1 = {0.f, 0.f, 0.f, 0.f};
#pragma unroll
    for (int w = 0; w < 4; ++w) {
      const float* rp = red + w * (64 * CV_REDSTR) + a * CV_REDSTR + b0;
      s0 += *(const f32x4*)(rp);
      s1 += *(const f32x4*)(rp + 4);
    }
    float yy[8] = {s0[0], s0[1], s0[2], s0[3], s1[0], s1[1], s1[2], s1[3]};
    u32x4 o;
#pragma unroll
    for (int i = 0; i < 4; ++i) {
      const float y0 = rbf(yy[2 * i]) + rbf(bflo(kvv[i]) * Dd);
      const float y1 = rbf(yy[2 * i + 1]) + rbf(bfhi(kvv[i]) * Dd);
      o[i] = pack2(y0, y1);
    }
    wst16(rs, WS_Y2T + (unsigned)(d * SEQ + 8 * t) * 2, o);
  }
  STAMP(1, 4);
}

__global__ __launch_bounds__(512) void k2_kernel(const u16* __restrict__ ssmk, const u16* __restrict__ ssmD,
                                                 const u16* __restrict__ wo, char* __restrict__ ws) {
  __shared__ __attribute__((aligned(16))) char smem[K2_SMEM];
  const int xcd = blockIdx.x & 7, bidx = blockIdx.x >> 3;
  const u32x4 pf0 = *(const u32x4*)(wo + (size_t)bidx * 8192 + threadIdx.x * 8);
  const u32x4 pf1 = *(const u32x4*)(wo + (size_t)bidx * 8192 + 4096 + threadIdx.x * 8);
  if (xcd == 0)
    k2_body<true>(ssmk, ssmD, ws, smem, bidx, 0, xcd * 32 + bidx);
  else
    k2_body<false>(ssmk, ssmD, ws, smem, bidx, xcd * 256, xcd * 32 + bidx);
  asm volatile("" ::"v"(pf0));
  asm volatile("" ::"v"(pf1));
}

#define K3_STAGE 32768
#define K3_CSTR 68
#define K3_YSTR 136

DI void k3_body(const u16* __restrict__ x, const u16* __restrict__ wo, const char* __restrict__ ws,
                u16* __restrict__ out, char* smem) {
  const int t = threadIdx.x;
  const int l = t & 63, wv = t >> 6;
  const int r = l & 31, hh = l >> 5;
  const int wm = wv & 1, wn = (wv >> 1) & 1, kh = wv >> 2;
  const int tm = (blockIdx.x & 7) * 4 + ((blockIdx.x >> 3) & 3), tn = blockIdx.x >> 5;
  const int m0 = tm * 64, n0 = tn * 64;
  const u16* o = (const u16*)(ws + WS_O);
  STAMP(2, 0);

  const u16* y2T = (const u16*)(ws + WS_Y2T);
  const float* gp = (const float*)(ws + WS_G);
  const int rr = t >> 3, ch = t & 7;
  const u32x4 yv = *(const u32x4*)(y2T + (size_t)(n0 + rr) * SEQ + m0 + ch * 8);
  const u32x4 xq = *(const u32x4*)(x + (size_t)(m0 + rr) * DM + n0 + ch * 8);
  const float g = gp[m0 + rr];
  __builtin_amdgcn_sched_barrier(0);
  {
    char* ldsw = smem + wv * 2048 + l * 16;
#pragma unroll
    for (int s4 = 0; s4 < 4; ++s4) {
      const int st = s4;
#pragma unroll
      for (int i = 0; i < 2; ++i) {
        const int ar = 8 * wv + 4 * i + (l >> 4);
        GLDS16(o + (size_t)(m0 + ar) * DM + st * 128 + (((l & 15) ^ (ar & 15)) * 8), ldsw + st * K3_STAGE + i * 1024);
      }
#pragma unroll
      for (int i = 0; i < 2; ++i) {
        const int br = 16 * wv + 8 * i + (l >> 3);
        GLDS16(wo + (size_t)(st * 128 + br) * DM + n0 + (((l & 7) ^ (((br >> 1) & 1) << 2)) * 8),
               ldsw + st * K3_STAGE + 16384 + i * 1024);
      }
    }
  }

  f32x16 acc;
#pragma unroll
  for (int e = 0; e < 16; ++e) acc[e] = 0.f;
  const int blk = (l >> 4) & 1, q4 = (l & 15) >> 2, p4 = l & 3;
  const int arow = wm * 32 + r;
  const int a_off = arow * 256;
  const int a_sw = arow & 15;
  const int b_off = 16384 + (8 * hh + q4) * 128 + (((wn * 4 + 2 * blk + (p4 >> 1)) ^ (((q4 >> 1) & 1) << 2)) * 16) +
                    (p4 & 1) * 8;
  const int a_x = (8 * kh + hh) ^ a_sw;
  const int b_k = b_off + 64 * kh * 128;
#pragma unroll
  for (int st = 0; st < 4; ++st) {
    if (st == 0) WAIT_V(12);
    else if (st == 1) WAIT_V(8);
    else if (st == 2) WAIT_V(4);
    else WAIT_V(0);
    RAWBAR();
    const char* sb = smem + st * K3_STAGE;
    bf16x8 af[4], bfr[4];
#pragma unroll
    for (int ks = 0; ks < 4; ++ks) {
      af[ks] = *(const bf16x8*)(sb + a_off + ((a_x ^ (2 * ks)) * 16));
      s16x4 lo = tr_read(sb + b_k + (16 * ks) * 128);
      s16x4 hi = tr_read(sb + b_k + (16 * ks + 4) * 128);
      bfr[ks] = cat_frag(lo, hi);
    }
    __builtin_amdgcn_sched_barrier(0);
#pragma unroll
    for (int ks = 0; ks < 4; ++ks) acc = MFMA32(af[ks], bfr[ks], acc);
    __builtin_amdgcn_sched_barrier(0);
  }
  __syncthreads();
  STAMP(2, 5);
  float* Cs = (float*)smem;
  char* Ys = smem + 2 * 64 * K3_CSTR * 4;
#pragma unroll
  for (int e = 0; e < 16; ++e) {
    const int row = wm * 32 + (e & 3) + 8 * (e >> 2) + 4 * hh;
    Cs[kh * 64 * K3_CSTR + row * K3_CSTR + wn * 32 + r] = acc[e];
  }
  {
    u32x2 a = {yv[0], yv[1]}, b = {yv[2], yv[3]};
    *(u32x2*)(Ys + rr * K3_YSTR + ch * 16) = a;
    *(u32x2*)(Ys + rr * K3_YSTR + ch * 16 + 8) = b;
  }
  __syncthreads();
  {
    const int row = rr;
    const int s = m0 + row;
    const float omg = rbf(1.f - g);
    const f32x4 c0 = *(const f32x4*)(Cs + row * K3_CSTR + ch * 8) + *(const f32x4*)(Cs + (64 + row) * K3_CSTR + ch * 8);
    const f32x4 c1 =
        *(const f32x4*)(Cs + row * K3_CSTR + ch * 8 + 4) + *(const f32x4*)(Cs + (64 + row) * K3_CSTR + ch * 8 + 4);
    float bb[8] = {c0[0], c0[1], c0[2], c0[3], c1[0], c1[1], c1[2], c1[3]};
    float res[8];
#pragma unroll
    for (int j = 0; j < 8; ++j) {
      const float y2 = bf2f(*(const u16*)(Ys + (ch * 8 + j) * K3_YSTR + row * 2));
      const float qv = (j & 1) ? bfhi(xq[j >> 1]) : bflo(xq[j >> 1]);
      const float a = rbf(qv * y2);
      const float ga = rbf(g * a);
      const float gb = rbf(omg * rbf(bb[j]));
      res[j] = ga + gb;
    }
    u32x4 ov;
    ov[0] = pack2(res[0], res[1]);
    ov[1] = pack2(res[2], res[3]);
    ov[2] = pack2(res[4], res[5]);
    ov[3] = pack2(res[6], res[7]);
    *(u32x4*)(out + (size_t)s * DM + n0 + ch * 8) = ov;
  }
  STAMP(2, 6);
}

__global__ __launch_bounds__(512) void k3_kernel(const u16* __restrict__ x, const u16* __restrict__ wo,
                                                 const char* __restrict__ ws, u16* __restrict__ out) {
  __shared__ __attribute__((aligned(16))) char smem[4 * K3_STAGE];
  k3_body(x, wo, ws, out, smem);
}

extern "C" void kernel_launch(void* const* d_in, const int* in_sizes, int n_in, void* d_out, int out_size,
                              void* d_ws, size_t ws_size, hipStream_t stream) {
  const u16* A_q = (const u16*)d_in[0];
  const u16* A_k = (const u16*)d_in[1];
  const u16* A_v = (const u16*)d_in[2];
  const u16* A_shift = (const u16*)d_in[3];
  const u16* A_ssmk = (const u16*)d_in[4];
  const u16* A_ssmD = (const u16*)d_in[5];
  const u16* B_wqkv = (const u16*)d_in[7];
  const u16* B_wo = (const u16*)d_in[8];
  const u16* gate_w = (const u16*)d_in[9];
  char* ws = (char*)d_ws;
  k1_kernel<<<256, 512, 0, stream>>>(A_q, A_k, A_v, A_shift, B_wqkv, gate_w, A_ssmk, ws);
  k2_kernel<<<256, 512, 0, stream>>>(A_ssmk, A_ssmD, B_wo, ws);
  k3_kernel<<<256, 512, 0, stream>>>(A_q, B_wo, ws, (u16*)d_out);
}
```

```cpp
#include <hip/hip_runtime.h>
#include <hip/hip_bf16.h>
#include <stdint.h>

#define DI __device__ __forceinline__
typedef __attribute__((ext_vector_type(8))) __bf16 bf16x8;
typedef __attribute__((ext_vector_type(4))) __bf16 bf16x4;
typedef __attribute__((ext_vector_type(2))) __bf16 bf16x2;
typedef __attribute__((ext_vector_type(4))) short s16x4;
typedef __attribute__((ext_vector_type(16))) float f32x16;
typedef __attribute__((ext_vector_type(4))) float f32x4;
typedef __attribute__((ext_vector_type(2))) float f32x2;
typedef __attribute__((ext_vector_type(4))) unsigned u32x4;
typedef __attribute__((ext_vector_type(2))) unsigned u32x2;
typedef unsigned short u16;

#define SEQ 2048
#define DM 512

#define MFMA32(a, b, c) __builtin_amdgcn_mfma_f32_32x32x16_bf16((a), (b), (c), 0, 0, 0)

DI float bf2f(u16 v) { return __uint_as_float(((unsigned)v) << 16); }
DI float bflo(unsigned v) { return __uint_as_float(v << 16); }
DI float bfhi(unsigned v) { return __uint_as_float(v & 0xffff0000u); }
DI unsigned pack2(float a, float b) {
  f32x2 f = {a, b};
  bf16x2 r = __builtin_convertvector(f, bf16x2);
  return __builtin_bit_cast(unsigned, r);
}
DI float rbf(float a) {
  return (float)((__bf16)a);
}
DI s16x4 tr_read(const char* p) {
  typedef __attribute__((address_space(3))) s16x4* lp;
  return __builtin_amdgcn_ds_read_tr16_b64_v4i16((lp)(p));
}
DI bf16x8 cat_frag(s16x4 lo, s16x4 hi) {
  typedef __attribute__((ext_vector_type(8))) short s16x8;
  s16x8 v = __builtin_shufflevector(lo, hi, 0, 1, 2, 3, 4, 5, 6, 7);
  return __builtin_bit_cast(bf16x8, v);
}

#define STAMP(k, i) do {} while (0)
#define WS_Q 0
#define WS_K (2u << 20)
#define WS_VT (4u << 20)
#define WS_KVT (6u << 20)
#define WS_Y2T (8u << 20)
#define WS_O (10u << 20)
#define WS_G (12u << 20)

#define WT_AUX 0
typedef __amdgpu_buffer_rsrc_t rsrc_t;
DI rsrc_t ws_rsrc(const char* ws) { return __builtin_amdgcn_make_buffer_rsrc((void*)ws, (short)0, 16 << 20, 0x00020000); }
DI void wst16(rsrc_t rs, unsigned off, u32x4 v) { __builtin_amdgcn_raw_buffer_store_b128(v, rs, (int)off, 0, WT_AUX); }
DI void wst8(rsrc_t rs, unsigned off, u32x2 v) { __builtin_amdgcn_raw_buffer_store_b64(v, rs, (int)off, 0, WT_AUX); }

#define GLDS16(g, l) __builtin_amdgcn_global_load_lds((const unsigned*)(g), (unsigned*)(l), 16, 0, 0)
#define WAIT_V(n) asm volatile("s_waitcnt vmcnt(" #n ")" ::: "memory")
#define RAWBAR() do { asm volatile("s_waitcnt lgkmcnt(0)" ::: "memory"); __builtin_amdgcn_s_barrier(); } while (0)
#define K1_STAGE 32768

template <int N>
DI void wait_vm() {
  if constexpr (N == 0) WAIT_V(0);
  else if constexpr (N == 4) WAIT_V(4);
  else if constexpr (N == 8) WAIT_V(8);
  else if constexpr (N == 12) WAIT_V(12);
  else if constexpr (N == 16) WAIT_V(16);
  else if constexpr (N == 24) WAIT_V(24);
}

#define QSCALE (0.25f * 1.44269504088896340736f)

template <int MODE>
DI void k1_gemm(const u16* __restrict__ x, const u16* __restrict__ w, char* __restrict__ ws, char* smem, int tm,
                int tn) {
  const int t = threadIdx.x;
  const int l = t & 63, wv = t >> 6;
  const int r = l & 31, hh = l >> 5;
  const int wm = wv & 1, wn = wv >> 1;
  const int m0 = tm * 128, n0 = tn * 128;

  const u16* xg[2];
  const u16* wg[2];
#pragma unroll
  for (int i = 0; i < 2; ++i) {
    const int ar = 16 * wv + 8 * i + (l >> 3);
    xg[i] = x + (size_t)(m0 + ar) * DM + (((l & 7) ^ ((ar >> 1) & 7)) * 8);
    const int br = 8 * wv + 4 * i + (l >> 4);
    wg[i] = w + (size_t)br * 1536 + n0 + (((l & 15) ^ ((br & 3) << 2)) * 8);
  }
  char* ldsw = smem + wv * 2048 + l * 16;

  const int rot = 0;
  auto issue = [&](int kt) {
    char* st = ldsw + (kt & 3) * K1_STAGE;
    const int kk = (kt + rot) & 7;
#pragma unroll
    for (int i = 0; i < 2; ++i) GLDS16(xg[i] + kk * 64, st + i * 1024);
#pragma unroll
    for (int i = 0; i < 2; ++i) GLDS16(wg[i] + (size_t)kk * 64 * 1536, st + 16384 + i * 1024);
  };

  f32x16 acc[2];
#pragma unroll
  for (int i = 0; i < 2; ++i)
#pragma unroll
    for (int e = 0; e < 16; ++e) acc[i][e] = 0.f;

  const int blk = (l >> 4) & 1, q4 = (l & 15) >> 2, p4 = l & 3;
  const int a_row_off = (wm * 64 + r) * 128;
  const int a_sw = (r >> 1) & 7;
  const int b_base = 16384 + (8 * hh + q4) * 256 + (p4 & 1) * 8 + (((wn * 4 + 2 * blk + (p4 >> 1)) ^ (q4 << 2)) * 16);

  STAMP(0, 0);
  issue(0);
  issue(1);
  issue(2);
  issue(3);
  bf16x8 fa[2][4][2], fb[2][4];
  auto ldfr = [&](int kt, bf16x8(&a)[4][2], bf16x8(&b)[4]) {
    const char* st = smem + (kt & 3) * K1_STAGE;
#pragma unroll
    for (int ks = 0; ks < 4; ++ks) {
#pragma unroll
      for (int i = 0; i < 2; ++i)
        a[ks][i] = *(const bf16x8*)(st + a_row_off + i * 32 * 128 + (((2 * ks + hh) ^ a_sw) * 16));
      s16x4 lo = tr_read(st + b_base + (16 * ks) * 256);
      s16x4 hi = tr_read(st + b_base + (16 * ks + 4) * 256);
      b[ks] = cat_frag(lo, hi);
    }
  };
  wait_vm<12>();
  RAWBAR();
  ldfr(0, fa[0], fb[0]);
#pragma unroll
  for (int kt = 0; kt < 8; ++kt) {
    if (kt < 7) {
      if (kt <= 4) wait_vm<8>();
      else if (kt == 5) wait_vm<4>();
      else wait_vm<0>();
      RAWBAR();
      ldfr(kt + 1, fa[(kt + 1) & 1], fb[(kt + 1) & 1]);
    }
    __builtin_amdgcn_sched_barrier(0);
#pragma unroll
    for (int ks = 0; ks < 4; ++ks) {
#pragma unroll
      for (int i = 0; i < 2; ++i) {
        if (MODE == 2)
          acc[i] = MFMA32(fa[kt & 1][ks][i], fb[kt & 1][ks], acc[i]);
        else
          acc[i] = MFMA32(fb[kt & 1][ks], fa[kt & 1][ks][i], acc[i]);
      }
      __builtin_amdgcn_sched_barrier(0);
      if (kt + 4 <= 7) {
        char* st4 = ldsw + (kt & 3) * K1_STAGE;
        const int kk = (kt + 4 + rot) & 7;
        if (ks < 2)
          GLDS16(xg[ks] + kk * 64, st4 + ks * 1024);
        else
          GLDS16(wg[ks - 2] + (size_t)kk * 64 * 1536, st4 + 16384 + (ks - 2) * 1024);
      }
      __builtin_amdgcn_sched_barrier(0);
    }
  }

  STAMP(0, 9);
  const rsrc_t rs = ws_rsrc(ws);
  if (MODE == 2) {
#pragma unroll
    for (int i = 0; i < 2; ++i) {
      const int n = (n0 - 1024) + wn * 32 + r;
      const int tok0 = m0 + wm * 64 + i * 32;
      const unsigned dst = WS_VT + (unsigned)(n * SEQ + tok0 + 8 * hh) * 2;
#pragma unroll
      for (int bb = 0; bb < 2; ++bb) {
        u32x4 v;
        v[0] = pack2(acc[i][8 * bb + 0], acc[i][8 * bb + 1]);
        v[1] = pack2(acc[i][8 * bb + 2], acc[i][8 * bb + 3]);
        v[2] = pack2(acc[i][8 * bb + 4], acc[i][8 * bb + 5]);
        v[3] = pack2(acc[i][8 * bb + 6], acc[i][8 * bb + 7]);
        wst16(rs, dst + 32 * bb, v);
      }
    }
  } else {
    const unsigned base = (MODE == 1 ? WS_K : WS_Q);
    const int nn0 = n0 & 511;
#pragma unroll
    for (int i = 0; i < 2; ++i) {
      const int tok = m0 + wm * 64 + i * 32 + r;
      const int nb = nn0 + wn * 32;
      const int head = nb >> 4;
#pragma unroll
      for (int g = 0; g < 4; ++g) {
        u32x2 v;
        const float sc = (MODE == 0) ? QSCALE : 1.f;
        v[0] = pack2(acc[i][4 * g + 0] * sc, acc[i][4 * g + 1] * sc);
        v[1] = pack2(acc[i][4 * g + 2] * sc, acc[i][4 * g + 3] * sc);
        wst8(rs, base + (unsigned)(((head + (g >> 1)) * SEQ + tok) * 16 + 8 * (g & 1) + 4 * hh) * 2, v);
      }
    }
  }
  STAMP(0, 10);
}

__global__ __launch_bounds__(512) void k1_kernel(const u16* __restrict__ x, const u16* __restrict__ kin,
                                                 const u16* __restrict__ vin, const u16* __restrict__ shiftk,
                                                 const u16* __restrict__ wqkv, const u16* __restrict__ ssmk,
                                                 char* __restrict__ ws) {
  __shared__ __attribute__((aligned(16))) char smem[4 * K1_STAGE];
  const int xcd = blockIdx.x & 7, bidx = blockIdx.x >> 3;
  if (bidx < 24) {
    const int tm = 2 * xcd + (bidx & 1), tn = bidx >> 1;
    if (tn >= 8)
      k1_gemm<2>(x, wqkv, ws, smem, tm, tn);
    else if (tn >= 4)
      k1_gemm<1>(x, wqkv, ws, smem, tm, tn);
    else
      k1_gemm<0>(x, wqkv, ws, smem, tm, tn);
    return;
  }
  const int l = threadIdx.x & 63, wv = threadIdx.x >> 6;
  const int t = threadIdx.x & 255, h2 = threadIdx.x >> 8;
  const int pb = xcd * 8 + (bidx - 24);
  STAMP(0, 0);
  u32x4 pf[4];
  {
    const int ch = t & 15, rg = t >> 4;
    u32x4 kr[7], vr[4], sk[4];
    const int tile = pb * 2 + h2;
    const int s0 = (tile >> 2) * 64, d0 = (tile & 3) * 128;
    {
      const int sb = s0 + rg * 4;
      const int dd = d0 + ch * 8;
#pragma unroll
      for (int i = 0; i < 7; ++i) {
        const int s = sb - 3 + i;
        u32x4 z = {0u, 0u, 0u, 0u};
        kr[i] = (s >= 0) ? *(const u32x4*)(kin + (size_t)s * DM + dd) : z;
      }
#pragma unroll
      for (int i = 0; i < 4; ++i) vr[i] = *(const u32x4*)(vin + (size_t)(sb + i) * DM + dd);
#pragma unroll
      for (int i = 0; i < 4; ++i) sk[i] = *(const u32x4*)(shiftk + (size_t)dd * 4 + i * 8);
    }
    __builtin_amdgcn_sched_barrier(0);
#pragma unroll
    for (int i = 0; i < 4; ++i) pf[i] = *(const u32x4*)(ssmk + (size_t)pb * 16384 + (i * 512 + threadIdx.x) * 8);
    __builtin_amdgcn_sched_barrier(0);
    char* sm = smem + h2 * 17408;
#pragma unroll
    for (int c = 0; c < 8; ++c) {
      const unsigned s01 = sk[c >> 1][(c & 1) * 2], s23 = sk[c >> 1][(c & 1) * 2 + 1];
      const float sk0 = bflo(s01), sk1 = bfhi(s01), sk2 = bflo(s23), sk3 = bfhi(s23);
      float kk[7];
#pragma unroll
      for (int i = 0; i < 7; ++i) kk[i] = (c & 1) ? bfhi(kr[i][c >> 1]) : bflo(kr[i][c >> 1]);
      unsigned o[2];
#pragma unroll
      for (int i = 0; i < 4; i += 2) {
        const f32x2 q3 = (f32x2){kk[i], kk[i + 1]} * (f32x2){sk3, sk3};
        const f32x2 q2 = (f32x2){kk[i + 1], kk[i + 2]} * (f32x2){sk2, sk2};
        const f32x2 q1 = (f32x2){kk[i + 2], kk[i + 3]} * (f32x2){sk1, sk1};
        const f32x2 q0 = (f32x2){kk[i + 3], kk[i + 4]} * (f32x2){sk0, sk0};
        const unsigned p3 = pack2(q3[0], q3[1]), p2 = pack2(q2[0], q2[1]);
        const unsigned p1 = pack2(q1[0], q1[1]), p0 = pack2(q0[0], q0[1]);
        f32x2 a = (f32x2){bflo(p3), bfhi(p3)} + (f32x2){bflo(p2), bfhi(p2)};
        unsigned ap = pack2(a[0], a[1]);
        a = (f32x2){bflo(ap), bfhi(ap)} + (f32x2){bflo(p1), bfhi(p1)};
        ap = pack2(a[0], a[1]);
        a = (f32x2){bflo(ap), bfhi(ap)} + (f32x2){bflo(p0), bfhi(p0)};
        ap = pack2(a[0], a[1]);
        const float v0 = (c & 1) ? bfhi(vr[i][c >> 1]) : bflo(vr[i][c >> 1]);
        const float v1 = (c & 1) ? bfhi(vr[i + 1][c >> 1]) : bflo(vr[i + 1][c >> 1]);
        a = (f32x2){bflo(ap), bfhi(ap)} * (f32x2){v0, v1};
        o[i >> 1] = pack2(a[0], a[1]);
      }
      *(u32x2*)(sm + (ch * 8 + c) * 136 + rg * 8) = (u32x2){o[0], o[1]};
    }
    STAMP(0, 14);
    __syncthreads();
    STAMP(0, 15);
    const rsrc_t rs = ws_rsrc(ws);
#pragma unroll
    for (int i = 0; i < 4; ++i) {
      const int idx = t + 256 * i;
      const int dr = idx >> 3, c8 = idx & 7;
      const u32x2 a = *(const u32x2*)(sm + dr * 136 + c8 * 16);
      const u32x2 b = *(const u32x2*)(sm + dr * 136 + c8 * 16 + 8);
      u32x4 v = {a[0], a[1], b[0], b[1]};
      wst16(rs, WS_KVT + (unsigned)((d0 + dr) * SEQ + s0 + c8 * 8) * 2, v);
    }
  }
  STAMP(0, 11);
#pragma unroll
  for (int i = 0; i < 4; ++i) asm volatile("" ::"v"(pf[i]));
  STAMP(0, 12);
}

#define CV_KVSTR 80
#define CV_HSTR 4160
#define CV_HBYTES (8 * CV_HSTR)
#define CV_REDSTR 36
#define CV_SMEM 40960

template <bool FIRST>
DI void attn_block(const char* __restrict__ ws, char* smem, int head, int qb) {
  const int l = threadIdx.x & 63, wv = threadIdx.x >> 6;
  const int r = l & 31, hh = l >> 5;
  const u16* Qh = (const u16*)(ws + WS_Q) + (size_t)head * SEQ * 16;
  const u16* Kh = (const u16*)(ws + WS_K) + (size_t)head * SEQ * 16;
  const u16* Vh = (const u16*)(ws + WS_VT) + (size_t)head * 16 * SEQ;
  u16* O = (u16*)(ws + WS_O);
  const int q0 = qb + 32 * wv;
  const int kt0 = FIRST ? (8 - wv) : 0;
  STAMP(1, 0);
  char* ks_ = smem;
  char* vs_ = smem + 16384;
  char* cs_ = smem + 32768;
  {
    const int key = l >> 1;
    const u16* ksrc = Kh + (size_t)key * 16 + (((l & 1) ^ ((key >> 3) & 1)) * 8);
    const int dd = l >> 2;
    const u16* vsrc = Vh + (size_t)dd * SEQ + (((l & 3) ^ ((dd >> 2) & 3)) * 8);
#pragma unroll
    for (int i = 0; i < 2; ++i) {
      const int u = wv + 8 * i;
      int key0 = qb - 256 + 32 * u;
      key0 = key0 < 0 ? 0 : key0;
      GLDS16(ksrc + (size_t)key0 * 16, ks_ + u * 1024 + l * 16);
      GLDS16(vsrc + key0, vs_ + u * 1024 + l * 16);
    }
  }
  const bf16x8 qf = *(const bf16x8*)(Qh + (size_t)(q0 + r) * 16 + 8 * hh);
  if (threadIdx.x < 2) {
    const unsigned o = threadIdx.x == 0 ? 0x3f803f80u : 0u;
    *(u32x4*)(cs_ + threadIdx.x * 16) = (u32x4){o, o, o, o};
  }
  WAIT_V(0);
  RAWBAR();
  STAMP(1, 1);
  const char* krd = ks_ + (32 * wv + r) * 32 + ((hh ^ ((r >> 3) & 1)) * 16);
  f32x16 zero;
#pragma unroll
  for (int e = 0; e < 16; ++e) zero[e] = 0.f;
  const float NEG = -3.0e38f;
  auto mask = [&](int kt, f32x16& s) {
    if (kt == 0) {
#pragma unroll
      for (int e = 0; e < 16; ++e) s[e] = ((e & 3) + 8 * (e >> 2) + 4 * hh > r) ? s[e] : NEG;
    }
    if (kt == 8) {
#pragma unroll
      for (int e = 0; e < 16; ++e) s[e] = ((e & 3) + 8 * (e >> 2) + 4 * hh <= r) ? s[e] : NEG;
    }
  };
  float mx = NEG, mx2 = NEG;
  {
    bf16x8 kf[9];
#pragma unroll
    for (int kt = 0; kt < 9; ++kt) kf[kt] = *(const bf16x8*)(krd + kt * 1024);
    f32x16 s[3];
    s[0] = MFMA32(kf[0], qf, zero);
    s[1] = MFMA32(kf[1], qf, zero);
#pragma unroll
    for (int kt = 0; kt < 9; ++kt) {
      if (kt < 7) s[(kt + 2) % 3] = MFMA32(kf[kt + 2], qf, zero);
      f32x16& c = s[kt % 3];
      mask(kt, c);
      float m = NEG, m2 = NEG;
#pragma unroll
      for (int e = 0; e < 8; e += 2) {
        m = fmaxf(fmaxf(c[e], c[e + 1]), m);
        m2 = fmaxf(fmaxf(c[8 + e], c[9 + e]), m2);
      }
      if (FIRST) {
        m = (kt >= kt0) ? m : NEG;
        m2 = (kt >= kt0) ? m2 : NEG;
      }
      mx = fmaxf(mx, m);
      mx2 = fmaxf(mx2, m2);
    }
  }
  mx = fmaxf(mx, mx2);
  mx = fmaxf(mx, __shfl_xor(mx, 32));
  f32x16 negm;
#pragma unroll
  for (int e = 0; e < 16; ++e) negm[e] = -mx;
  STAMP(1, 2);
  const int vsw = (r >> 2) & 3;
  const bool isd = r < 16;
  const int cad = 32768 + ((r == 16 || r == 20) ? 0 : 16);
  int va0 = isd ? 16384 + wv * 1024 + r * 64 + ((hh ^ vsw) * 16) : cad;
  int va1 = isd ? 16384 + wv * 1024 + r * 64 + (((2 + hh) ^ vsw) * 16) : cad;
  const int vstep = isd ? 1024 : 0;
  f32x16 oacc = zero;
  {
    f32x16 s[2];
    bf16x8 kf = *(const bf16x8*)(krd);
    s[0] = MFMA32(kf, qf, negm);
#pragma unroll
    for (int kt = 0; kt < 9; ++kt) {
      const bf16x8 vf0 = *(const bf16x8*)(smem + va0);
      const bf16x8 vf1 = *(const bf16x8*)(smem + va1);
      va0 += vstep;
      va1 += vstep;
      if (kt < 8) {
        kf = *(const bf16x8*)(krd + (kt + 1) * 1024);
        s[(kt + 1) & 1] = MFMA32(kf, qf, negm);
      }
      f32x16& c = s[kt & 1];
      mask(kt, c);
      if (!FIRST || kt >= kt0) {
        u32x4 pp[2];
#pragma unroll
        for (int e = 0; e < 16; e += 2) {
          pp[e >> 3][(e >> 1) & 3] = pack2(__builtin_amdgcn_exp2f(c[e]), __builtin_amdgcn_exp2f(c[e + 1]));
        }
        oacc = MFMA32(vf0, __builtin_bit_cast(bf16x8, pp[0]), oacc);
        oacc = MFMA32(vf1, __builtin_bit_cast(bf16x8, pp[1]), oacc);
      }
    }
  }
  STAMP(1, 3);
  const float inv = 1.f / oacc[8];
  u32x2 v0 = {pack2(oacc[0] * inv, oacc[1] * inv), pack2(oacc[2] * inv, oacc[3] * inv)};
  u32x2 v1 = {pack2(oacc[4] * inv, oacc[5] * inv), pack2(oacc[6] * inv, oacc[7] * inv)};
  const rsrc_t rs = ws_rsrc(ws);
  const unsigned op = WS_O + (unsigned)((q0 + r) * DM + head * 16 + 4 * hh) * 2;
  wst8(rs, op, v0);
  wst8(rs, op + 16, v1);
  STAMP(1, 4);
}

DI void conv_block(const u16* __restrict__ ssmk, const u16* __restrict__ ssmD, char* __restrict__ ws, char* smem,
                   int d) {
  const int t = threadIdx.x & 255;
  const int l = t & 63, wv = t >> 6;
  const int r = l & 31, hh = l >> 5;
  char* hs = smem;
  char* kvs = smem + CV_HBYTES;
  const u16* kvT = (const u16*)(ws + WS_KVT) + (size_t)d * SEQ;
  const u16* hrow = ssmk + (size_t)d * SEQ;
  STAMP(1, 0);
  const u32x4 kvv = *(const u32x4*)(kvT + 8 * t);
  const u32x4 whi = *(const u32x4*)(hrow + 8 * t);
  const u32x4 wlo0 = *(const u32x4*)(hrow + 8 * (t > 0 ? t - 1 : 0));
  const float Dd = bf2f(ssmD[d]);
  *(u32x4*)(kvs + ((t >> 2) + 32) * CV_KVSTR + (t & 3) * 16) = kvv;
  if (t < 128) {
    u32x4 z = {0u, 0u, 0u, 0u};
    *(u32x4*)(kvs + (t >> 2) * CV_KVSTR + (t & 3) * 16) = z;
  }
  {
    const int E = t;
    u32x4 wlo = wlo0;
    if (E == 0) wlo = (u32x4){0u, 0u, 0u, 0u};
    unsigned z[8];
#pragma unroll
    for (int i = 0; i < 4; ++i) {
      const unsigned a = whi[3 - i];
      z[i] = (a >> 16) | (a << 16);
      const unsigned b = wlo[3 - i];
      z[4 + i] = (b >> 16) | (b << 16);
    }
    const int M = 255 - E;
#pragma unroll
    for (int p = 0; p < 8; ++p) {
      const int o = 7 - p;
      u32x4 v;
      if ((o & 1) == 0) {
        v[0] = z[o / 2];
        v[1] = z[o / 2 + 1];
        v[2] = z[o / 2 + 2];
        v[3] = z[o / 2 + 3];
      } else {
        v[0] = (z[o / 2] >> 16) | (z[o / 2 + 1] << 16);
        v[1] = (z[o / 2 + 1] >> 16) | (z[o / 2 + 2] << 16);
        v[2] = (z[o / 2 + 2] >> 16) | (z[o / 2 + 3] << 16);
        v[3] = (z[o / 2 + 3] >> 16) | (z[o / 2 + 4] << 16);
      }
      *(u32x4*)(hs + p * CV_HSTR + 16 * M) = v;
    }
    if (t < 24) {
      u32x4 zz = {0u, 0u, 0u, 0u};
      *(u32x4*)(hs + (t & 7) * CV_HSTR + 16 * (256 + (t >> 3))) = zz;
    }
  }
  __syncthreads();
  STAMP(1, 1);
  f32x16 acc0, acc1, acc2;
#pragma unroll
  for (int e = 0; e < 16; ++e) {
    acc0[e] = 0.f;
    acc1[e] = 0.f;
    acc2[e] = 0.f;
  }
  const char* abase = hs + (r & 7) * CV_HSTR + 16 * (255 - (r >> 3) + hh) - 64 * wv;
  const char* bbase = kvs + (r + 32 - wv) * CV_KVSTR + 16 * hh;
  {
    bf16x8 fa[2][2], fb[2][2];
    auto ldc = [&](int step, bf16x8(&a)[2], bf16x8(&b)[2]) {
      const int i = step >> 1, ks = step & 1;
      a[0] = *(const bf16x8*)(abase - 256 * i + 32 * ks);
      a[1] = *(const bf16x8*)(abase - 256 * i - 2048 + 32 * ks);
      b[1] = *(const bf16x8*)(bbase + (32 - 4 * i) * CV_KVSTR + 32 * ks);
      b[0] = *(const bf16x8*)(bbase - (4 * i) * CV_KVSTR + 32 * ks);
    };
    ldc(0, fa[0], fb[0]);
#pragma unroll
    for (int step = 0; step < 16; ++step) {
      if (step < 15) ldc(step + 1, fa[(step + 1) & 1], fb[(step + 1) & 1]);
      __builtin_amdgcn_sched_barrier(0);
      acc1 = MFMA32(fa[step & 1][0], fb[step & 1][1], acc1);
      acc0 = MFMA32(fa[step & 1][0], fb[step & 1][0], acc0);
      acc2 = MFMA32(fa[step & 1][1], fb[step & 1][0], acc2);
      __builtin_amdgcn_sched_barrier(0);
    }
  }
  acc1 += acc2;
  STAMP(1, 2);
  __syncthreads();
  STAMP(1, 6);
  float* red = (float*)smem;
  {
    float* rw = red + wv * (64 * CV_REDSTR);
#pragma unroll
    for (int g = 0; g < 4; ++g) {
      f32x4 v0 = {acc0[4 * g], acc0[4 * g + 1], acc0[4 * g + 2], acc0[4 * g + 3]};
      f32x4 v1 = {acc1[4 * g], acc1[4 * g + 1], acc1[4 * g + 2], acc1[4 * g + 3]};
      *(f32x4*)(rw + r * CV_REDSTR + 8 * g + 4 * hh) = v0;
      *(f32x4*)(rw + (32 + r) * CV_REDSTR + 8 * g + 4 * hh) = v1;
    }
  }
  __syncthreads();
  STAMP(1, 7);
  {
    const int a = t >> 2, b0 = 8 * (t & 3);
    f32x4 s0 = {0.f, 0.f, 0.f, 0.f}, s1 = {0.f, 0.f, 0.f, 0.f};
#pragma unroll
    for (int w = 0; w < 4; ++w) {
      const float* rp = red + w * (64 * CV_REDSTR) + a * CV_REDSTR + b0;
      s0 += *(const f32x4*)(rp);
      s1 += *(const f32x4*)(rp + 4);
    }
    float yy[8] = {s0[0], s0[1], s0[2], s0[3], s1[0], s1[1], s1[2], s1[3]};
    u32x4 o;
#pragma unroll
    for (int i = 0; i < 4; ++i) {
      const float y0 = rbf(yy[2 * i]) + rbf(bflo(kvv[i]) * Dd);
      const float y1 = rbf(yy[2 * i + 1]) + rbf(bfhi(kvv[i]) * Dd);
      o[i] = pack2(y0, y1);
    }
    wst16(ws_rsrc(ws), WS_Y2T + (unsigned)(d * SEQ + 8 * t) * 2, o);
  }
  STAMP(1, 4);
}

__global__ __launch_bounds__(512, 4) void k2_kernel(const u16* __restrict__ ssmk, const u16* __restrict__ ssmD,
                                                    const u16* __restrict__ wo, const u16* __restrict__ x,
                                                    const u16* __restrict__ gatew, char* __restrict__ ws) {
  __shared__ __attribute__((aligned(16))) char smem[2 * CV_SMEM];
  const int xcd = blockIdx.x & 7, bidx = (blockIdx.x >> 3) ^ 32;
  if (bidx >= 32) {
    const u32x4 pf0 = *(const u32x4*)(wo + (size_t)(bidx - 32) * 8192 + threadIdx.x * 8);
    const u32x4 pf1 = *(const u32x4*)(wo + (size_t)(bidx - 32) * 8192 + 4096 + threadIdx.x * 8);
    const int half = threadIdx.x >> 8;
    conv_block(ssmk, ssmD, ws, smem + half * CV_SMEM, xcd * 64 + (bidx - 32) * 2 + half);
    {
      const int l = threadIdx.x & 63;
      const int row = (xcd * 32 + (bidx - 32)) * 8 + (threadIdx.x >> 6);
      const u32x4 gw = *(const u32x4*)(gatew + l * 8);
      const u32x4 xv = *(const u32x4*)(x + (size_t)row * DM + l * 8);
      float sum = 0.f;
#pragma unroll
      for (int i = 0; i < 4; ++i) {
        const unsigned pr = pack2(bflo(xv[i]) * bflo(gw[i]), bfhi(xv[i]) * bfhi(gw[i]));
        sum += bflo(pr);
        sum += bfhi(pr);
      }
#pragma unroll
      for (int o = 32; o >= 1; o >>= 1) sum += __shfl_xor(sum, o);
      if (l == 0) ((float*)(ws + WS_G))[row] = rbf(1.f / (1.f + __expf(-rbf(sum))));
    }
    asm volatile("" ::"v"(pf0));
    asm volatile("" ::"v"(pf1));
  } else {
    if (xcd == 0)
      attn_block<true>(ws, smem, bidx, 0);
    else
      attn_block<false>(ws, smem, bidx, xcd * 256);
  }
}

#define K3_STAGE 32768
#define K3_CSTR 68
#define K3_YSTR 136

DI void k3_body(const u16* __restrict__ x, const u16* __restrict__ wo, const char* __restrict__ ws,
                u16* __restrict__ out, char* smem) {
  const int t = threadIdx.x;
  const int l = t & 63, wv = t >> 6;
  const int r = l & 31, hh = l >> 5;
  const int wm = wv & 1, wn = (wv >> 1) & 1, kh = wv >> 2;
  const int tm = (blockIdx.x & 7) * 4 + ((blockIdx.x >> 3) & 3), tn = blockIdx.x >> 5;
  const int m0 = tm * 64, n0 = tn * 64;
  const u16* o = (const u16*)(ws + WS_O);
  STAMP(2, 0);

  const u16* y2T = (const u16*)(ws + WS_Y2T);
  const float* gp = (const float*)(ws + WS_G);
  const int rr = t >> 3, ch = t & 7;
  const u32x4 yv = *(const u32x4*)(y2T + (size_t)(n0 + rr) * SEQ + m0 + ch * 8);
  const u32x4 xq = *(const u32x4*)(x + (size_t)(m0 + rr) * DM + n0 + ch * 8);
  const float g = gp[m0 + rr];
  __builtin_amdgcn_sched_barrier(0);
  {
    char* ldsw = smem + wv * 2048 + l * 16;
#pragma unroll
    for (int s4 = 0; s4 < 4; ++s4) {
      const int st = s4;
#pragma unroll
      for (int i = 0; i < 2; ++i) {
        const int ar = 8 * wv + 4 * i + (l >> 4);
        GLDS16(o + (size_t)(m0 + ar) * DM + st * 128 + (((l & 15) ^ (ar & 15)) * 8), ldsw + st * K3_STAGE + i * 1024);
      }
#pragma unroll
      for (int i = 0; i < 2; ++i) {
        const int br = 16 * wv + 8 * i + (l >> 3);
        GLDS16(wo + (size_t)(st * 128 + br) * DM + n0 + (((l & 7) ^ (((br >> 1) & 1) << 2)) * 8),
               ldsw + st * K3_STAGE + 16384 + i * 1024);
      }
    }
  }

  f32x16 acc;
#pragma unroll
  for (int e = 0; e < 16; ++e) acc[e] = 0.f;
  const int blk = (l >> 4) & 1, q4 = (l & 15) >> 2, p4 = l & 3;
  const int arow = wm * 32 + r;
  const int a_off = arow * 256;
  const int a_sw = arow & 15;
  const int b_off = 16384 + (8 * hh + q4) * 128 + (((wn * 4 + 2 * blk + (p4 >> 1)) ^ (((q4 >> 1) & 1) << 2)) * 16) +
                    (p4 & 1) * 8;
  const int a_x = (8 * kh + hh) ^ a_sw;
  const int b_k = b_off + 64 * kh * 128;
#pragma unroll
  for (int st = 0; st < 4; ++st) {
    if (st == 0) WAIT_V(12);
    else if (st == 1) WAIT_V(8);
    else if (st == 2) WAIT_V(4);
    else WAIT_V(0);
    RAWBAR();
    const char* sb = smem + st * K3_STAGE;
    bf16x8 af[4], bfr[4];
#pragma unroll
    for (int ks = 0; ks < 4; ++ks) {
      af[ks] = *(const bf16x8*)(sb + a_off + ((a_x ^ (2 * ks)) * 16));
      s16x4 lo = tr_read(sb + b_k + (16 * ks) * 128);
      s16x4 hi = tr_read(sb + b_k + (16 * ks + 4) * 128);
      bfr[ks] = cat_frag(lo, hi);
    }
    __builtin_amdgcn_sched_barrier(0);
#pragma unroll
    for (int ks = 0; ks < 4; ++ks) acc = MFMA32(af[ks], bfr[ks], acc);
    __builtin_amdgcn_sched_barrier(0);
  }
  __syncthreads();
  STAMP(2, 5);
  float* Cs = (float*)smem;
  char* Ys = smem + 2 * 64 * K3_CSTR * 4;
#pragma unroll
  for (int e = 0; e < 16; ++e) {
    const int row = wm * 32 + (e & 3) + 8 * (e >> 2) + 4 * hh;
    Cs[kh * 64 * K3_CSTR + row * K3_CSTR + wn * 32 + r] = acc[e];
  }
  {
    u32x2 a = {yv[0], yv[1]}, b = {yv[2], yv[3]};
    *(u32x2*)(Ys + rr * K3_YSTR + ch * 16) = a;
    *(u32x2*)(Ys + rr * K3_YSTR + ch * 16 + 8) = b;
  }
  __syncthreads();
  {
    const int row = rr;
    const int s = m0 + row;
    const float omg = rbf(1.f - g);
    const f32x4 c0 = *(const f32x4*)(Cs + row * K3_CSTR + ch * 8) + *(const f32x4*)(Cs + (64 + row) * K3_CSTR + ch * 8);
    const f32x4 c1 =
        *(const f32x4*)(Cs + row * K3_CSTR + ch * 8 + 4) + *(const f32x4*)(Cs + (64 + row) * K3_CSTR + ch * 8 + 4);
    float bb[8] = {c0[0], c0[1], c0[2], c0[3], c1[0], c1[1], c1[2], c1[3]};
    float res[8];
#pragma unroll
    for (int j = 0; j < 8; ++j) {
      const float y2 = bf2f(*(const u16*)(Ys + (ch * 8 + j) * K3_YSTR + row * 2));
      const float qv = (j & 1) ? bfhi(xq[j >> 1]) : bflo(xq[j >> 1]);
      const float a = rbf(qv * y2);
      const float ga = rbf(g * a);
      const float gb = rbf(omg * rbf(bb[j]));
      res[j] = ga + gb;
    }
    u32x4 ov;
    ov[0] = pack2(res[0], res[1]);
    ov[1] = pack2(res[2], res[3]);
    ov[2] = pack2(res[4], res[5]);
    ov[3] = pack2(res[6], res[7]);
    *(u32x4*)(out + (size_t)s * DM + n0 + ch * 8) = ov;
  }
  STAMP(2, 6);
}

__global__ __launch_bounds__(512) void k3_kernel(const u16* __restrict__ x, const u16* __restrict__ wo,
                                                 const char* __restrict__ ws, u16* __restrict__ out) {
  __shared__ __attribute__((aligned(16))) char smem[4 * K3_STAGE];
  k3_body(x, wo, ws, out, smem);
}

extern "C" void kernel_launch(void* const* d_in, const int* in_sizes, int n_in, void* d_out, int out_size,
                              void* d_ws, size_t ws_size, hipStream_t stream) {
  const u16* A_q = (const u16*)d_in[0];
  const u16* A_k = (const u16*)d_in[1];
  const u16* A_v = (const u16*)d_in[2];
  const u16* A_shift = (const u16*)d_in[3];
  const u16* A_ssmk = (const u16*)d_in[4];
  const u16* A_ssmD = (const u16*)d_in[5];
  const u16* B_wqkv = (const u16*)d_in[7];
  const u16* B_wo = (const u16*)d_in[8];
  const u16* gate_w = (const u16*)d_in[9];
  char* ws = (char*)d_ws;
  k1_kernel<<<256, 512, 0, stream>>>(A_q, A_k, A_v, A_shift, B_wqkv, A_ssmk, ws);
  k2_kernel<<<512, 512, 0, stream>>>(A_ssmk, A_ssmD, B_wo, A_q, gate_w, ws);
  k3_kernel<<<256, 512, 0, stream>>>(A_q, B_wo, ws, (u16*)d_out);
}
```

```cpp
#include <hip/hip_runtime.h>
#include <hip/hip_bf16.h>
#include <stdint.h>

#define DI __device__ __forceinline__
typedef __attribute__((ext_vector_type(8))) __bf16 bf16x8;
typedef __attribute__((ext_vector_type(4))) __bf16 bf16x4;
typedef __attribute__((ext_vector_type(2))) __bf16 bf16x2;
typedef __attribute__((ext_vector_type(4))) short s16x4;
typedef __attribute__((ext_vector_type(16))) float f32x16;
typedef __attribute__((ext_vector_type(4))) float f32x4;
typedef __attribute__((ext_vector_type(2))) float f32x2;
typedef __attribute__((ext_vector_type(4))) unsigned u32x4;
typedef __attribute__((ext_vector_type(2))) unsigned u32x2;
typedef unsigned short u16;

#define SEQ 2048
#define DM 512

#define MFMA32(a, b, c) __builtin_amdgcn_mfma_f32_32x32x16_bf16((a), (b), (c), 0, 0, 0)

DI float bf2f(u16 v) { return __uint_as_float(((unsigned)v) << 16); }
DI float bflo(unsigned v) { return __uint_as_float(v << 16); }
DI float bfhi(unsigned v) { return __uint_as_float(v & 0xffff0000u); }
DI unsigned pack2(float a, float b) {
  f32x2 f = {a, b};
  bf16x2 r = __builtin_convertvector(f, bf16x2);
  return __builtin_bit_cast(unsigned, r);
}
DI float rbf(float a) {
  return (float)((__bf16)a);
}
DI s16x4 tr_read(const char* p) {
  typedef __attribute__((address_space(3))) s16x4* lp;
  return __builtin_amdgcn_ds_read_tr16_b64_v4i16((lp)(p));
}
DI bf16x8 cat_frag(s16x4 lo, s16x4 hi) {
  typedef __attribute__((ext_vector_type(8))) short s16x8;
  s16x8 v = __builtin_shufflevector(lo, hi, 0, 1, 2, 3, 4, 5, 6, 7);
  return __builtin_bit_cast(bf16x8, v);
}

#define STAMP(k, i) do {} while (0)
#define WS_Q 0
#define WS_K (2u << 20)
#define WS_VT (4u << 20)
#define WS_KVT (6u << 20)
#define WS_Y2T (8u << 20)
#define WS_O (10u << 20)
#define WS_G (12u << 20)

#define WT_AUX 0
typedef __amdgpu_buffer_rsrc_t rsrc_t;
DI rsrc_t ws_rsrc(const char* ws) { return __builtin_amdgcn_make_buffer_rsrc((void*)ws, (short)0, 16 << 20, 0x00020000); }
#define AUX_ST1 0
#define AUX_ST2 0
#define AUX_KVT 16
#define AUX_Y2T 16
#define NT_KV
#define AUX_LD2 0
#define AUX_LD3 0
template <int AUX>
DI void wst16(rsrc_t rs, unsigned off, u32x4 v) { __builtin_amdgcn_raw_buffer_store_b128(v, rs, (int)off, 0, AUX); }
template <int AUX>
DI void wst8(rsrc_t rs, unsigned off, u32x2 v) { __builtin_amdgcn_raw_buffer_store_b64(v, rs, (int)off, 0, AUX); }
template <bool NT>
DI u32x4 ld16(const void* p) {
  if constexpr (NT) return __builtin_nontemporal_load((const u32x4*)p);
  else return *(const u32x4*)p;
}

#define GLDS16(g, l) __builtin_amdgcn_global_load_lds((const unsigned*)(g), (unsigned*)(l), 16, 0, 0)
#define GLDS16A(g, l, aux) __builtin_amdgcn_global_load_lds((const unsigned*)(g), (unsigned*)(l), 16, 0, aux)
#define AUX_X 2
#define AUX_W 0
#define WAIT_V(n) asm volatile("s_waitcnt vmcnt(" #n ")" ::: "memory")
#define RAWBAR() do { asm volatile("s_waitcnt lgkmcnt(0)" ::: "memory"); __builtin_amdgcn_s_barrier(); } while (0)
#define K1_STAGE 32768

template <int N>
DI void wait_vm() {
  if constexpr (N == 0) WAIT_V(0);
  else if constexpr (N == 4) WAIT_V(4);
  else if constexpr (N == 8) WAIT_V(8);
  else if constexpr (N == 12) WAIT_V(12);
  else if constexpr (N == 16) WAIT_V(16);
  else if constexpr (N == 24) WAIT_V(24);
}

#define QSCALE (0.25f * 1.44269504088896340736f)

template <int MODE>
DI void k1_gemm(const u16* __restrict__ x, const u16* __restrict__ w, char* __restrict__ ws, char* smem, int tm,
                int tn) {
  const int t = threadIdx.x;
  const int l = t & 63, wv = t >> 6;
  const int r = l & 31, hh = l >> 5;
  const int wm = wv & 1, wn = wv >> 1;
  const int m0 = tm * 128, n0 = tn * 128;

  const u16* xg[2];
  const u16* wg[2];
#pragma unroll
  for (int i = 0; i < 2; ++i) {
    const int ar = 16 * wv + 8 * i + (l >> 3);
    xg[i] = x + (size_t)(m0 + ar) * DM + (((l & 7) ^ ((ar >> 1) & 7)) * 8);
    const int br = 8 * wv + 4 * i + (l >> 4);
    wg[i] = w + (size_t)br * 1536 + n0 + (((l & 15) ^ ((br & 3) << 2)) * 8);
  }
  char* ldsw = smem + wv * 2048 + l * 16;

  const int rot = 0;
  auto issue = [&](int kt) {
    char* st = ldsw + (kt & 3) * K1_STAGE;
    const int kk = (kt + rot) & 7;
#pragma unroll
    for (int i = 0; i < 2; ++i) GLDS16A(xg[i] + kk * 64, st + i * 1024, AUX_X);
#pragma unroll
    for (int i = 0; i < 2; ++i) GLDS16A(wg[i] + (size_t)kk * 64 * 1536, st + 16384 + i * 1024, AUX_W);
  };

  f32x16 acc[2];
#pragma unroll
  for (int i = 0; i < 2; ++i)
#pragma unroll
    for (int e = 0; e < 16; ++e) acc[i][e] = 0.f;

  const int blk = (l >> 4) & 1, q4 = (l & 15) >> 2, p4 = l & 3;
  const int a_row_off = (wm * 64 + r) * 128;
  const int a_sw = (r >> 1) & 7;
  const int b_base = 16384 + (8 * hh + q4) * 256 + (p4 & 1) * 8 + (((wn * 4 + 2 * blk + (p4 >> 1)) ^ (q4 << 2)) * 16);

  STAMP(0, 0);
  issue(0);
  issue(1);
  issue(2);
  issue(3);
  bf16x8 fa[2][4][2], fb[2][4];
  auto ldfr = [&](int kt, bf16x8(&a)[4][2], bf16x8(&b)[4]) {
    const char* st = smem + (kt & 3) * K1_STAGE;
#pragma unroll
    for (int ks = 0; ks < 4; ++ks) {
#pragma unroll
      for (int i = 0; i < 2; ++i)
        a[ks][i] = *(const bf16x8*)(st + a_row_off + i * 32 * 128 + (((2 * ks + hh) ^ a_sw) * 16));
      s16x4 lo = tr_read(st + b_base + (16 * ks) * 256);
      s16x4 hi = tr_read(st + b_base + (16 * ks + 4) * 256);
      b[ks] = cat_frag(lo, hi);
    }
  };
  wait_vm<12>();
  RAWBAR();
  ldfr(0, fa[0], fb[0]);
#pragma unroll
  for (int kt = 0; kt < 8; ++kt) {
    if (kt < 7) {
      if (kt <= 4) wait_vm<8>();
      else if (kt == 5) wait_vm<4>();
      else wait_vm<0>();
      RAWBAR();
      ldfr(kt + 1, fa[(kt + 1) & 1], fb[(kt + 1) & 1]);
    }
    __builtin_amdgcn_sched_barrier(0);
#pragma unroll
    for (int ks = 0; ks < 4; ++ks) {
#pragma unroll
      for (int i = 0; i < 2; ++i) {
        if (MODE == 2)
          acc[i] = MFMA32(fa[kt & 1][ks][i], fb[kt & 1][ks], acc[i]);
        else
          acc[i] = MFMA32(fb[kt & 1][ks], fa[kt & 1][ks][i], acc[i]);
      }
      __builtin_amdgcn_sched_barrier(0);
      if (kt + 4 <= 7) {
        char* st4 = ldsw + (kt & 3) * K1_STAGE;
        const int kk = (kt + 4 + rot) & 7;
        if (ks < 2)
          GLDS16A(xg[ks] + kk * 64, st4 + ks * 1024, AUX_X);
        else
          GLDS16A(wg[ks - 2] + (size_t)kk * 64 * 1536, st4 + 16384 + (ks - 2) * 1024, AUX_W);
      }
      __builtin_amdgcn_sched_barrier(0);
    }
  }

  STAMP(0, 9);
  const rsrc_t rs = ws_rsrc(ws);
  if (MODE == 2) {
#pragma unroll
    for (int i = 0; i < 2; ++i) {
      const int n = (n0 - 1024) + wn * 32 + r;
      const int tok0 = m0 + wm * 64 + i * 32;
      const unsigned dst = WS_VT + (unsigned)(n * SEQ + tok0 + 8 * hh) * 2;
#pragma unroll
      for (int bb = 0; bb < 2; ++bb) {
        u32x4 v;
        v[0] = pack2(acc[i][8 * bb + 0], acc[i][8 * bb + 1]);
        v[1] = pack2(acc[i][8 * bb + 2], acc[i][8 * bb + 3]);
        v[2] = pack2(acc[i][8 * bb + 4], acc[i][8 * bb + 5]);
        v[3] = pack2(acc[i][8 * bb + 6], acc[i][8 * bb + 7]);
        wst16<AUX_ST1>(rs, dst + 32 * bb, v);
      }
    }
  } else {
    const unsigned base = (MODE == 1 ? WS_K : WS_Q);
    const int nn0 = n0 & 511;
#pragma unroll
    for (int i = 0; i < 2; ++i) {
      const int tok = m0 + wm * 64 + i * 32 + r;
      const int nb = nn0 + wn * 32;
      const int head = nb >> 4;
#pragma unroll
      for (int g = 0; g < 4; ++g) {
        u32x2 v;
        const float sc = (MODE == 0) ? QSCALE : 1.f;
        v[0] = pack2(acc[i][4 * g + 0] * sc, acc[i][4 * g + 1] * sc);
        v[1] = pack2(acc[i][4 * g + 2] * sc, acc[i][4 * g + 3] * sc);
        wst8<AUX_ST1>(rs, base + (unsigned)(((head + (g >> 1)) * SEQ + tok) * 16 + 8 * (g & 1) + 4 * hh) * 2, v);
      }
    }
  }
  STAMP(0, 10);
}

__global__ __launch_bounds__(512) void k1_kernel(const u16* __restrict__ x, const u16* __restrict__ kin,
                                                 const u16* __restrict__ vin, const u16* __restrict__ shiftk,
                                                 const u16* __restrict__ wqkv, const u16* __restrict__ ssmk,
                                                 char* __restrict__ ws) {
  __shared__ __attribute__((aligned(16))) char smem[4 * K1_STAGE];
  const int xcd = blockIdx.x & 7, bidx = blockIdx.x >> 3;
  if (bidx < 24) {
    const int tm = 2 * xcd + (bidx & 1), tn = bidx >> 1;
    if (tn >= 8)
      k1_gemm<2>(x, wqkv, ws, smem, tm, tn);
    else if (tn >= 4)
      k1_gemm<1>(x, wqkv, ws, smem, tm, tn);
    else
      k1_gemm<0>(x, wqkv, ws, smem, tm, tn);
    return;
  }
  const int l = threadIdx.x & 63, wv = threadIdx.x >> 6;
  const int t = threadIdx.x & 255, h2 = threadIdx.x >> 8;
  const int pb = xcd * 8 + (bidx - 24);
  STAMP(0, 0);
  u32x4 pf[4];
  {
    const int ch = t & 15, rg = t >> 4;
    u32x4 kr[7], vr[4], sk[4];
    const int tile = pb * 2 + h2;
    const int s0 = (tile >> 2) * 64, d0 = (tile & 3) * 128;
    {
      const int sb = s0 + rg * 4;
      const int dd = d0 + ch * 8;
#pragma unroll
      for (int i = 0; i < 7; ++i) {
        const int s = sb - 3 + i;
        u32x4 z = {0u, 0u, 0u, 0u};
        kr[i] = (s >= 0) ? __builtin_nontemporal_load((const u32x4*)(kin + (size_t)s * DM + dd)) : z;
      }
#pragma unroll
      for (int i = 0; i < 4; ++i) vr[i] = __builtin_nontemporal_load((const u32x4*)(vin + (size_t)(sb + i) * DM + dd));
#pragma unroll
      for (int i = 0; i < 4; ++i) sk[i] = *(const u32x4*)(shiftk + (size_t)dd * 4 + i * 8);
    }
    __builtin_amdgcn_sched_barrier(0);
#pragma unroll
    for (int i = 0; i < 4; ++i) pf[i] = *(const u32x4*)(ssmk + (size_t)pb * 16384 + (i * 512 + threadIdx.x) * 8);
    __builtin_amdgcn_sched_barrier(0);
    char* sm = smem + h2 * 17408;
#pragma unroll
    for (int c = 0; c < 8; ++c) {
      const unsigned s01 = sk[c >> 1][(c & 1) * 2], s23 = sk[c >> 1][(c & 1) * 2 + 1];
      const float sk0 = bflo(s01), sk1 = bfhi(s01), sk2 = bflo(s23), sk3 = bfhi(s23);
      float kk[7];
#pragma unroll
      for (int i = 0; i < 7; ++i) kk[i] = (c & 1) ? bfhi(kr[i][c >> 1]) : bflo(kr[i][c >> 1]);
      unsigned o[2];
#pragma unroll
      for (int i = 0; i < 4; i += 2) {
        const f32x2 q3 = (f32x2){kk[i], kk[i + 1]} * (f32x2){sk3, sk3};
        const f32x2 q2 = (f32x2){kk[i + 1], kk[i + 2]} * (f32x2){sk2, sk2};
        const f32x2 q1 = (f32x2){kk[i + 2], kk[i + 3]} * (f32x2){sk1, sk1};
        const f32x2 q0 = (f32x2){kk[i + 3], kk[i + 4]} * (f32x2){sk0, sk0};
        const unsigned p3 = pack2(q3[0], q3[1]), p2 = pack2(q2[0], q2[1]);
        const unsigned p1 = pack2(q1[0], q1[1]), p0 = pack2(q0[0], q0[1]);
        f32x2 a = (f32x2){bflo(p3), bfhi(p3)} + (f32x2){bflo(p2), bfhi(p2)};
        unsigned ap = pack2(a[0], a[1]);
        a = (f32x2){bflo(ap), bfhi(ap)} + (f32x2){bflo(p1), bfhi(p1)};
        ap = pack2(a[0], a[1]);
        a = (f32x2){bflo(ap), bfhi(ap)} + (f32x2){bflo(p0), bfhi(p0)};
        ap = pack2(a[0], a[1]);
        const float v0 = (c & 1) ? bfhi(vr[i][c >> 1]) : bflo(vr[i][c >> 1]);
        const float v1 = (c & 1) ? bfhi(vr[i + 1][c >> 1]) : bflo(vr[i + 1][c >> 1]);
        a = (f32x2){bflo(ap), bfhi(ap)} * (f32x2){v0, v1};
        o[i >> 1] = pack2(a[0], a[1]);
      }
      *(u32x2*)(sm + (ch * 8 + c) * 136 + rg * 8) = (u32x2){o[0], o[1]};
    }
    STAMP(0, 14);
    __syncthreads();
    STAMP(0, 15);
    const rsrc_t rs = ws_rsrc(ws);
#pragma unroll
    for (int i = 0; i < 4; ++i) {
      const int idx = t + 256 * i;
      const int dr = idx >> 3, c8 = idx & 7;
      const u32x2 a = *(const u32x2*)(sm + dr * 136 + c8 * 16);
      const u32x2 b = *(const u32x2*)(sm + dr * 136 + c8 * 16 + 8);
      u32x4 v = {a[0], a[1], b[0], b[1]};
      wst16<AUX_KVT>(rs, WS_KVT + (unsigned)((d0 + dr) * SEQ + s0 + c8 * 8) * 2, v);
    }
  }
  STAMP(0, 11);
#pragma unroll
  for (int i = 0; i < 4; ++i) asm volatile("" ::"v"(pf[i]));
  STAMP(0, 12);
}

#define CV_KVSTR 80
#define CV_HSTR 4160
#define CV_HBYTES (8 * CV_HSTR)
#define CV_REDSTR 36
#define CV_SMEM 40960

template <bool FIRST>
DI void attn_block(const char* __restrict__ ws, char* smem, int head, int qb) {
  const int l = threadIdx.x & 63, wv = threadIdx.x >> 6;
  const int r = l & 31, hh = l >> 5;
  const u16* Qh = (const u16*)(ws + WS_Q) + (size_t)head * SEQ * 16;
  const u16* Kh = (const u16*)(ws + WS_K) + (size_t)head * SEQ * 16;
  const u16* Vh = (const u16*)(ws + WS_VT) + (size_t)head * 16 * SEQ;
  u16* O = (u16*)(ws + WS_O);
  const int q0 = qb + 32 * wv;
  const int kt0 = FIRST ? (8 - wv) : 0;
  STAMP(1, 0);
  char* ks_ = smem;
  char* vs_ = smem + 16384;
  char* cs_ = smem + 32768;
  {
    const int key = l >> 1;
    const u16* ksrc = Kh + (size_t)key * 16 + (((l & 1) ^ ((key >> 3) & 1)) * 8);
    const int dd = l >> 2;
    const u16* vsrc = Vh + (size_t)dd * SEQ + (((l & 3) ^ ((dd >> 2) & 3)) * 8);
#pragma unroll
    for (int i = 0; i < 2; ++i) {
      const int u = wv + 8 * i;
      int key0 = qb - 256 + 32 * u;
      key0 = key0 < 0 ? 0 : key0;
      GLDS16A(ksrc + (size_t)key0 * 16, ks_ + u * 1024 + l * 16, AUX_LD2);
      GLDS16A(vsrc + key0, vs_ + u * 1024 + l * 16, AUX_LD2);
    }
  }
  const bf16x8 qf = __builtin_bit_cast(bf16x8, ld16<AUX_LD2 != 0>(Qh + (size_t)(q0 + r) * 16 + 8 * hh));
  if (threadIdx.x < 2) {
    const unsigned o = threadIdx.x == 0 ? 0x3f803f80u : 0u;
    *(u32x4*)(cs_ + threadIdx.x * 16) = (u32x4){o, o, o, o};
  }
  WAIT_V(0);
  RAWBAR();
  STAMP(1, 1);
  const char* krd = ks_ + (32 * wv + r) * 32 + ((hh ^ ((r >> 3) & 1)) * 16);
  f32x16 zero;
#pragma unroll
  for (int e = 0; e < 16; ++e) zero[e] = 0.f;
  const float NEG = -3.0e38f;
  auto mask = [&](int kt, f32x16& s) {
    if (kt == 0) {
#pragma unroll
      for (int e = 0; e < 16; ++e) s[e] = ((e & 3) + 8 * (e >> 2) + 4 * hh > r) ? s[e] : NEG;
    }
    if (kt == 8) {
#pragma unroll
      for (int e = 0; e < 16; ++e) s[e] = ((e & 3) + 8 * (e >> 2) + 4 * hh <= r) ? s[e] : NEG;
    }
  };
  float mx = NEG, mx2 = NEG;
  {
    bf16x8 kf[9];
#pragma unroll
    for (int kt = 0; kt < 9; ++kt) kf[kt] = *(const bf16x8*)(krd + kt * 1024);
    f32x16 s[3];
    s[0] = MFMA32(kf[0], qf, zero);
    s[1] = MFMA32(kf[1], qf, zero);
#pragma unroll
    for (int kt = 0; kt < 9; ++kt) {
      if (kt < 7) s[(kt + 2) % 3] = MFMA32(kf[kt + 2], qf, zero);
      f32x16& c = s[kt % 3];
      mask(kt, c);
      float m = NEG, m2 = NEG;
#pragma unroll
      for (int e = 0; e < 8; e += 2) {
        m = fmaxf(fmaxf(c[e], c[e + 1]), m);
        m2 = fmaxf(fmaxf(c[8 + e], c[9 + e]), m2);
      }
      if (FIRST) {
        m = (kt >= kt0) ? m : NEG;
        m2 = (kt >= kt0) ? m2 : NEG;
      }
      mx = fmaxf(mx, m);
      mx2 = fmaxf(mx2, m2);
    }
  }
  mx = fmaxf(mx, mx2);
  mx = fmaxf(mx, __shfl_xor(mx, 32));
  f32x16 negm;
#pragma unroll
  for (int e = 0; e < 16; ++e) negm[e] = -mx;
  STAMP(1, 2);
  const int vsw = (r >> 2) & 3;
  const bool isd = r < 16;
  const int cad = 32768 + ((r == 16 || r == 20) ? 0 : 16);
  int va0 = isd ? 16384 + wv * 1024 + r * 64 + ((hh ^ vsw) * 16) : cad;
  int va1 = isd ? 16384 + wv * 1024 + r * 64 + (((2 + hh) ^ vsw) * 16) : cad;
  const int vstep = isd ? 1024 : 0;
  f32x16 oacc = zero;
  {
    f32x16 s[2];
    bf16x8 kf = *(const bf16x8*)(krd);
    s[0] = MFMA32(kf, qf, negm);
#pragma unroll
    for (int kt = 0; kt < 9; ++kt) {
      const bf16x8 vf0 = *(const bf16x8*)(smem + va0);
      const bf16x8 vf1 = *(const bf16x8*)(smem + va1);
      va0 += vstep;
      va1 += vstep;
      if (kt < 8) {
        kf = *(const bf16x8*)(krd + (kt + 1) * 1024);
        s[(kt + 1) & 1] = MFMA32(kf, qf, negm);
      }
      f32x16& c = s[kt & 1];
      mask(kt, c);
      if (!FIRST || kt >= kt0) {
        u32x4 pp[2];
#pragma unroll
        for (int e = 0; e < 16; e += 2) {
          pp[e >> 3][(e >> 1) & 3] = pack2(__builtin_amdgcn_exp2f(c[e]), __builtin_amdgcn_exp2f(c[e + 1]));
        }
        oacc = MFMA32(vf0, __builtin_bit_cast(bf16x8, pp[0]), oacc);
        oacc = MFMA32(vf1, __builtin_bit_cast(bf16x8, pp[1]), oacc);
      }
    }
  }
  STAMP(1, 3);
  const float inv = 1.f / oacc[8];
  u32x2 v0 = {pack2(oacc[0] * inv, oacc[1] * inv), pack2(oacc[2] * inv, oacc[3] * inv)};
  u32x2 v1 = {pack2(oacc[4] * inv, oacc[5] * inv), pack2(oacc[6] * inv, oacc[7] * inv)};
  const rsrc_t rs = ws_rsrc(ws);
  const unsigned op = WS_O + (unsigned)((q0 + r) * DM + head * 16 + 4 * hh) * 2;
  wst8<AUX_ST2>(rs, op, v0);
  wst8<AUX_ST2>(rs, op + 16, v1);
  STAMP(1, 4);
}

DI void conv_block(const u16* __restrict__ ssmk, const u16* __restrict__ ssmD, char* __restrict__ ws, char* smem,
                   int d) {
  const int t = threadIdx.x & 255;
  const int l = t & 63, wv = t >> 6;
  const int r = l & 31, hh = l >> 5;
  char* hs = smem;
  char* kvs = smem + CV_HBYTES;
  const u16* kvT = (const u16*)(ws + WS_KVT) + (size_t)d * SEQ;
  const u16* hrow = ssmk + (size_t)d * SEQ;
  STAMP(1, 0);
  const u32x4 kvv = ld16<AUX_LD2 != 0>(kvT + 8 * t);
  const u32x4 whi = *(const u32x4*)(hrow + 8 * t);
  const u32x4 wlo0 = *(const u32x4*)(hrow + 8 * (t > 0 ? t - 1 : 0));
  const float Dd = bf2f(ssmD[d]);
  *(u32x4*)(kvs + ((t >> 2) + 32) * CV_KVSTR + (t & 3) * 16) = kvv;
  if (t < 128) {
    u32x4 z = {0u, 0u, 0u, 0u};
    *(u32x4*)(kvs + (t >> 2) * CV_KVSTR + (t & 3) * 16) = z;
  }
  {
    const int E = t;
    u32x4 wlo = wlo0;
    if (E == 0) wlo = (u32x4){0u, 0u, 0u, 0u};
    unsigned z[8];
#pragma unroll
    for (int i = 0; i < 4; ++i) {
      const unsigned a = whi[3 - i];
      z[i] = (a >> 16) | (a << 16);
      const unsigned b = wlo[3 - i];
      z[4 + i] = (b >> 16) | (b << 16);
    }
    const int M = 255 - E;
#pragma unroll
    for (int p = 0; p < 8; ++p) {
      const int o = 7 - p;
      u32x4 v;
      if ((o & 1) == 0) {
        v[0] = z[o / 2];
        v[1] = z[o / 2 + 1];
        v[2] = z[o / 2 + 2];
        v[3] = z[o / 2 + 3];
      } else {
        v[0] = (z[o / 2] >> 16) | (z[o / 2 + 1] << 16);
        v[1] = (z[o / 2 + 1] >> 16) | (z[o / 2 + 2] << 16);
        v[2] = (z[o / 2 + 2] >> 16) | (z[o / 2 + 3] << 16);
        v[3] = (z[o / 2 + 3] >> 16) | (z[o / 2 + 4] << 16);
      }
      *(u32x4*)(hs + p * CV_HSTR + 16 * M) = v;
    }
    if (t < 24) {
      u32x4 zz = {0u, 0u, 0u, 0u};
      *(u32x4*)(hs + (t & 7) * CV_HSTR + 16 * (256 + (t >> 3))) = zz;
    }
  }
  __syncthreads();
  STAMP(1, 1);
  f32x16 acc0, acc1, acc2;
#pragma unroll
  for (int e = 0; e < 16; ++e) {
    acc0[e] = 0.f;
    acc1[e] = 0.f;
    acc2[e] = 0.f;
  }
  const char* abase = hs + (r & 7) * CV_HSTR + 16 * (255 - (r >> 3) + hh) - 64 * wv;
  const char* bbase = kvs + (r + 32 - wv) * CV_KVSTR + 16 * hh;
  {
    bf16x8 fa[2][2], fb[2][2];
    auto ldc = [&](int step, bf16x8(&a)[2], bf16x8(&b)[2]) {
      const int i = step >> 1, ks = step & 1;
      a[0] = *(const bf16x8*)(abase - 256 * i + 32 * ks);
      a[1] = *(const bf16x8*)(abase - 256 * i - 2048 + 32 * ks);
      b[1] = *(const bf16x8*)(bbase + (32 - 4 * i) * CV_KVSTR + 32 * ks);
      b[0] = *(const bf16x8*)(bbase - (4 * i) * CV_KVSTR + 32 * ks);
    };
    ldc(0, fa[0], fb[0]);
#pragma unroll
    for (int step = 0; step < 16; ++step) {
      if (step < 15) ldc(step + 1, fa[(step + 1) & 1], fb[(step + 1) & 1]);
      __builtin_amdgcn_sched_barrier(0);
      acc1 = MFMA32(fa[step & 1][0], fb[step & 1][1], acc1);
      acc0 = MFMA32(fa[step & 1][0], fb[step & 1][0], acc0);
      acc2 = MFMA32(fa[step & 1][1], fb[step & 1][0], acc2);
      __builtin_amdgcn_sched_barrier(0);
    }
  }
  acc1 += acc2;
  STAMP(1, 2);
  __syncthreads();
  STAMP(1, 6);
  float* red = (float*)smem;
  {
    float* rw = red + wv * (64 * CV_REDSTR);
#pragma unroll
    for (int g = 0; g < 4; ++g) {
      f32x4 v0 = {acc0[4 * g], acc0[4 * g + 1], acc0[4 * g + 2], acc0[4 * g + 3]};
      f32x4 v1 = {acc1[4 * g], acc1[4 * g + 1], acc1[4 * g + 2], acc1[4 * g + 3]};
      *(f32x4*)(rw + r * CV_REDSTR + 8 * g + 4 * hh) = v0;
      *(f32x4*)(rw + (32 + r) * CV_REDSTR + 8 * g + 4 * hh) = v1;
    }
  }
  __syncthreads();
  STAMP(1, 7);
  {
    const int a = t >> 2, b0 = 8 * (t & 3);
    f32x4 s0 = {0.f, 0.f, 0.f, 0.f}, s1 = {0.f, 0.f, 0.f, 0.f};
#pragma unroll
    for (int w = 0; w < 4; ++w) {
      const float* rp = red + w * (64 * CV_REDSTR) + a * CV_REDSTR + b0;
      s0 += *(const f32x4*)(rp);
      s1 += *(const f32x4*)(rp + 4);
    }
    float yy[8] = {s0[0], s0[1], s0[2], s0[3], s1[0], s1[1], s1[2], s1[3]};
    u32x4 o;
#pragma unroll
    for (int i = 0; i < 4; ++i) {
      const float y0 = rbf(yy[2 * i]) + rbf(bflo(kvv[i]) * Dd);
      const float y1 = rbf(yy[2 * i + 1]) + rbf(bfhi(kvv[i]) * Dd);
      o[i] = pack2(y0, y1);
    }
    wst16<AUX_Y2T>(ws_rsrc(ws), WS_Y2T + (unsigned)(d * SEQ + 8 * t) * 2, o);
  }
  STAMP(1, 4);
}

__global__ __launch_bounds__(512, 4) void k2_kernel(const u16* __restrict__ ssmk, const u16* __restrict__ ssmD,
                                                    const u16* __restrict__ wo, const u16* __restrict__ x,
                                                    const u16* __restrict__ gatew, char* __restrict__ ws) {
  __shared__ __attribute__((aligned(16))) char smem[2 * CV_SMEM];
  const int xcd = blockIdx.x & 7, bidx = (blockIdx.x >> 3) ^ 32;
  if (bidx >= 32) {
    const u32x4 pf0 = *(const u32x4*)(wo + (size_t)(bidx - 32) * 8192 + threadIdx.x * 8);
    const u32x4 pf1 = *(const u32x4*)(wo + (size_t)(bidx - 32) * 8192 + 4096 + threadIdx.x * 8);
    const int half = threadIdx.x >> 8;
    conv_block(ssmk, ssmD, ws, smem + half * CV_SMEM, xcd * 64 + (bidx - 32) * 2 + half);
    {
      const int l = threadIdx.x & 63;
      const int row = (xcd * 32 + (bidx - 32)) * 8 + (threadIdx.x >> 6);
      const u32x4 gw = *(const u32x4*)(gatew + l * 8);
      const u32x4 xv = ld16<false>(x + (size_t)row * DM + l * 8);
      float sum = 0.f;
#pragma unroll
      for (int i = 0; i < 4; ++i) {
        const unsigned pr = pack2(bflo(xv[i]) * bflo(gw[i]), bfhi(xv[i]) * bfhi(gw[i]));
        sum += bflo(pr);
        sum += bfhi(pr);
      }
#pragma unroll
      for (int o = 32; o >= 1; o >>= 1) sum += __shfl_xor(sum, o);
      if (l == 0) ((float*)(ws + WS_G))[row] = rbf(1.f / (1.f + __expf(-rbf(sum))));
    }
    asm volatile("" ::"v"(pf0));
    asm volatile("" ::"v"(pf1));
  } else {
    if (xcd == 0)
      attn_block<true>(ws, smem, bidx, 0);
    else
      attn_block<false>(ws, smem, bidx, xcd * 256);
  }
}

#define K3_STAGE 32768
#define K3_CSTR 68
#define K3_YSTR 136

DI void k3_body(const u16* __restrict__ x, const u16* __restrict__ wo, const char* __restrict__ ws,
                u16* __restrict__ out, char* smem) {
  const int t = threadIdx.x;
  const int l = t & 63, wv = t >> 6;
  const int r = l & 31, hh = l >> 5;
  const int wm = wv & 1, wn = (wv >> 1) & 1, kh = wv >> 2;
  const int tm = (blockIdx.x & 7) * 4 + ((blockIdx.x >> 3) & 3), tn = blockIdx.x >> 5;
  const int m0 = tm * 64, n0 = tn * 64;
  const u16* o = (const u16*)(ws + WS_O);
  STAMP(2, 0);

  const u16* y2T = (const u16*)(ws + WS_Y2T);
  const float* gp = (const float*)(ws + WS_G);
  const int rr = t >> 3, ch = t & 7;
  const u32x4 yv = ld16<AUX_LD3 != 0>(y2T + (size_t)(n0 + rr) * SEQ + m0 + ch * 8);
  const u32x4 xq = ld16<false>(x + (size_t)(m0 + rr) * DM + n0 + ch * 8);
  const float g = gp[m0 + rr];
  __builtin_amdgcn_sched_barrier(0);
  {
    char* ldsw = smem + wv * 2048 + l * 16;
#pragma unroll
    for (int s4 = 0; s4 < 4; ++s4) {
      const int st = s4;
#pragma unroll
      for (int i = 0; i < 2; ++i) {
        const int ar = 8 * wv + 4 * i + (l >> 4);
        GLDS16A(o + (size_t)(m0 + ar) * DM + st * 128 + (((l & 15) ^ (ar & 15)) * 8), ldsw + st * K3_STAGE + i * 1024,
                AUX_LD3);
      }
#pragma unroll
      for (int i = 0; i < 2; ++i) {
        const int br = 16 * wv + 8 * i + (l >> 3);
        GLDS16(wo + (size_t)(st * 128 + br) * DM + n0 + (((l & 7) ^ (((br >> 1) & 1) << 2)) * 8),
               ldsw + st * K3_STAGE + 16384 + i * 1024);
      }
    }
  }

  f32x16 acc;
#pragma unroll
  for (int e = 0; e < 16; ++e) acc[e] = 0.f;
  const int blk = (l >> 4) & 1, q4 = (l & 15) >> 2, p4 = l & 3;
  const int arow = wm * 32 + r;
  const int a_off = arow * 256;
  const int a_sw = arow & 15;
  const int b_off = 16384 + (8 * hh + q4) * 128 + (((wn * 4 + 2 * blk + (p4 >> 1)) ^ (((q4 >> 1) & 1) << 2)) * 16) +
                    (p4 & 1) * 8;
  const int a_x = (8 * kh + hh) ^ a_sw;
  const int b_k = b_off + 64 * kh * 128;
#pragma unroll
  for (int st = 0; st < 4; ++st) {
    if (st == 0) WAIT_V(12);
    else if (st == 1) WAIT_V(8);
    else if (st == 2) WAIT_V(4);
    else WAIT_V(0);
    RAWBAR();
    const char* sb = smem + st * K3_STAGE;
    bf16x8 af[4], bfr[4];
#pragma unroll
    for (int ks = 0; ks < 4; ++ks) {
      af[ks] = *(const bf16x8*)(sb + a_off + ((a_x ^ (2 * ks)) * 16));
      s16x4 lo = tr_read(sb + b_k + (16 * ks) * 128);
      s16x4 hi = tr_read(sb + b_k + (16 * ks + 4) * 128);
      bfr[ks] = cat_frag(lo, hi);
    }
    __builtin_amdgcn_sched_barrier(0);
#pragma unroll
    for (int ks = 0; ks < 4; ++ks) acc = MFMA32(af[ks], bfr[ks], acc);
    __builtin_amdgcn_sched_barrier(0);
  }
  __syncthreads();
  STAMP(2, 5);
  float* Cs = (float*)smem;
  char* Ys = smem + 2 * 64 * K3_CSTR * 4;
#pragma unroll
  for (int e = 0; e < 16; ++e) {
    const int row = wm * 32 + (e & 3) + 8 * (e >> 2) + 4 * hh;
    Cs[kh * 64 * K3_CSTR + row * K3_CSTR + wn * 32 + r] = acc[e];
  }
  {
    u32x2 a = {yv[0], yv[1]}, b = {yv[2], yv[3]};
    *(u32x2*)(Ys + rr * K3_YSTR + ch * 16) = a;
    *(u32x2*)(Ys + rr * K3_YSTR + ch * 16 + 8) = b;
  }
  __syncthreads();
  {
    const int row = rr;
    const int s = m0 + row;
    const float omg = rbf(1.f - g);
    const f32x4 c0 = *(const f32x4*)(Cs + row * K3_CSTR + ch * 8) + *(const f32x4*)(Cs + (64 + row) * K3_CSTR + ch * 8);
    const f32x4 c1 =
        *(const f32x4*)(Cs + row * K3_CSTR + ch * 8 + 4) + *(const f32x4*)(Cs + (64 + row) * K3_CSTR + ch * 8 + 4);
    float bb[8] = {c0[0], c0[1], c0[2], c0[3], c1[0], c1[1], c1[2], c1[3]};
    float res[8];
#pragma unroll
    for (int j = 0; j < 8; ++j) {
      const float y2 = bf2f(*(const u16*)(Ys + (ch * 8 + j) * K3_YSTR + row * 2));
      const float qv = (j & 1) ? bfhi(xq[j >> 1]) : bflo(xq[j >> 1]);
      const float a = rbf(qv * y2);
      const float ga = rbf(g * a);
      const float gb = rbf(omg * rbf(bb[j]));
      res[j] = ga + gb;
    }
    u32x4 ov;
    ov[0] = pack2(res[0], res[1]);
    ov[1] = pack2(res[2], res[3]);
    ov[2] = pack2(res[4], res[5]);
    ov[3] = pack2(res[6], res[7]);
    *(u32x4*)(out + (size_t)s * DM + n0 + ch * 8) = ov;
  }
  STAMP(2, 6);
}

__global__ __launch_bounds__(512) void k3_kernel(const u16* __restrict__ x, const u16* __restrict__ wo,
                                                 const char* __restrict__ ws, u16* __restrict__ out) {
  __shared__ __attribute__((aligned(16))) char smem[4 * K3_STAGE];
  k3_body(x, wo, ws, out, smem);
}

extern "C" void kernel_launch(void* const* d_in, const int* in_sizes, int n_in, void* d_out, int out_size,
                              void* d_ws, size_t ws_size, hipStream_t stream) {
  const u16* A_q = (const u16*)d_in[0];
  const u16* A_k = (const u16*)d_in[1];
  const u16* A_v = (const u16*)d_in[2];
  const u16* A_shift = (const u16*)d_in[3];
  const u16* A_ssmk = (const u16*)d_in[4];
  const u16* A_ssmD = (const u16*)d_in[5];
  const u16* B_wqkv = (const u16*)d_in[7];
  const u16* B_wo = (const u16*)d_in[8];
  const u16* gate_w = (const u16*)d_in[9];
  char* ws = (char*)d_ws;
  k1_kernel<<<256, 512, 0, stream>>>(A_q, A_k, A_v, A_shift, B_wqkv, A_ssmk, ws);
  k2_kernel<<<512, 512, 0, stream>>>(A_ssmk, A_ssmD, B_wo, A_q, gate_w, ws);
  k3_kernel<<<256, 512, 0, stream>>>(A_q, B_wo, ws, (u16*)d_out);
}
```

```cpp
#define ATT_MAP
#include <hip/hip_runtime.h>
#include <hip/hip_bf16.h>
#include <stdint.h>

#define DI __device__ __forceinline__
typedef __attribute__((ext_vector_type(8))) __bf16 bf16x8;
typedef __attribute__((ext_vector_type(4))) __bf16 bf16x4;
typedef __attribute__((ext_vector_type(2))) __bf16 bf16x2;
typedef __attribute__((ext_vector_type(4))) short s16x4;
typedef __attribute__((ext_vector_type(16))) float f32x16;
typedef __attribute__((ext_vector_type(4))) float f32x4;
typedef __attribute__((ext_vector_type(2))) float f32x2;
typedef __attribute__((ext_vector_type(4))) unsigned u32x4;
typedef __attribute__((ext_vector_type(2))) unsigned u32x2;
typedef unsigned short u16;

#define SEQ 2048
#define DM 512

#define MFMA32(a, b, c) __builtin_amdgcn_mfma_f32_32x32x16_bf16((a), (b), (c), 0, 0, 0)

DI float bf2f(u16 v) { return __uint_as_float(((unsigned)v) << 16); }
DI float bflo(unsigned v) { return __uint_as_float(v << 16); }
DI float bfhi(unsigned v) { return __uint_as_float(v & 0xffff0000u); }
DI unsigned pack2(float a, float b) {
  f32x2 f = {a, b};
  bf16x2 r = __builtin_convertvector(f, bf16x2);
  return __builtin_bit_cast(unsigned, r);
}
DI float rbf(float a) {
  return (float)((__bf16)a);
}
DI s16x4 tr_read(const char* p) {
  typedef __attribute__((address_space(3))) s16x4* lp;
  return __builtin_amdgcn_ds_read_tr16_b64_v4i16((lp)(p));
}
DI bf16x8 cat_frag(s16x4 lo, s16x4 hi) {
  typedef __attribute__((ext_vector_type(8))) short s16x8;
  s16x8 v = __builtin_shufflevector(lo, hi, 0, 1, 2, 3, 4, 5, 6, 7);
  return __builtin_bit_cast(bf16x8, v);
}

#define STAMP(k, i) do {} while (0)
#define WS_Q 0
#define WS_K (2u << 20)
#define WS_VT (4u << 20)
#define WS_KVT (6u << 20)
#define WS_Y2T (8u << 20)
#define WS_O (10u << 20)
#define WS_G (12u << 20)

#define WT_AUX 0
typedef __amdgpu_buffer_rsrc_t rsrc_t;
DI rsrc_t ws_rsrc(const char* ws) { return __builtin_amdgcn_make_buffer_rsrc((void*)ws, (short)0, 16 << 20, 0x00020000); }
#define AUX_ST1 0
#define AUX_ST2 0
#define AUX_KVT 16
#define AUX_Y2T 16
#define NT_KV
#define PF_LATE 1
#define AUX_LD2 0
#define AUX_LD3 0
template <int AUX>
DI void wst16(rsrc_t rs, unsigned off, u32x4 v) { __builtin_amdgcn_raw_buffer_store_b128(v, rs, (int)off, 0, AUX); }
template <int AUX>
DI void wst8(rsrc_t rs, unsigned off, u32x2 v) { __builtin_amdgcn_raw_buffer_store_b64(v, rs, (int)off, 0, AUX); }
template <bool NT>
DI u32x4 ld16(const void* p) {
  if constexpr (NT) return __builtin_nontemporal_load((const u32x4*)p);
  else return *(const u32x4*)p;
}

#define GLDS16(g, l) __builtin_amdgcn_global_load_lds((const unsigned*)(g), (unsigned*)(l), 16, 0, 0)
#define GLDS16A(g, l, aux) __builtin_amdgcn_global_load_lds((const unsigned*)(g), (unsigned*)(l), 16, 0, aux)
#define AUX_X 2
#define AUX_W 0
#define WAIT_V(n) asm volatile("s_waitcnt vmcnt(" #n ")" ::: "memory")
#define RAWBAR() do { asm volatile("s_waitcnt lgkmcnt(0)" ::: "memory"); __builtin_amdgcn_s_barrier(); } while (0)
#define K1_STAGE 32768

template <int N>
DI void wait_vm() {
  if constexpr (N == 0) WAIT_V(0);
  else if constexpr (N == 4) WAIT_V(4);
  else if constexpr (N == 8) WAIT_V(8);
  else if constexpr (N == 12) WAIT_V(12);
  else if constexpr (N == 16) WAIT_V(16);
  else if constexpr (N == 24) WAIT_V(24);
}

#define QSCALE (0.25f * 1.44269504088896340736f)

template <int MODE>
DI void k1_gemm(const u16* __restrict__ x, const u16* __restrict__ w, char* __restrict__ ws, char* smem, int tm,
                int tn) {
  const int t = threadIdx.x;
  const int l = t & 63, wv = t >> 6;
  const int r = l & 31, hh = l >> 5;
  const int wm = wv & 1, wn = wv >> 1;
  const int m0 = tm * 128, n0 = tn * 128;

  const u16* xg[2];
  const u16* wg[2];
#pragma unroll
  for (int i = 0; i < 2; ++i) {
    const int ar = 16 * wv + 8 * i + (l >> 3);
    xg[i] = x + (size_t)(m0 + ar) * DM + (((l & 7) ^ ((ar >> 1) & 7)) * 8);
    const int br = 8 * wv + 4 * i + (l >> 4);
    wg[i] = w + (size_t)br * 1536 + n0 + (((l & 15) ^ ((br & 3) << 2)) * 8);
  }
  char* ldsw = smem + wv * 2048 + l * 16;

  const int rot = 0;
  auto issue = [&](int kt) {
    char* st = ldsw + (kt & 3) * K1_STAGE;
    const int kk = (kt + rot) & 7;
#pragma unroll
    for (int i = 0; i < 2; ++i) GLDS16A(xg[i] + kk * 64, st + i * 1024, AUX_X);
#pragma unroll
    for (int i = 0; i < 2; ++i) GLDS16A(wg[i] + (size_t)kk * 64 * 1536, st + 16384 + i * 1024, AUX_W);
  };

  f32x16 acc[2];
#pragma unroll
  for (int i = 0; i < 2; ++i)
#pragma unroll
    for (int e = 0; e < 16; ++e) acc[i][e] = 0.f;

  const int blk = (l >> 4) & 1, q4 = (l & 15) >> 2, p4 = l & 3;
  const int a_row_off = (wm * 64 + r) * 128;
  const int a_sw = (r >> 1) & 7;
  const int b_base = 16384 + (8 * hh + q4) * 256 + (p4 & 1) * 8 + (((wn * 4 + 2 * blk + (p4 >> 1)) ^ (q4 << 2)) * 16);

  STAMP(0, 0);
  issue(0);
  issue(1);
  issue(2);
  issue(3);
  bf16x8 fa[2][4][2], fb[2][4];
  auto ldfr = [&](int kt, bf16x8(&a)[4][2], bf16x8(&b)[4]) {
    const char* st = smem + (kt & 3) * K1_STAGE;
#pragma unroll
    for (int ks = 0; ks < 4; ++ks) {
#pragma unroll
      for (int i = 0; i < 2; ++i)
        a[ks][i] = *(const bf16x8*)(st + a_row_off + i * 32 * 128 + (((2 * ks + hh) ^ a_sw) * 16));
      s16x4 lo = tr_read(st + b_base + (16 * ks) * 256);
      s16x4 hi = tr_read(st + b_base + (16 * ks + 4) * 256);
      b[ks] = cat_frag(lo, hi);
    }
  };
  wait_vm<12>();
  RAWBAR();
  ldfr(0, fa[0], fb[0]);
#define K1_ILV 2
#pragma unroll
  for (int kt = 0; kt < 8; ++kt) {
    const char* stn = smem + ((kt + 1) & 3) * K1_STAGE;
    bf16x8(&an)[4][2] = fa[(kt + 1) & 1];
    bf16x8(&bn)[4] = fb[(kt + 1) & 1];
    s16x4 lo, hi;
    auto rdpair = [&](int j) {
      const int ks = j >> 1, i = j & 1;
      an[ks][i] = *(const bf16x8*)(stn + a_row_off + i * 32 * 128 + (((2 * ks + hh) ^ a_sw) * 16));
      if (i == 0) {
        lo = tr_read(stn + b_base + (16 * ks) * 256);
      } else {
        hi = tr_read(stn + b_base + (16 * ks + 4) * 256);
        bn[ks] = cat_frag(lo, hi);
      }
    };
    if (kt < 7) {
      if (kt <= 4) wait_vm<8>();
      else if (kt == 5) wait_vm<4>();
      else wait_vm<0>();
      RAWBAR();
    }
    __builtin_amdgcn_sched_barrier(0);
#pragma unroll
    for (int g = 0; g < 8; ++g) {
      const int ks = g >> 1, i = g & 1;
      if (MODE == 2)
        acc[i] = MFMA32(fa[kt & 1][ks][i], fb[kt & 1][ks], acc[i]);
      else
        acc[i] = MFMA32(fb[kt & 1][ks], fa[kt & 1][ks][i], acc[i]);
      __builtin_amdgcn_sched_barrier(0);
      if (kt < 7) {
        if (g < 2) {
          rdpair(2 * g);
          rdpair(2 * g + 1);
        } else if (g < 6) {
          rdpair(g + 2);
        }
      }
      if (i == 1 && kt + 4 <= 7) {
        char* st4 = ldsw + (kt & 3) * K1_STAGE;
        const int kk = (kt + 4 + rot) & 7;
        if (ks < 2)
          GLDS16A(xg[ks] + kk * 64, st4 + ks * 1024, AUX_X);
        else
          GLDS16A(wg[ks - 2] + (size_t)kk * 64 * 1536, st4 + 16384 + (ks - 2) * 1024, AUX_W);
      }
      __builtin_amdgcn_sched_barrier(0);
    }
  }
  STAMP(0, 9);
  const rsrc_t rs = ws_rsrc(ws);
  if (MODE == 2) {
#pragma unroll
    for (int i = 0; i < 2; ++i) {
      const int n = (n0 - 1024) + wn * 32 + r;
      const int tok0 = m0 + wm * 64 + i * 32;
      const unsigned dst = WS_VT + (unsigned)(n * SEQ + tok0 + 8 * hh) * 2;
#pragma unroll
      for (int bb = 0; bb < 2; ++bb) {
        u32x4 v;
        v[0] = pack2(acc[i][8 * bb + 0], acc[i][8 * bb + 1]);
        v[1] = pack2(acc[i][8 * bb + 2], acc[i][8 * bb + 3]);
        v[2] = pack2(acc[i][8 * bb + 4], acc[i][8 * bb + 5]);
        v[3] = pack2(acc[i][8 * bb + 6], acc[i][8 * bb + 7]);
        wst16<AUX_ST1>(rs, dst + 32 * bb, v);
      }
    }
  } else {
    const unsigned base = (MODE == 1 ? WS_K : WS_Q);
    const int nn0 = n0 & 511;
#pragma unroll
    for (int i = 0; i < 2; ++i) {
      const int tok = m0 + wm * 64 + i * 32 + r;
      const int nb = nn0 + wn * 32;
      const int head = nb >> 4;
#pragma unroll
      for (int g = 0; g < 4; ++g) {
        u32x2 v;
        const float sc = (MODE == 0) ? QSCALE : 1.f;
        v[0] = pack2(acc[i][4 * g + 0] * sc, acc[i][4 * g + 1] * sc);
        v[1] = pack2(acc[i][4 * g + 2] * sc, acc[i][4 * g + 3] * sc);
        wst8<AUX_ST1>(rs, base + (unsigned)(((head + (g >> 1)) * SEQ + tok) * 16 + 8 * (g & 1) + 4 * hh) * 2, v);
      }
    }
  }
  STAMP(0, 10);
}

__global__ __launch_bounds__(512) void k1_kernel(const u16* __restrict__ x, const u16* __restrict__ kin,
                                                 const u16* __restrict__ vin, const u16* __restrict__ shiftk,
                                                 const u16* __restrict__ wqkv, const u16* __restrict__ ssmk,
                                                 char* __restrict__ ws) {
  __shared__ __attribute__((aligned(16))) char smem[4 * K1_STAGE];
  const int xcd = blockIdx.x & 7, bidx = blockIdx.x >> 3;
  if (bidx < 24) {
    const int tm = 2 * xcd + (bidx & 1), tn = bidx >> 1;
    if (tn >= 8)
      k1_gemm<2>(x, wqkv, ws, smem, tm, tn);
    else if (tn >= 4)
      k1_gemm<1>(x, wqkv, ws, smem, tm, tn);
    else
      k1_gemm<0>(x, wqkv, ws, smem, tm, tn);
    return;
  }
  const int l = threadIdx.x & 63, wv = threadIdx.x >> 6;
  const int t = threadIdx.x & 255, h2 = threadIdx.x >> 8;
  const int pb = xcd * 8 + (bidx - 24);
  STAMP(0, 0);
  u32x4 pf[4];
  {
    const int ch = t & 15, rg = t >> 4;
    u32x4 kr[7], vr[4], sk[4];
    const int tile = pb * 2 + h2;
    const int s0 = (tile >> 2) * 64, d0 = (tile & 3) * 128;
    {
      const int sb = s0 + rg * 4;
      const int dd = d0 + ch * 8;
#pragma unroll
      for (int i = 0; i < 7; ++i) {
        const int s = sb - 3 + i;
        u32x4 z = {0u, 0u, 0u, 0u};
        kr[i] = (s >= 0) ? __builtin_nontemporal_load((const u32x4*)(kin + (size_t)s * DM + dd)) : z;
      }
#pragma unroll
      for (int i = 0; i < 4; ++i) vr[i] = __builtin_nontemporal_load((const u32x4*)(vin + (size_t)(sb + i) * DM + dd));
#pragma unroll
      for (int i = 0; i < 4; ++i) sk[i] = *(const u32x4*)(shiftk + (size_t)dd * 4 + i * 8);
    }
    __builtin_amdgcn_sched_barrier(0);
    __builtin_amdgcn_sched_barrier(0);
    char* sm = smem + h2 * 17408;
#pragma unroll
    for (int c = 0; c < 4; ++c) {
      const f32x2 w0 = {bflo(sk[c][0]), bflo(sk[c][2])}, w1 = {bfhi(sk[c][0]), bfhi(sk[c][2])};
      const f32x2 w2 = {bflo(sk[c][1]), bflo(sk[c][3])}, w3 = {bfhi(sk[c][1]), bfhi(sk[c][3])};
      f32x2 kk[7];
#pragma unroll
      for (int i = 0; i < 7; ++i) kk[i] = (f32x2){bflo(kr[i][c]), bfhi(kr[i][c])};
      f32x2 r[4];
#pragma unroll
      for (int i = 0; i < 4; ++i) {
        f32x2 a = kk[i] * w3;
        a = __builtin_elementwise_fma(kk[i + 1], w2, a);
        a = __builtin_elementwise_fma(kk[i + 2], w1, a);
        a = __builtin_elementwise_fma(kk[i + 3], w0, a);
        const unsigned ap = pack2(a[0], a[1]);
        r[i] = (f32x2){bflo(ap), bfhi(ap)} * (f32x2){bflo(vr[i][c]), bfhi(vr[i][c])};
      }
      *(u32x2*)(sm + (ch * 8 + 2 * c) * 136 + rg * 8) = (u32x2){pack2(r[0][0], r[1][0]), pack2(r[2][0], r[3][0])};
      *(u32x2*)(sm + (ch * 8 + 2 * c + 1) * 136 + rg * 8) = (u32x2){pack2(r[0][1], r[1][1]), pack2(r[2][1], r[3][1])};
    }
    STAMP(0, 14);
    __builtin_amdgcn_sched_barrier(0);
#pragma unroll
    for (int i = 0; i < 4; ++i) pf[i] = *(const u32x4*)(ssmk + (size_t)pb * 16384 + (i * 512 + threadIdx.x) * 8);
    __builtin_amdgcn_sched_barrier(0);
    RAWBAR();
    STAMP(0, 15);
    const rsrc_t rs = ws_rsrc(ws);
#pragma unroll
    for (int i = 0; i < 4; ++i) {
      const int idx = t + 256 * i;
      const int dr = idx >> 3, c8 = idx & 7;
      const u32x2 a = *(const u32x2*)(sm + dr * 136 + c8 * 16);
      const u32x2 b = *(const u32x2*)(sm + dr * 136 + c8 * 16 + 8);
      u32x4 v = {a[0], a[1], b[0], b[1]};
      wst16<AUX_KVT>(rs, WS_KVT + (unsigned)((d0 + dr) * SEQ + s0 + c8 * 8) * 2, v);
    }
  }
  STAMP(0, 11);
#pragma unroll
  for (int i = 0; i < 4; ++i) asm volatile("" ::"v"(pf[i]));
  STAMP(0, 12);
}

#define CV_KVSTR 80
#define CV_HSTR 4160
#define CV_HBYTES (8 * CV_HSTR)
#define CV_REDSTR 36
#define CV_SMEM 40960

template <bool FIRST>
DI void attn_block(const char* __restrict__ ws, char* smem, int head, int qb) {
  const int l = threadIdx.x & 63, wv = threadIdx.x >> 6;
  const int r = l & 31, hh = l >> 5;
  const u16* Qh = (const u16*)(ws + WS_Q) + (size_t)head * SEQ * 16;
  const u16* Kh = (const u16*)(ws + WS_K) + (size_t)head * SEQ * 16;
  const u16* Vh = (const u16*)(ws + WS_VT) + (size_t)head * 16 * SEQ;
  u16* O = (u16*)(ws + WS_O);
  const int q0 = qb + 32 * wv;
  const int kt0 = FIRST ? (8 - wv) : 0;
  STAMP(1, 0);
  char* ks_ = smem;
  char* vs_ = smem + 16384;
  char* cs_ = smem + 32768;
  {
    const int key = l >> 1;
    const u16* ksrc = Kh + (size_t)key * 16 + (((l & 1) ^ ((key >> 3) & 1)) * 8);
    const int dd = l >> 2;
    const u16* vsrc = Vh + (size_t)dd * SEQ + (((l & 3) ^ ((dd >> 2) & 3)) * 8);
#pragma unroll
    for (int i = 0; i < 2; ++i) {
      const int u = wv + 8 * i;
      int key0 = qb - 256 + 32 * u;
      key0 = key0 < 0 ? 0 : key0;
      GLDS16A(ksrc + (size_t)key0 * 16, ks_ + u * 1024 + l * 16, AUX_LD2);
      GLDS16A(vsrc + key0, vs_ + u * 1024 + l * 16, AUX_LD2);
    }
  }
  const bf16x8 qf = __builtin_bit_cast(bf16x8, ld16<AUX_LD2 != 0>(Qh + (size_t)(q0 + r) * 16 + 8 * hh));
  if (threadIdx.x < 2) {
    const unsigned o = threadIdx.x == 0 ? 0x3f803f80u : 0u;
    *(u32x4*)(cs_ + threadIdx.x * 16) = (u32x4){o, o, o, o};
  }
  WAIT_V(0);
  RAWBAR();
  STAMP(1, 1);
  const char* krd = ks_ + (32 * wv + r) * 32 + ((hh ^ ((r >> 3) & 1)) * 16);
  f32x16 zero;
#pragma unroll
  for (int e = 0; e < 16; ++e) zero[e] = 0.f;
  const float NEG = -3.0e38f;
  auto mask = [&](int kt, f32x16& s) {
    if (kt == 0) {
#pragma unroll
      for (int e = 0; e < 16; ++e) s[e] = ((e & 3) + 8 * (e >> 2) + 4 * hh > r) ? s[e] : NEG;
    }
    if (kt == 8) {
#pragma unroll
      for (int e = 0; e < 16; ++e) s[e] = ((e & 3) + 8 * (e >> 2) + 4 * hh <= r) ? s[e] : NEG;
    }
  };
  float mx = NEG, mx2 = NEG;
  {
    bf16x8 kf[9];
#pragma unroll
    for (int kt = 0; kt < 9; ++kt) kf[kt] = *(const bf16x8*)(krd + kt * 1024);
    f32x16 s[3];
    s[0] = MFMA32(kf[0], qf, zero);
    s[1] = MFMA32(kf[1], qf, zero);
#pragma unroll
    for (int kt = 0; kt < 9; ++kt) {
      if (kt < 7) s[(kt + 2) % 3] = MFMA32(kf[kt + 2], qf, zero);
      f32x16& c = s[kt % 3];
      mask(kt, c);
      float m = NEG, m2 = NEG;
#pragma unroll
      for (int e = 0; e < 8; e += 2) {
        m = fmaxf(fmaxf(c[e], c[e + 1]), m);
        m2 = fmaxf(fmaxf(c[8 + e], c[9 + e]), m2);
      }
      if (FIRST) {
        m = (kt >= kt0) ? m : NEG;
        m2 = (kt >= kt0) ? m2 : NEG;
      }
      mx = fmaxf(mx, m);
      mx2 = fmaxf(mx2, m2);
    }
  }
  mx = fmaxf(mx, mx2);
  mx = fmaxf(mx, __shfl_xor(mx, 32));
  f32x16 negm;
#pragma unroll
  for (int e = 0; e < 16; ++e) negm[e] = -mx;
  STAMP(1, 2);
  const int vsw = (r >> 2) & 3;
  const bool isd = r < 16;
  const int cad = 32768 + ((r == 16 || r == 20) ? 0 : 16);
  int va0 = isd ? 16384 + wv * 1024 + r * 64 + ((hh ^ vsw) * 16) : cad;
  int va1 = isd ? 16384 + wv * 1024 + r * 64 + (((2 + hh) ^ vsw) * 16) : cad;
  const int vstep = isd ? 1024 : 0;
  f32x16 oacc = zero;
  {
    f32x16 s[2];
    bf16x8 kf = *(const bf16x8*)(krd);
    s[0] = MFMA32(kf, qf, negm);
#pragma unroll
    for (int kt = 0; kt < 9; ++kt) {
      const bf16x8 vf0 = *(const bf16x8*)(smem + va0);
      const bf16x8 vf1 = *(const bf16x8*)(smem + va1);
      va0 += vstep;
      va1 += vstep;
      if (kt < 8) {
        kf = *(const bf16x8*)(krd + (kt + 1) * 1024);
        s[(kt + 1) & 1] = MFMA32(kf, qf, negm);
      }
      f32x16& c = s[kt & 1];
      mask(kt, c);
      if (!FIRST || kt >= kt0) {
        u32x4 pp[2];
#pragma unroll
        for (int e = 0; e < 16; e += 2) {
          pp[e >> 3][(e >> 1) & 3] = pack2(__builtin_amdgcn_exp2f(c[e]), __builtin_amdgcn_exp2f(c[e + 1]));
        }
        oacc = MFMA32(vf0, __builtin_bit_cast(bf16x8, pp[0]), oacc);
        oacc = MFMA32(vf1, __builtin_bit_cast(bf16x8, pp[1]), oacc);
      }
    }
  }
  STAMP(1, 3);
  const float inv = 1.f / oacc[8];
  u32x2 v0 = {pack2(oacc[0] * inv, oacc[1] * inv), pack2(oacc[2] * inv, oacc[3] * inv)};
  u32x2 v1 = {pack2(oacc[4] * inv, oacc[5] * inv), pack2(oacc[6] * inv, oacc[7] * inv)};
  const rsrc_t rs = ws_rsrc(ws);
  const unsigned op = WS_O + (unsigned)((q0 + r) * DM + head * 16 + 4 * hh) * 2;
  wst8<AUX_ST2>(rs, op, v0);
  wst8<AUX_ST2>(rs, op + 16, v1);
  STAMP(1, 4);
}

DI void conv_block(const u16* __restrict__ ssmk, const u16* __restrict__ ssmD, char* __restrict__ ws, char* smem,
                   int d) {
  const int t = threadIdx.x & 255;
  const int l = t & 63, wv = t >> 6;
  const int r = l & 31, hh = l >> 5;
  char* hs = smem;
  char* kvs = smem + CV_HBYTES;
  const u16* kvT = (const u16*)(ws + WS_KVT) + (size_t)d * SEQ;
  const u16* hrow = ssmk + (size_t)d * SEQ;
  STAMP(1, 0);
  const u32x4 kvv = ld16<AUX_LD2 != 0>(kvT + 8 * t);
  const u32x4 whi = *(const u32x4*)(hrow + 8 * t);
  const u32x4 wlo0 = *(const u32x4*)(hrow + 8 * (t > 0 ? t - 1 : 0));
  const float Dd = bf2f(ssmD[d]);
  *(u32x4*)(kvs + ((t >> 2) + 32) * CV_KVSTR + (t & 3) * 16) = kvv;
  if (t < 128) {
    u32x4 z = {0u, 0u, 0u, 0u};
    *(u32x4*)(kvs + (t >> 2) * CV_KVSTR + (t & 3) * 16) = z;
  }
  {
    const int E = t;
    u32x4 wlo = wlo0;
    if (E == 0) wlo = (u32x4){0u, 0u, 0u, 0u};
    unsigned z[8];
#pragma unroll
    for (int i = 0; i < 4; ++i) {
      const unsigned a = whi[3 - i];
      z[i] = (a >> 16) | (a << 16);
      const unsigned b = wlo[3 - i];
      z[4 + i] = (b >> 16) | (b << 16);
    }
    const int M = 255 - E;
#pragma unroll
    for (int p = 0; p < 8; ++p) {
      const int o = 7 - p;
      u32x4 v;
      if ((o & 1) == 0) {
        v[0] = z[o / 2];
        v[1] = z[o / 2 + 1];
        v[2] = z[o / 2 + 2];
        v[3] = z[o / 2 + 3];
      } else {
        v[0] = (z[o / 2] >> 16) | (z[o / 2 + 1] << 16);
        v[1] = (z[o / 2 + 1] >> 16) | (z[o / 2 + 2] << 16);
        v[2] = (z[o / 2 + 2] >> 16) | (z[o / 2 + 3] << 16);
        v[3] = (z[o / 2 + 3] >> 16) | (z[o / 2 + 4] << 16);
      }
      *(u32x4*)(hs + p * CV_HSTR + 16 * M) = v;
    }
    if (t < 24) {
      u32x4 zz = {0u, 0u, 0u, 0u};
      *(u32x4*)(hs + (t & 7) * CV_HSTR + 16 * (256 + (t >> 3))) = zz;
    }
  }
  __syncthreads();
  STAMP(1, 1);
  f32x16 acc0, acc1, acc2;
#pragma unroll
  for (int e = 0; e < 16; ++e) {
    acc0[e] = 0.f;
    acc1[e] = 0.f;
    acc2[e] = 0.f;
  }
  const char* abase = hs + (r & 7) * CV_HSTR + 16 * (255 - (r >> 3) + hh) - 64 * wv;
  const char* bbase = kvs + (r + 32 - wv) * CV_KVSTR + 16 * hh;
  {
    bf16x8 fa[2][2], fb[2][2];
    auto ldc = [&](int step, bf16x8(&a)[2], bf16x8(&b)[2]) {
      const int i = step >> 1, ks = step & 1;
      a[0] = *(const bf16x8*)(abase - 256 * i + 32 * ks);
      a[1] = *(const bf16x8*)(abase - 256 * i - 2048 + 32 * ks);
      b[1] = *(const bf16x8*)(bbase + (32 - 4 * i) * CV_KVSTR + 32 * ks);
      b[0] = *(const bf16x8*)(bbase - (4 * i) * CV_KVSTR + 32 * ks);
    };
    ldc(0, fa[0], fb[0]);
#pragma unroll
    for (int step = 0; step < 16; ++step) {
      if (step < 15) ldc(step + 1, fa[(step + 1) & 1], fb[(step + 1) & 1]);
      __builtin_amdgcn_sched_barrier(0);
      acc1 = MFMA32(fa[step & 1][0], fb[step & 1][1], acc1);
      acc0 = MFMA32(fa[step & 1][0], fb[step & 1][0], acc0);
      acc2 = MFMA32(fa[step & 1][1], fb[step & 1][0], acc2);
      __builtin_amdgcn_sched_barrier(0);
    }
  }
  acc1 += acc2;
  STAMP(1, 2);
  __syncthreads();
  STAMP(1, 6);
  float* red = (float*)smem;
  {
    float* rw = red + wv * (64 * CV_REDSTR);
#pragma unroll
    for (int g = 0; g < 4; ++g) {
      f32x4 v0 = {acc0[4 * g], acc0[4 * g + 1], acc0[4 * g + 2], acc0[4 * g + 3]};
      f32x4 v1 = {acc1[4 * g], acc1[4 * g + 1], acc1[4 * g + 2], acc1[4 * g + 3]};
      *(f32x4*)(rw + r * CV_REDSTR + 8 * g + 4 * hh) = v0;
      *(f32x4*)(rw + (32 + r) * CV_REDSTR + 8 * g + 4 * hh) = v1;
    }
  }
  __syncthreads();
  STAMP(1, 7);
  {
    const int a = t >> 2, b0 = 8 * (t & 3);
    f32x4 s0 = {0.f, 0.f, 0.f, 0.f}, s1 = {0.f, 0.f, 0.f, 0.f};
#pragma unroll
    for (int w = 0; w < 4; ++w) {
      const float* rp = red + w * (64 * CV_REDSTR) + a * CV_REDSTR + b0;
      s0 += *(const f32x4*)(rp);
      s1 += *(const f32x4*)(rp + 4);
    }
    float yy[8] = {s0[0], s0[1], s0[2], s0[3], s1[0], s1[1], s1[2], s1[3]};
    u32x4 o;
#pragma unroll
    for (int i = 0; i < 4; ++i) {
      const float y0 = rbf(yy[2 * i]) + rbf(bflo(kvv[i]) * Dd);
      const float y1 = rbf(yy[2 * i + 1]) + rbf(bfhi(kvv[i]) * Dd);
      o[i] = pack2(y0, y1);
    }
    wst16<AUX_Y2T>(ws_rsrc(ws), WS_Y2T + (unsigned)(d * SEQ + 8 * t) * 2, o);
  }
  STAMP(1, 4);
}

__global__ __launch_bounds__(512, 4) void k2_kernel(const u16* __restrict__ ssmk, const u16* __restrict__ ssmD,
                                                    const u16* __restrict__ wo, const u16* __restrict__ x,
                                                    const u16* __restrict__ gatew, char* __restrict__ ws) {
  __shared__ __attribute__((aligned(16))) char smem[2 * CV_SMEM];
  const int xcd = blockIdx.x & 7, bidx = (blockIdx.x >> 3) ^ 32;
  if (bidx >= 32) {
#define PF_WO 2
    const int half = threadIdx.x >> 8;
    conv_block(ssmk, ssmD, ws, smem + half * CV_SMEM, xcd * 64 + (bidx - 32) * 2 + half);
    const u32x4 pf0 = *(const u32x4*)(wo + (size_t)(bidx - 32) * 8192 + threadIdx.x * 8);
    const u32x4 pf1 = *(const u32x4*)(wo + (size_t)(bidx - 32) * 8192 + 4096 + threadIdx.x * 8);
    {
      const int l = threadIdx.x & 63;
      const int row = (xcd * 32 + (bidx - 32)) * 8 + (threadIdx.x >> 6);
      const u32x4 gw = *(const u32x4*)(gatew + l * 8);
      const u32x4 xv = ld16<false>(x + (size_t)row * DM + l * 8);
      float sum = 0.f;
#pragma unroll
      for (int i = 0; i < 4; ++i) {
        const unsigned pr = pack2(bflo(xv[i]) * bflo(gw[i]), bfhi(xv[i]) * bfhi(gw[i]));
        sum += bflo(pr);
        sum += bfhi(pr);
      }
#pragma unroll
      for (int o = 32; o >= 1; o >>= 1) sum += __shfl_xor(sum, o);
      if (l == 0) ((float*)(ws + WS_G))[row] = rbf(1.f / (1.f + __expf(-rbf(sum))));
    }
    asm volatile("" ::"v"(pf0));
    asm volatile("" ::"v"(pf1));
  } else {
    const int head = 4 * xcd + (bidx & 3), win = bidx >> 2;
    if (win == 0)
      attn_block<true>(ws, smem, head, 0);
    else
      attn_block<false>(ws, smem, head, win * 256);
  }
}

#define K3_STAGE 32768
#define K3_CSTR 68
#define K3_YSTR 136

DI void k3_body(const u16* __restrict__ x, const u16* __restrict__ wo, const char* __restrict__ ws,
                u16* __restrict__ out, char* smem) {
  const int t = threadIdx.x;
  const int l = t & 63, wv = t >> 6;
  const int r = l & 31, hh = l >> 5;
  const int wm = wv & 1, wn = (wv >> 1) & 1, kh = wv >> 2;
  const int tm = (blockIdx.x & 7) * 4 + ((blockIdx.x >> 3) & 3), tn = blockIdx.x >> 5;
  const int m0 = tm * 64, n0 = tn * 64;
  const u16* o = (const u16*)(ws + WS_O);
  STAMP(2, 0);

  const u16* y2T = (const u16*)(ws + WS_Y2T);
  const float* gp = (const float*)(ws + WS_G);
  const int rr = t >> 3, ch = t & 7;
  const u32x4 yv = ld16<AUX_LD3 != 0>(y2T + (size_t)(n0 + rr) * SEQ + m0 + ch * 8);
  const u32x4 xq = ld16<false>(x + (size_t)(m0 + rr) * DM + n0 + ch * 8);
  const float g = gp[m0 + rr];
  __builtin_amdgcn_sched_barrier(0);
  {
    char* ldsw = smem + wv * 2048 + l * 16;
#pragma unroll
    for (int s4 = 0; s4 < 4; ++s4) {
      const int st = s4;
#pragma unroll
      for (int i = 0; i < 2; ++i) {
        const int ar = 8 * wv + 4 * i + (l >> 4);
        GLDS16A(o + (size_t)(m0 + ar) * DM + st * 128 + (((l & 15) ^ (ar & 15)) * 8), ldsw + st * K3_STAGE + i * 1024,
                AUX_LD3);
      }
#pragma unroll
      for (int i = 0; i < 2; ++i) {
        const int br = 16 * wv + 8 * i + (l >> 3);
        GLDS16(wo + (size_t)(st * 128 + br) * DM + n0 + (((l & 7) ^ (((br >> 1) & 1) << 2)) * 8),
               ldsw + st * K3_STAGE + 16384 + i * 1024);
      }
    }
  }

  f32x16 acc;
#pragma unroll
  for (int e = 0; e < 16; ++e) acc[e] = 0.f;
  const int blk = (l >> 4) & 1, q4 = (l & 15) >> 2, p4 = l & 3;
  const int arow = wm * 32 + r;
  const int a_off = arow * 256;
  const int a_sw = arow & 15;
  const int b_off = 16384 + (8 * hh + q4) * 128 + (((wn * 4 + 2 * blk + (p4 >> 1)) ^ (((q4 >> 1) & 1) << 2)) * 16) +
                    (p4 & 1) * 8;
  const int a_x = (8 * kh + hh) ^ a_sw;
  const int b_k = b_off + 64 * kh * 128;
#pragma unroll
  for (int st = 0; st < 4; ++st) {
    if (st == 0) WAIT_V(12);
    else if (st == 1) WAIT_V(8);
    else if (st == 2) WAIT_V(4);
    else WAIT_V(0);
    RAWBAR();
    const char* sb = smem + st * K3_STAGE;
    bf16x8 af[4], bfr[4];
#pragma unroll
    for (int ks = 0; ks < 4; ++ks) {
      af[ks] = *(const bf16x8*)(sb + a_off + ((a_x ^ (2 * ks)) * 16));
      s16x4 lo = tr_read(sb + b_k + (16 * ks) * 128);
      s16x4 hi = tr_read(sb + b_k + (16 * ks + 4) * 128);
      bfr[ks] = cat_frag(lo, hi);
    }
    __builtin_amdgcn_sched_barrier(0);
#pragma unroll
    for (int ks = 0; ks < 4; ++ks) acc = MFMA32(af[ks], bfr[ks], acc);
    __builtin_amdgcn_sched_barrier(0);
  }
  __syncthreads();
  STAMP(2, 5);
  float* Cs = (float*)smem;
  char* Ys = smem + 2 * 64 * K3_CSTR * 4;
#pragma unroll
  for (int e = 0; e < 16; ++e) {
    const int row = wm * 32 + (e & 3) + 8 * (e >> 2) + 4 * hh;
    Cs[kh * 64 * K3_CSTR + row * K3_CSTR + wn * 32 + r] = acc[e];
  }
  {
    u32x2 a = {yv[0], yv[1]}, b = {yv[2], yv[3]};
    *(u32x2*)(Ys + rr * K3_YSTR + ch * 16) = a;
    *(u32x2*)(Ys + rr * K3_YSTR + ch * 16 + 8) = b;
  }
  __syncthreads();
  {
    const int row = rr;
    const int s = m0 + row;
    const float omg = rbf(1.f - g);
    const f32x4 c0 = *(const f32x4*)(Cs + row * K3_CSTR + ch * 8) + *(const f32x4*)(Cs + (64 + row) * K3_CSTR + ch * 8);
    const f32x4 c1 =
        *(const f32x4*)(Cs + row * K3_CSTR + ch * 8 + 4) + *(const f32x4*)(Cs + (64 + row) * K3_CSTR + ch * 8 + 4);
    float bb[8] = {c0[0], c0[1], c0[2], c0[3], c1[0], c1[1], c1[2], c1[3]};
    float res[8];
#pragma unroll
    for (int j = 0; j < 8; ++j) {
      const float y2 = bf2f(*(const u16*)(Ys + (ch * 8 + j) * K3_YSTR + row * 2));
      const float qv = (j & 1) ? bfhi(xq[j >> 1]) : bflo(xq[j >> 1]);
      const float a = rbf(qv * y2);
      const float ga = rbf(g * a);
      const float gb = rbf(omg * rbf(bb[j]));
      res[j] = ga + gb;
    }
    u32x4 ov;
    ov[0] = pack2(res[0], res[1]);
    ov[1] = pack2(res[2], res[3]);
    ov[2] = pack2(res[4], res[5]);
    ov[3] = pack2(res[6], res[7]);
    *(u32x4*)(out + (size_t)s * DM + n0 + ch * 8) = ov;
  }
  STAMP(2, 6);
}

__global__ __launch_bounds__(512) void k3_kernel(const u16* __restrict__ x, const u16* __restrict__ wo,
                                                 const char* __restrict__ ws, u16* __restrict__ out) {
  __shared__ __attribute__((aligned(16))) char smem[4 * K3_STAGE];
  k3_body(x, wo, ws, out, smem);
}

extern "C" void kernel_launch(void* const* d_in, const int* in_sizes, int n_in, void* d_out, int out_size,
                              void* d_ws, size_t ws_size, hipStream_t stream) {
  const u16* A_q = (const u16*)d_in[0];
  const u16* A_k = (const u16*)d_in[1];
  const u16* A_v = (const u16*)d_in[2];
  const u16* A_shift = (const u16*)d_in[3];
  const u16* A_ssmk = (const u16*)d_in[4];
  const u16* A_ssmD = (const u16*)d_in[5];
  const u16* B_wqkv = (const u16*)d_in[7];
  const u16* B_wo = (const u16*)d_in[8];
  const u16* gate_w = (const u16*)d_in[9];
  char* ws = (char*)d_ws;
  k1_kernel<<<256, 512, 0, stream>>>(A_q, A_k, A_v, A_shift, B_wqkv, A_ssmk, ws);
  k2_kernel<<<512, 512, 0, stream>>>(A_ssmk, A_ssmD, B_wo, A_q, gate_w, ws);
  k3_kernel<<<256, 512, 0, stream>>>(A_q, B_wo, ws, (u16*)d_out);
}
```

```cpp
#define TLB_TOUCH
#include <hip/hip_runtime.h>
#include <hip/hip_bf16.h>
#include <stdint.h>

#define DI __device__ __forceinline__
typedef __attribute__((ext_vector_type(8))) __bf16 bf16x8;
typedef __attribute__((ext_vector_type(4))) __bf16 bf16x4;
typedef __attribute__((ext_vector_type(2))) __bf16 bf16x2;
typedef __attribute__((ext_vector_type(4))) short s16x4;
typedef __attribute__((ext_vector_type(16))) float f32x16;
typedef __attribute__((ext_vector_type(4))) float f32x4;
typedef __attribute__((ext_vector_type(2))) float f32x2;
typedef __attribute__((ext_vector_type(4))) unsigned u32x4;
typedef __attribute__((ext_vector_type(2))) unsigned u32x2;
typedef unsigned short u16;

#define SEQ 2048
#define DM 512

#define MFMA32(a, b, c) __builtin_amdgcn_mfma_f32_32x32x16_bf16((a), (b), (c), 0, 0, 0)

DI float bf2f(u16 v) { return __uint_as_float(((unsigned)v) << 16); }
DI float bflo(unsigned v) { return __uint_as_float(v << 16); }
DI float bfhi(unsigned v) { return __uint_as_float(v & 0xffff0000u); }
DI unsigned pack2(float a, float b) {
  f32x2 f = {a, b};
  bf16x2 r = __builtin_convertvector(f, bf16x2);
  return __builtin_bit_cast(unsigned, r);
}
DI float rbf(float a) {
  return (float)((__bf16)a);
}
DI s16x4 tr_read(const char* p) {
  typedef __attribute__((address_space(3))) s16x4* lp;
  return __builtin_amdgcn_ds_read_tr16_b64_v4i16((lp)(p));
}
DI bf16x8 cat_frag(s16x4 lo, s16x4 hi) {
  typedef __attribute__((ext_vector_type(8))) short s16x8;
  s16x8 v = __builtin_shufflevector(lo, hi, 0, 1, 2, 3, 4, 5, 6, 7);
  return __builtin_bit_cast(bf16x8, v);
}

#define STAMP(k, i) do {} while (0)
#define WS_Q 0
#define WS_K (2u << 20)
#define WS_VT (4u << 20)
#define WS_KVT (6u << 20)
#define WS_Y2T (8u << 20)
#define WS_O (10u << 20)
#define WS_G (12u << 20)

#define WT_AUX 0
typedef __amdgpu_buffer_rsrc_t rsrc_t;
DI rsrc_t ws_rsrc(const char* ws) { return __builtin_amdgcn_make_buffer_rsrc((void*)ws, (short)0, 16 << 20, 0x00020000); }
#define AUX_ST1 0
#define AUX_ST2 0
#define AUX_KVT 16
#define AUX_Y2T 16
#define NT_KV
#define PF_LATE 1
#define ATT_MAP
#define AUX_LD2 0
#define AUX_LD3 0
template <int AUX>
DI void wst16(rsrc_t rs, unsigned off, u32x4 v) { __builtin_amdgcn_raw_buffer_store_b128(v, rs, (int)off, 0, AUX); }
template <int AUX>
DI void wst8(rsrc_t rs, unsigned off, u32x2 v) { __builtin_amdgcn_raw_buffer_store_b64(v, rs, (int)off, 0, AUX); }
template <bool NT>
DI u32x4 ld16(const void* p) {
  if constexpr (NT) return __builtin_nontemporal_load((const u32x4*)p);
  else return *(const u32x4*)p;
}

#define GLDS16(g, l) __builtin_amdgcn_global_load_lds((const unsigned*)(g), (unsigned*)(l), 16, 0, 0)
#define GLDS16A(g, l, aux) __builtin_amdgcn_global_load_lds((const unsigned*)(g), (unsigned*)(l), 16, 0, aux)
#define AUX_X 2
#define AUX_W 0
#define WAIT_V(n) asm volatile("s_waitcnt vmcnt(" #n ")" ::: "memory")
#define RAWBAR() do { asm volatile("s_waitcnt lgkmcnt(0)" ::: "memory"); __builtin_amdgcn_s_barrier(); } while (0)
#define K1_STAGE 32768

template <int N>
DI void wait_vm() {
  if constexpr (N == 0) WAIT_V(0);
  else if constexpr (N == 4) WAIT_V(4);
  else if constexpr (N == 8) WAIT_V(8);
  else if constexpr (N == 12) WAIT_V(12);
  else if constexpr (N == 16) WAIT_V(16);
  else if constexpr (N == 24) WAIT_V(24);
}

#define QSCALE (0.25f * 1.44269504088896340736f)

template <int MODE>
DI void k1_gemm(const u16* __restrict__ x, const u16* __restrict__ w, char* __restrict__ ws, char* smem, int tm,
                int tn) {
  const int t = threadIdx.x;
  const int l = t & 63, wv = t >> 6;
  const int r = l & 31, hh = l >> 5;
  const int wm = wv & 1, wn = wv >> 1;
  const int m0 = tm * 128, n0 = tn * 128;

  const u16* xg[2];
  const u16* wg[2];
#pragma unroll
  for (int i = 0; i < 2; ++i) {
    const int ar = 16 * wv + 8 * i + (l >> 3);
    xg[i] = x + (size_t)(m0 + ar) * DM + (((l & 7) ^ ((ar >> 1) & 7)) * 8);
    const int br = 8 * wv + 4 * i + (l >> 4);
    wg[i] = w + (size_t)br * 1536 + n0 + (((l & 15) ^ ((br & 3) << 2)) * 8);
  }
  char* ldsw = smem + wv * 2048 + l * 16;

  const int rot = 0;
  auto issue = [&](int kt) {
    char* st = ldsw + (kt & 3) * K1_STAGE;
    const int kk = (kt + rot) & 7;
#pragma unroll
    for (int i = 0; i < 2; ++i) GLDS16A(xg[i] + kk * 64, st + i * 1024, AUX_X);
#pragma unroll
    for (int i = 0; i < 2; ++i) GLDS16A(wg[i] + (size_t)kk * 64 * 1536, st + 16384 + i * 1024, AUX_W);
  };

  f32x16 acc[2];
#pragma unroll
  for (int i = 0; i < 2; ++i)
#pragma unroll
    for (int e = 0; e < 16; ++e) acc[i][e] = 0.f;

  const int blk = (l >> 4) & 1, q4 = (l & 15) >> 2, p4 = l & 3;
  const int a_row_off = (wm * 64 + r) * 128;
  const int a_sw = (r >> 1) & 7;
  const int b_base = 16384 + (8 * hh + q4) * 256 + (p4 & 1) * 8 + (((wn * 4 + 2 * blk + (p4 >> 1)) ^ (q4 << 2)) * 16);

  STAMP(0, 0);
  issue(0);
  issue(1);
  issue(2);
  issue(3);
  bf16x8 fa[2][4][2], fb[2][4];
  auto ldfr = [&](int kt, bf16x8(&a)[4][2], bf16x8(&b)[4]) {
    const char* st = smem + (kt & 3) * K1_STAGE;
#pragma unroll
    for (int ks = 0; ks < 4; ++ks) {
#pragma unroll
      for (int i = 0; i < 2; ++i)
        a[ks][i] = *(const bf16x8*)(st + a_row_off + i * 32 * 128 + (((2 * ks + hh) ^ a_sw) * 16));
      s16x4 lo = tr_read(st + b_base + (16 * ks) * 256);
      s16x4 hi = tr_read(st + b_base + (16 * ks + 4) * 256);
      b[ks] = cat_frag(lo, hi);
    }
  };
  wait_vm<12>();
  RAWBAR();
  ldfr(0, fa[0], fb[0]);
#define K1_ILV 2
#pragma unroll
  for (int kt = 0; kt < 8; ++kt) {
    const char* stn = smem + ((kt + 1) & 3) * K1_STAGE;
    bf16x8(&an)[4][2] = fa[(kt + 1) & 1];
    bf16x8(&bn)[4] = fb[(kt + 1) & 1];
    s16x4 lo, hi;
    auto rdpair = [&](int j) {
      const int ks = j >> 1, i = j & 1;
      an[ks][i] = *(const bf16x8*)(stn + a_row_off + i * 32 * 128 + (((2 * ks + hh) ^ a_sw) * 16));
      if (i == 0) {
        lo = tr_read(stn + b_base + (16 * ks) * 256);
      } else {
        hi = tr_read(stn + b_base + (16 * ks + 4) * 256);
        bn[ks] = cat_frag(lo, hi);
      }
    };
    if (kt < 7) {
      if (kt <= 4) wait_vm<8>();
      else if (kt == 5) wait_vm<4>();
      else wait_vm<0>();
      RAWBAR();
    }
    __builtin_amdgcn_sched_barrier(0);
#pragma unroll
    for (int g = 0; g < 8; ++g) {
      const int ks = g >> 1, i = g & 1;
      if (MODE == 2)
        acc[i] = MFMA32(fa[kt & 1][ks][i], fb[kt & 1][ks], acc[i]);
      else
        acc[i] = MFMA32(fb[kt & 1][ks], fa[kt & 1][ks][i], acc[i]);
      __builtin_amdgcn_sched_barrier(0);
      if (kt < 7) {
        if (g < 2) {
          rdpair(2 * g);
          rdpair(2 * g + 1);
        } else if (g < 6) {
          rdpair(g + 2);
        }
      }
      if (i == 1 && kt + 4 <= 7) {
        char* st4 = ldsw + (kt & 3) * K1_STAGE;
        const int kk = (kt + 4 + rot) & 7;
        if (ks < 2)
          GLDS16A(xg[ks] + kk * 64, st4 + ks * 1024, AUX_X);
        else
          GLDS16A(wg[ks - 2] + (size_t)kk * 64 * 1536, st4 + 16384 + (ks - 2) * 1024, AUX_W);
      }
      __builtin_amdgcn_sched_barrier(0);
    }
  }
  STAMP(0, 9);
  const rsrc_t rs = ws_rsrc(ws);
  if (MODE == 2) {
#pragma unroll
    for (int i = 0; i < 2; ++i) {
      const int n = (n0 - 1024) + wn * 32 + r;
      const int tok0 = m0 + wm * 64 + i * 32;
      const unsigned dst = WS_VT + (unsigned)(n * SEQ + tok0 + 8 * hh) * 2;
#pragma unroll
      for (int bb = 0; bb < 2; ++bb) {
        u32x4 v;
        v[0] = pack2(acc[i][8 * bb + 0], acc[i][8 * bb + 1]);
        v[1] = pack2(acc[i][8 * bb + 2], acc[i][8 * bb + 3]);
        v[2] = pack2(acc[i][8 * bb + 4], acc[i][8 * bb + 5]);
        v[3] = pack2(acc[i][8 * bb + 6], acc[i][8 * bb + 7]);
        wst16<AUX_ST1>(rs, dst + 32 * bb, v);
      }
    }
  } else {
    const unsigned base = (MODE == 1 ? WS_K : WS_Q);
    const int nn0 = n0 & 511;
#pragma unroll
    for (int i = 0; i < 2; ++i) {
      const int tok = m0 + wm * 64 + i * 32 + r;
      const int nb = nn0 + wn * 32;
      const int head = nb >> 4;
#pragma unroll
      for (int g = 0; g < 4; ++g) {
        u32x2 v;
        const float sc = (MODE == 0) ? QSCALE : 1.f;
        v[0] = pack2(acc[i][4 * g + 0] * sc, acc[i][4 * g + 1] * sc);
        v[1] = pack2(acc[i][4 * g + 2] * sc, acc[i][4 * g + 3] * sc);
        wst8<AUX_ST1>(rs, base + (unsigned)(((head + (g >> 1)) * SEQ + tok) * 16 + 8 * (g & 1) + 4 * hh) * 2, v);
      }
    }
  }
  STAMP(0, 10);
}

__global__ __launch_bounds__(512) void k1_kernel(const u16* __restrict__ x, const u16* __restrict__ kin,
                                                 const u16* __restrict__ vin, const u16* __restrict__ shiftk,
                                                 const u16* __restrict__ wqkv, const u16* __restrict__ ssmk,
                                                 const u16* __restrict__ wo, const u16* __restrict__ ssmD,
                                                 const u16* __restrict__ gatew, const u16* __restrict__ outp,
                                                 char* __restrict__ ws) {
  __shared__ __attribute__((aligned(16))) char smem[4 * K1_STAGE];
  const int xcd = blockIdx.x & 7, bidx = blockIdx.x >> 3;
  if (bidx < 24) {
    const int tm = 2 * xcd + (bidx & 1), tn = bidx >> 1;
    if (tn >= 8)
      k1_gemm<2>(x, wqkv, ws, smem, tm, tn);
    else if (tn >= 4)
      k1_gemm<1>(x, wqkv, ws, smem, tm, tn);
    else
      k1_gemm<0>(x, wqkv, ws, smem, tm, tn);
    return;
  }
  const int l = threadIdx.x & 63, wv = threadIdx.x >> 6;
  const int t = threadIdx.x & 255, h2 = threadIdx.x >> 8;
  const int pb = xcd * 8 + (bidx - 24);
  STAMP(0, 0);
  u32x4 pf[4];
  unsigned tch = 0;
  {
    const int tt = threadIdx.x;
    const char* tp = ws + (size_t)tt * 65536;
    if (tt >= 208 && tt < 240) tp = (const char*)outp + (size_t)(tt - 208) * 65536;
    if (tt >= 240 && tt < 248) tp = (const char*)wo + (size_t)(tt - 240) * 65536;
    if (tt == 248) tp = (const char*)ssmD;
    if (tt == 249) tp = (const char*)gatew;
    if (tt >= 250 && tt < 282) tp = (const char*)ssmk + (size_t)(tt - 250) * 65536;
    if (tt < 282) tch = *(const unsigned*)tp;
  }
  {
    const int ch = t & 15, rg = t >> 4;
    u32x4 kr[7], vr[4], sk[4];
    const int tile = pb * 2 + h2;
    const int s0 = (tile >> 2) * 64, d0 = (tile & 3) * 128;
    {
      const int sb = s0 + rg * 4;
      const int dd = d0 + ch * 8;
#pragma unroll
      for (int i = 0; i < 7; ++i) {
        const int s = sb - 3 + i;
        u32x4 z = {0u, 0u, 0u, 0u};
        kr[i] = (s >= 0) ? __builtin_nontemporal_load((const u32x4*)(kin + (size_t)s * DM + dd)) : z;
      }
#pragma unroll
      for (int i = 0; i < 4; ++i) vr[i] = __builtin_nontemporal_load((const u32x4*)(vin + (size_t)(sb + i) * DM + dd));
#pragma unroll
      for (int i = 0; i < 4; ++i) sk[i] = *(const u32x4*)(shiftk + (size_t)dd * 4 + i * 8);
    }
    __builtin_amdgcn_sched_barrier(0);
    __builtin_amdgcn_sched_barrier(0);
    char* sm = smem + h2 * 17408;
#pragma unroll
    for (int c = 0; c < 4; ++c) {
      const f32x2 w0 = {bflo(sk[c][0]), bflo(sk[c][2])}, w1 = {bfhi(sk[c][0]), bfhi(sk[c][2])};
      const f32x2 w2 = {bflo(sk[c][1]), bflo(sk[c][3])}, w3 = {bfhi(sk[c][1]), bfhi(sk[c][3])};
      f32x2 kk[7];
#pragma unroll
      for (int i = 0; i < 7; ++i) kk[i] = (f32x2){bflo(kr[i][c]), bfhi(kr[i][c])};
      f32x2 r[4];
#pragma unroll
      for (int i = 0; i < 4; ++i) {
        f32x2 a = kk[i] * w3;
        a = __builtin_elementwise_fma(kk[i + 1], w2, a);
        a = __builtin_elementwise_fma(kk[i + 2], w1, a);
        a = __builtin_elementwise_fma(kk[i + 3], w0, a);
        const unsigned ap = pack2(a[0], a[1]);
        r[i] = (f32x2){bflo(ap), bfhi(ap)} * (f32x2){bflo(vr[i][c]), bfhi(vr[i][c])};
      }
      *(u32x2*)(sm + (ch * 8 + 2 * c) * 136 + rg * 8) = (u32x2){pack2(r[0][0], r[1][0]), pack2(r[2][0], r[3][0])};
      *(u32x2*)(sm + (ch * 8 + 2 * c + 1) * 136 + rg * 8) = (u32x2){pack2(r[0][1], r[1][1]), pack2(r[2][1], r[3][1])};
    }
    STAMP(0, 14);
    __builtin_amdgcn_sched_barrier(0);
#pragma unroll
    for (int i = 0; i < 4; ++i) pf[i] = *(const u32x4*)(ssmk + (size_t)pb * 16384 + (i * 512 + threadIdx.x) * 8);
    __builtin_amdgcn_sched_barrier(0);
    RAWBAR();
    STAMP(0, 15);
    const rsrc_t rs = ws_rsrc(ws);
#pragma unroll
    for (int i = 0; i < 4; ++i) {
      const int idx = t + 256 * i;
      const int dr = idx >> 3, c8 = idx & 7;
      const u32x2 a = *(const u32x2*)(sm + dr * 136 + c8 * 16);
      const u32x2 b = *(const u32x2*)(sm + dr * 136 + c8 * 16 + 8);
      u32x4 v = {a[0], a[1], b[0], b[1]};
      wst16<AUX_KVT>(rs, WS_KVT + (unsigned)((d0 + dr) * SEQ + s0 + c8 * 8) * 2, v);
    }
  }
  STAMP(0, 11);
#pragma unroll
  for (int i = 0; i < 4; ++i) asm volatile("" ::"v"(pf[i]));
  asm volatile("" ::"v"(tch));
  STAMP(0, 12);
}

#define CV_KVSTR 80
#define CV_HSTR 4160
#define CV_HBYTES (8 * CV_HSTR)
#define CV_REDSTR 36
#define CV_SMEM 40960

template <bool FIRST>
DI void attn_block(const char* __restrict__ ws, char* smem, int head, int qb) {
  const int l = threadIdx.x & 63, wv = threadIdx.x >> 6;
  const int r = l & 31, hh = l >> 5;
  const u16* Qh = (const u16*)(ws + WS_Q) + (size_t)head * SEQ * 16;
  const u16* Kh = (const u16*)(ws + WS_K) + (size_t)head * SEQ * 16;
  const u16* Vh = (const u16*)(ws + WS_VT) + (size_t)head * 16 * SEQ;
  u16* O = (u16*)(ws + WS_O);
  const int q0 = qb + 32 * wv;
  const int kt0 = FIRST ? (8 - wv) : 0;
  STAMP(1, 0);
  char* ks_ = smem;
  char* vs_ = smem + 16384;
  char* cs_ = smem + 32768;
  {
    const int key = l >> 1;
    const u16* ksrc = Kh + (size_t)key * 16 + (((l & 1) ^ ((key >> 3) & 1)) * 8);
    const int dd = l >> 2;
    const u16* vsrc = Vh + (size_t)dd * SEQ + (((l & 3) ^ ((dd >> 2) & 3)) * 8);
#pragma unroll
    for (int i = 0; i < 2; ++i) {
      const int u = wv + 8 * i;
      int key0 = qb - 256 + 32 * u;
      key0 = key0 < 0 ? 0 : key0;
      GLDS16A(ksrc + (size_t)key0 * 16, ks_ + u * 1024 + l * 16, AUX_LD2);
      GLDS16A(vsrc + key0, vs_ + u * 1024 + l * 16, AUX_LD2);
    }
  }
  const bf16x8 qf = __builtin_bit_cast(bf16x8, ld16<AUX_LD2 != 0>(Qh + (size_t)(q0 + r) * 16 + 8 * hh));
  if (threadIdx.x < 2) {
    const unsigned o = threadIdx.x == 0 ? 0x3f803f80u : 0u;
    *(u32x4*)(cs_ + threadIdx.x * 16) = (u32x4){o, o, o, o};
  }
  WAIT_V(0);
  RAWBAR();
  STAMP(1, 1);
  const char* krd = ks_ + (32 * wv + r) * 32 + ((hh ^ ((r >> 3) & 1)) * 16);
  f32x16 zero;
#pragma unroll
  for (int e = 0; e < 16; ++e) zero[e] = 0.f;
  const float NEG = -3.0e38f;
  auto mask = [&](int kt, f32x16& s) {
    if (kt == 0) {
#pragma unroll
      for (int e = 0; e < 16; ++e) s[e] = ((e & 3) + 8 * (e >> 2) + 4 * hh > r) ? s[e] : NEG;
    }
    if (kt == 8) {
#pragma unroll
      for (int e = 0; e < 16; ++e) s[e] = ((e & 3) + 8 * (e >> 2) + 4 * hh <= r) ? s[e] : NEG;
    }
  };
  float mx = NEG, mx2 = NEG;
  {
    bf16x8 kf[9];
#pragma unroll
    for (int kt = 0; kt < 9; ++kt) kf[kt] = *(const bf16x8*)(krd + kt * 1024);
    f32x16 s[3];
    s[0] = MFMA32(kf[0], qf, zero);
    s[1] = MFMA32(kf[1], qf, zero);
#pragma unroll
    for (int kt = 0; kt < 9; ++kt) {
      if (kt < 7) s[(kt + 2) % 3] = MFMA32(kf[kt + 2], qf, zero);
      f32x16& c = s[kt % 3];
      mask(kt, c);
      float m = NEG, m2 = NEG;
#pragma unroll
      for (int e = 0; e < 8; e += 2) {
        m = fmaxf(fmaxf(c[e], c[e + 1]), m);
        m2 = fmaxf(fmaxf(c[8 + e], c[9 + e]), m2);
      }
      if (FIRST) {
        m = (kt >= kt0) ? m : NEG;
        m2 = (kt >= kt0) ? m2 : NEG;
      }
      mx = fmaxf(mx, m);
      mx2 = fmaxf(mx2, m2);
    }
  }
  mx = fmaxf(mx, mx2);
  mx = fmaxf(mx, __shfl_xor(mx, 32));
  f32x16 negm;
#pragma unroll
  for (int e = 0; e < 16; ++e) negm[e] = -mx;
  STAMP(1, 2);
  const int vsw = (r >> 2) & 3;
  const bool isd = r < 16;
  const int cad = 32768 + ((r == 16 || r == 20) ? 0 : 16);
  int va0 = isd ? 16384 + wv * 1024 + r * 64 + ((hh ^ vsw) * 16) : cad;
  int va1 = isd ? 16384 + wv * 1024 + r * 64 + (((2 + hh) ^ vsw) * 16) : cad;
  const int vstep = isd ? 1024 : 0;
  f32x16 oacc = zero;
  {
    f32x16 s[2];
    bf16x8 kf = *(const bf16x8*)(krd);
    s[0] = MFMA32(kf, qf, negm);
#pragma unroll
    for (int kt = 0; kt < 9; ++kt) {
      const bf16x8 vf0 = *(const bf16x8*)(smem + va0);
      const bf16x8 vf1 = *(const bf16x8*)(smem + va1);
      va0 += vstep;
      va1 += vstep;
      if (kt < 8) {
        kf = *(const bf16x8*)(krd + (kt + 1) * 1024);
        s[(kt + 1) & 1] = MFMA32(kf, qf, negm);
      }
      f32x16& c = s[kt & 1];
      mask(kt, c);
      if (!FIRST || kt >= kt0) {
        u32x4 pp[2];
#pragma unroll
        for (int e = 0; e < 16; e += 2) {
          pp[e >> 3][(e >> 1) & 3] = pack2(__builtin_amdgcn_exp2f(c[e]), __builtin_amdgcn_exp2f(c[e + 1]));
        }
        oacc = MFMA32(vf0, __builtin_bit_cast(bf16x8, pp[0]), oacc);
        oacc = MFMA32(vf1, __builtin_bit_cast(bf16x8, pp[1]), oacc);
      }
    }
  }
  STAMP(1, 3);
  const float inv = 1.f / oacc[8];
  u32x2 v0 = {pack2(oacc[0] * inv, oacc[1] * inv), pack2(oacc[2] * inv, oacc[3] * inv)};
  u32x2 v1 = {pack2(oacc[4] * inv, oacc[5] * inv), pack2(oacc[6] * inv, oacc[7] * inv)};
  const rsrc_t rs = ws_rsrc(ws);
  const unsigned op = WS_O + (unsigned)((q0 + r) * DM + head * 16 + 4 * hh) * 2;
  wst8<AUX_ST2>(rs, op, v0);
  wst8<AUX_ST2>(rs, op + 16, v1);
  STAMP(1, 4);
}

DI void conv_block(const u16* __restrict__ ssmk, const u16* __restrict__ ssmD, char* __restrict__ ws, char* smem,
                   int d) {
  const int t = threadIdx.x & 255;
  const int l = t & 63, wv = t >> 6;
  const int r = l & 31, hh = l >> 5;
  char* hs = smem;
  char* kvs = smem + CV_HBYTES;
  const u16* kvT = (const u16*)(ws + WS_KVT) + (size_t)d * SEQ;
  const u16* hrow = ssmk + (size_t)d * SEQ;
  STAMP(1, 0);
  const u32x4 kvv = ld16<AUX_LD2 != 0>(kvT + 8 * t);
  const u32x4 whi = *(const u32x4*)(hrow + 8 * t);
  const u32x4 wlo0 = *(const u32x4*)(hrow + 8 * (t > 0 ? t - 1 : 0));
  const float Dd = bf2f(ssmD[d]);
  *(u32x4*)(kvs + ((t >> 2) + 32) * CV_KVSTR + (t & 3) * 16) = kvv;
  if (t < 128) {
    u32x4 z = {0u, 0u, 0u, 0u};
    *(u32x4*)(kvs + (t >> 2) * CV_KVSTR + (t & 3) * 16) = z;
  }
  {
    const int E = t;
    u32x4 wlo = wlo0;
    if (E == 0) wlo = (u32x4){0u, 0u, 0u, 0u};
    unsigned z[8];
#pragma unroll
    for (int i = 0; i < 4; ++i) {
      const unsigned a = whi[3 - i];
      z[i] = (a >> 16) | (a << 16);
      const unsigned b = wlo[3 - i];
      z[4 + i] = (b >> 16) | (b << 16);
    }
    const int M = 255 - E;
#pragma unroll
    for (int p = 0; p < 8; ++p) {
      const int o = 7 - p;
      u32x4 v;
      if ((o & 1) == 0) {
        v[0] = z[o / 2];
        v[1] = z[o / 2 + 1];
        v[2] = z[o / 2 + 2];
        v[3] = z[o / 2 + 3];
      } else {
        v[0] = (z[o / 2] >> 16) | (z[o / 2 + 1] << 16);
        v[1] = (z[o / 2 + 1] >> 16) | (z[o / 2 + 2] << 16);
        v[2] = (z[o / 2 + 2] >> 16) | (z[o / 2 + 3] << 16);
        v[3] = (z[o / 2 + 3] >> 16) | (z[o / 2 + 4] << 16);
      }
      *(u32x4*)(hs + p * CV_HSTR + 16 * M) = v;
    }
    if (t < 24) {
      u32x4 zz = {0u, 0u, 0u, 0u};
      *(u32x4*)(hs + (t & 7) * CV_HSTR + 16 * (256 + (t >> 3))) = zz;
    }
  }
  __syncthreads();
  STAMP(1, 1);
  f32x16 acc0, acc1, acc2;
#pragma unroll
  for (int e = 0; e < 16; ++e) {
    acc0[e] = 0.f;
    acc1[e] = 0.f;
    acc2[e] = 0.f;
  }
  const char* abase = hs + (r & 7) * CV_HSTR + 16 * (255 - (r >> 3) + hh) - 64 * wv;
  const char* bbase = kvs + (r + 32 - wv) * CV_KVSTR + 16 * hh;
  {
    bf16x8 fa[2][2], fb[2][2];
    auto ldc = [&](int step, bf16x8(&a)[2], bf16x8(&b)[2]) {
      const int i = step >> 1, ks = step & 1;
      a[0] = *(const bf16x8*)(abase - 256 * i + 32 * ks);
      a[1] = *(const bf16x8*)(abase - 256 * i - 2048 + 32 * ks);
      b[1] = *(const bf16x8*)(bbase + (32 - 4 * i) * CV_KVSTR + 32 * ks);
      b[0] = *(const bf16x8*)(bbase - (4 * i) * CV_KVSTR + 32 * ks);
    };
    ldc(0, fa[0], fb[0]);
#pragma unroll
    for (int step = 0; step < 16; ++step) {
      if (step < 15) ldc(step + 1, fa[(step + 1) & 1], fb[(step + 1) & 1]);
      __builtin_amdgcn_sched_barrier(0);
      acc1 = MFMA32(fa[step & 1][0], fb[step & 1][1], acc1);
      acc0 = MFMA32(fa[step & 1][0], fb[step & 1][0], acc0);
      acc2 = MFMA32(fa[step & 1][1], fb[step & 1][0], acc2);
      __builtin_amdgcn_sched_barrier(0);
    }
  }
  acc1 += acc2;
  STAMP(1, 2);
  __syncthreads();
  STAMP(1, 6);
  float* red = (float*)smem;
  {
    float* rw = red + wv * (64 * CV_REDSTR);
#pragma unroll
    for (int g = 0; g < 4; ++g) {
      f32x4 v0 = {acc0[4 * g], acc0[4 * g + 1], acc0[4 * g + 2], acc0[4 * g + 3]};
      f32x4 v1 = {acc1[4 * g], acc1[4 * g + 1], acc1[4 * g + 2], acc1[4 * g + 3]};
      *(f32x4*)(rw + r * CV_REDSTR + 8 * g + 4 * hh) = v0;
      *(f32x4*)(rw + (32 + r) * CV_REDSTR + 8 * g + 4 * hh) = v1;
    }
  }
  __syncthreads();
  STAMP(1, 7);
  {
    const int a = t >> 2, b0 = 8 * (t & 3);
    f32x4 s0 = {0.f, 0.f, 0.f, 0.f}, s1 = {0.f, 0.f, 0.f, 0.f};
#pragma unroll
    for (int w = 0; w < 4; ++w) {
      const float* rp = red + w * (64 * CV_REDSTR) + a * CV_REDSTR + b0;
      s0 += *(const f32x4*)(rp);
      s1 += *(const f32x4*)(rp + 4);
    }
    float yy[8] = {s0[0], s0[1], s0[2], s0[3], s1[0], s1[1], s1[2], s1[3]};
    u32x4 o;
#pragma unroll
    for (int i = 0; i < 4; ++i) {
      const float y0 = rbf(yy[2 * i]) + rbf(bflo(kvv[i]) * Dd);
      const float y1 = rbf(yy[2 * i + 1]) + rbf(bfhi(kvv[i]) * Dd);
      o[i] = pack2(y0, y1);
    }
    wst16<AUX_Y2T>(ws_rsrc(ws), WS_Y2T + (unsigned)(d * SEQ + 8 * t) * 2, o);
  }
  STAMP(1, 4);
}

__global__ __launch_bounds__(512, 4) void k2_kernel(const u16* __restrict__ ssmk, const u16* __restrict__ ssmD,
                                                    const u16* __restrict__ wo, const u16* __restrict__ x,
                                                    const u16* __restrict__ gatew, char* __restrict__ ws) {
  __shared__ __attribute__((aligned(16))) char smem[2 * CV_SMEM];
  const int xcd = blockIdx.x & 7, bidx = (blockIdx.x >> 3) ^ 32;
  if (bidx >= 32) {
#define PF_WO 2
    const int half = threadIdx.x >> 8;
    conv_block(ssmk, ssmD, ws, smem + half * CV_SMEM, xcd * 64 + (bidx - 32) * 2 + half);
    const u32x4 pf0 = *(const u32x4*)(wo + (size_t)(bidx - 32) * 8192 + threadIdx.x * 8);
    const u32x4 pf1 = *(const u32x4*)(wo + (size_t)(bidx - 32) * 8192 + 4096 + threadIdx.x * 8);
    {
      const int l = threadIdx.x & 63;
      const int row = (xcd * 32 + (bidx - 32)) * 8 + (threadIdx.x >> 6);
      const u32x4 gw = *(const u32x4*)(gatew + l * 8);
      const u32x4 xv = ld16<false>(x + (size_t)row * DM + l * 8);
      float sum = 0.f;
#pragma unroll
      for (int i = 0; i < 4; ++i) {
        const unsigned pr = pack2(bflo(xv[i]) * bflo(gw[i]), bfhi(xv[i]) * bfhi(gw[i]));
        sum += bflo(pr);
        sum += bfhi(pr);
      }
#pragma unroll
      for (int o = 32; o >= 1; o >>= 1) sum += __shfl_xor(sum, o);
      if (l == 0) ((float*)(ws + WS_G))[row] = rbf(1.f / (1.f + __expf(-rbf(sum))));
    }
    asm volatile("" ::"v"(pf0));
    asm volatile("" ::"v"(pf1));
  } else {
    const int head = 4 * xcd + (bidx & 3), win = bidx >> 2;
    if (win == 0)
      attn_block<true>(ws, smem, head, 0);
    else
      attn_block<false>(ws, smem, head, win * 256);
  }
}

#define K3_STAGE 32768
#define K3_CSTR 68
#define K3_YSTR 136

DI void k3_body(const u16* __restrict__ x, const u16* __restrict__ wo, const char* __restrict__ ws,
                u16* __restrict__ out, char* smem) {
  const int t = threadIdx.x;
  const int l = t & 63, wv = t >> 6;
  const int r = l & 31, hh = l >> 5;
  const int wm = wv & 1, wn = (wv >> 1) & 1, kh = wv >> 2;
  const int tm = (blockIdx.x & 7) * 4 + ((blockIdx.x >> 3) & 3), tn = blockIdx.x >> 5;
  const int m0 = tm * 64, n0 = tn * 64;
  const u16* o = (const u16*)(ws + WS_O);
  STAMP(2, 0);

  const u16* y2T = (const u16*)(ws + WS_Y2T);
  const float* gp = (const float*)(ws + WS_G);
  const int rr = t >> 3, ch = t & 7;
  const u32x4 yv = ld16<AUX_LD3 != 0>(y2T + (size_t)(n0 + rr) * SEQ + m0 + ch * 8);
  const u32x4 xq = ld16<false>(x + (size_t)(m0 + rr) * DM + n0 + ch * 8);
  const float g = gp[m0 + rr];
  __builtin_amdgcn_sched_barrier(0);
  {
    char* ldsw = smem + wv * 2048 + l * 16;
#pragma unroll
    for (int s4 = 0; s4 < 4; ++s4) {
      const int st = s4;
#pragma unroll
      for (int i = 0; i < 2; ++i) {
        const int ar = 8 * wv + 4 * i + (l >> 4);
        GLDS16A(o + (size_t)(m0 + ar) * DM + st * 128 + (((l & 15) ^ (ar & 15)) * 8), ldsw + st * K3_STAGE + i * 1024,
                AUX_LD3);
      }
#pragma unroll
      for (int i = 0; i < 2; ++i) {
        const int br = 16 * wv + 8 * i + (l >> 3);
        GLDS16(wo + (size_t)(st * 128 + br) * DM + n0 + (((l & 7) ^ (((br >> 1) & 1) << 2)) * 8),
               ldsw + st * K3_STAGE + 16384 + i * 1024);
      }
    }
  }

  f32x16 acc;
#pragma unroll
  for (int e = 0; e < 16; ++e) acc[e] = 0.f;
  const int blk = (l >> 4) & 1, q4 = (l & 15) >> 2, p4 = l & 3;
  const int arow = wm * 32 + r;
  const int a_off = arow * 256;
  const int a_sw = arow & 15;
  const int b_off = 16384 + (8 * hh + q4) * 128 + (((wn * 4 + 2 * blk + (p4 >> 1)) ^ (((q4 >> 1) & 1) << 2)) * 16) +
                    (p4 & 1) * 8;
  const int a_x = (8 * kh + hh) ^ a_sw;
  const int b_k = b_off + 64 * kh * 128;
#pragma unroll
  for (int st = 0; st < 4; ++st) {
    if (st == 0) WAIT_V(12);
    else if (st == 1) WAIT_V(8);
    else if (st == 2) WAIT_V(4);
    else WAIT_V(0);
    RAWBAR();
    const char* sb = smem + st * K3_STAGE;
    bf16x8 af[4], bfr[4];
#pragma unroll
    for (int ks = 0; ks < 4; ++ks) {
      af[ks] = *(const bf16x8*)(sb + a_off + ((a_x ^ (2 * ks)) * 16));
      s16x4 lo = tr_read(sb + b_k + (16 * ks) * 128);
      s16x4 hi = tr_read(sb + b_k + (16 * ks + 4) * 128);
      bfr[ks] = cat_frag(lo, hi);
    }
    __builtin_amdgcn_sched_barrier(0);
#pragma unroll
    for (int ks = 0; ks < 4; ++ks) acc = MFMA32(af[ks], bfr[ks], acc);
    __builtin_amdgcn_sched_barrier(0);
  }
  __syncthreads();
  STAMP(2, 5);
  float* Cs = (float*)smem;
  char* Ys = smem + 2 * 64 * K3_CSTR * 4;
#pragma unroll
  for (int e = 0; e < 16; ++e) {
    const int row = wm * 32 + (e & 3) + 8 * (e >> 2) + 4 * hh;
    Cs[kh * 64 * K3_CSTR + row * K3_CSTR + wn * 32 + r] = acc[e];
  }
  {
    u32x2 a = {yv[0], yv[1]}, b = {yv[2], yv[3]};
    *(u32x2*)(Ys + rr * K3_YSTR + ch * 16) = a;
    *(u32x2*)(Ys + rr * K3_YSTR + ch * 16 + 8) = b;
  }
  __syncthreads();
  {
    const int row = rr;
    const int s = m0 + row;
    const float omg = rbf(1.f - g);
    const f32x4 c0 = *(const f32x4*)(Cs + row * K3_CSTR + ch * 8) + *(const f32x4*)(Cs + (64 + row) * K3_CSTR + ch * 8);
    const f32x4 c1 =
        *(const f32x4*)(Cs + row * K3_CSTR + ch * 8 + 4) + *(const f32x4*)(Cs + (64 + row) * K3_CSTR + ch * 8 + 4);
    float bb[8] = {c0[0], c0[1], c0[2], c0[3], c1[0], c1[1], c1[2], c1[3]};
    float res[8];
#pragma unroll
    for (int j = 0; j < 8; ++j) {
      const float y2 = bf2f(*(const u16*)(Ys + (ch * 8 + j) * K3_YSTR + row * 2));
      const float qv = (j & 1) ? bfhi(xq[j >> 1]) : bflo(xq[j >> 1]);
      const float a = rbf(qv * y2);
      const float ga = rbf(g * a);
      const float gb = rbf(omg * rbf(bb[j]));
      res[j] = ga + gb;
    }
    u32x4 ov;
    ov[0] = pack2(res[0], res[1]);
    ov[1] = pack2(res[2], res[3]);
    ov[2] = pack2(res[4], res[5]);
    ov[3] = pack2(res[6], res[7]);
    *(u32x4*)(out + (size_t)s * DM + n0 + ch * 8) = ov;
  }
  STAMP(2, 6);
}

__global__ __launch_bounds__(512) void k3_kernel(const u16* __restrict__ x, const u16* __restrict__ wo,
                                                 const char* __restrict__ ws, u16* __restrict__ out) {
  __shared__ __attribute__((aligned(16))) char smem[4 * K3_STAGE];
  k3_body(x, wo, ws, out, smem);
}

extern "C" void kernel_launch(void* const* d_in, const int* in_sizes, int n_in, void* d_out, int out_size,
                              void* d_ws, size_t ws_size, hipStream_t stream) {
  const u16* A_q = (const u16*)d_in[0];
  const u16* A_k = (const u16*)d_in[1];
  const u16* A_v = (const u16*)d_in[2];
  const u16* A_shift = (const u16*)d_in[3];
  const u16* A_ssmk = (const u16*)d_in[4];
  const u16* A_ssmD = (const u16*)d_in[5];
  const u16* B_wqkv = (const u16*)d_in[7];
  const u16* B_wo = (const u16*)d_in[8];
  const u16* gate_w = (const u16*)d_in[9];
  char* ws = (char*)d_ws;
  k1_kernel<<<256, 512, 0, stream>>>(A_q, A_k, A_v, A_shift, B_wqkv, A_ssmk, B_wo, A_ssmD, gate_w, (const u16*)d_out, ws);
  k2_kernel<<<512, 512, 0, stream>>>(A_ssmk, A_ssmD, B_wo, A_q, gate_w, ws);
  k3_kernel<<<256, 512, 0, stream>>>(A_q, B_wo, ws, (u16*)d_out);
}
```

```cpp
#define TLB_TOUCH 20
#include <hip/hip_runtime.h>
#include <hip/hip_bf16.h>
#include <stdint.h>

#define DI __device__ __forceinline__
typedef __attribute__((ext_vector_type(8))) __bf16 bf16x8;
typedef __attribute__((ext_vector_type(4))) __bf16 bf16x4;
typedef __attribute__((ext_vector_type(2))) __bf16 bf16x2;
typedef __attribute__((ext_vector_type(4))) short s16x4;
typedef __attribute__((ext_vector_type(16))) float f32x16;
typedef __attribute__((ext_vector_type(4))) float f32x4;
typedef __attribute__((ext_vector_type(2))) float f32x2;
typedef __attribute__((ext_vector_type(4))) unsigned u32x4;
typedef __attribute__((ext_vector_type(2))) unsigned u32x2;
typedef unsigned short u16;

#define SEQ 2048
#define DM 512

#define MFMA32(a, b, c) __builtin_amdgcn_mfma_f32_32x32x16_bf16((a), (b), (c), 0, 0, 0)

DI float bf2f(u16 v) { return __uint_as_float(((unsigned)v) << 16); }
DI float bflo(unsigned v) { return __uint_as_float(v << 16); }
DI float bfhi(unsigned v) { return __uint_as_float(v & 0xffff0000u); }
DI unsigned pack2(float a, float b) {
  f32x2 f = {a, b};
  bf16x2 r = __builtin_convertvector(f, bf16x2);
  return __builtin_bit_cast(unsigned, r);
}
DI float rbf(float a) {
  return (float)((__bf16)a);
}
DI s16x4 tr_read(const char* p) {
  typedef __attribute__((address_space(3))) s16x4* lp;
  return __builtin_amdgcn_ds_read_tr16_b64_v4i16((lp)(p));
}
DI bf16x8 cat_frag(s16x4 lo, s16x4 hi) {
  typedef __attribute__((ext_vector_type(8))) short s16x8;
  s16x8 v = __builtin_shufflevector(lo, hi, 0, 1, 2, 3, 4, 5, 6, 7);
  return __builtin_bit_cast(bf16x8, v);
}

#define STAMP(k, i) do {} while (0)
#define WS_Q 0
#define WS_K (2u << 20)
#define WS_VT (4u << 20)
#define WS_KVT (6u << 20)
#define WS_Y2T (8u << 20)
#define WS_O (10u << 20)
#define WS_G (12u << 20)

#define WT_AUX 0
typedef __amdgpu_buffer_rsrc_t rsrc_t;
DI rsrc_t ws_rsrc(const char* ws) { return __builtin_amdgcn_make_buffer_rsrc((void*)ws, (short)0, 16 << 20, 0x00020000); }
#define AUX_ST1 0
#define AUX_ST2 0
#define AUX_KVT 16
#define AUX_Y2T 16
#define NT_KV
#define PF_LATE 1
#define ATT_MAP
#define AUX_LD2 0
#define AUX_LD3 0
template <int AUX>
DI void wst16(rsrc_t rs, unsigned off, u32x4 v) { __builtin_amdgcn_raw_buffer_store_b128(v, rs, (int)off, 0, AUX); }
template <int AUX>
DI void wst8(rsrc_t rs, unsigned off, u32x2 v) { __builtin_amdgcn_raw_buffer_store_b64(v, rs, (int)off, 0, AUX); }
template <bool NT>
DI u32x4 ld16(const void* p) {
  if constexpr (NT) return __builtin_nontemporal_load((const u32x4*)p);
  else return *(const u32x4*)p;
}

#define GLDS16(g, l) __builtin_amdgcn_global_load_lds((const unsigned*)(g), (unsigned*)(l), 16, 0, 0)
#define GLDS16A(g, l, aux) __builtin_amdgcn_global_load_lds((const unsigned*)(g), (unsigned*)(l), 16, 0, aux)
#define AUX_X 2
#define AUX_W 0
#define WAIT_V(n) asm volatile("s_waitcnt vmcnt(" #n ")" ::: "memory")
#define RAWBAR() do { asm volatile("s_waitcnt lgkmcnt(0)" ::: "memory"); __builtin_amdgcn_s_barrier(); } while (0)
#define K1_STAGE 32768

template <int N>
DI void wait_vm() {
  if constexpr (N == 0) WAIT_V(0);
  else if constexpr (N == 4) WAIT_V(4);
  else if constexpr (N == 8) WAIT_V(8);
  else if constexpr (N == 12) WAIT_V(12);
  else if constexpr (N == 16) WAIT_V(16);
  else if constexpr (N == 24) WAIT_V(24);
}

#define QSCALE (0.25f * 1.44269504088896340736f)

template <int MODE>
DI void k1_gemm(const u16* __restrict__ x, const u16* __restrict__ w, char* __restrict__ ws, char* smem, int tm,
                int tn) {
  const int t = threadIdx.x;
  const int l = t & 63, wv = t >> 6;
  const int r = l & 31, hh = l >> 5;
  const int wm = wv & 1, wn = wv >> 1;
  const int m0 = tm * 128, n0 = tn * 128;

  const u16* xg[2];
  const u16* wg[2];
#pragma unroll
  for (int i = 0; i < 2; ++i) {
    const int ar = 16 * wv + 8 * i + (l >> 3);
    xg[i] = x + (size_t)(m0 + ar) * DM + (((l & 7) ^ ((ar >> 1) & 7)) * 8);
    const int br = 8 * wv + 4 * i + (l >> 4);
    wg[i] = w + (size_t)br * 1536 + n0 + (((l & 15) ^ ((br & 3) << 2)) * 8);
  }
  char* ldsw = smem + wv * 2048 + l * 16;

  const int rot = 0;
  auto issue = [&](int kt) {
    char* st = ldsw + (kt & 3) * K1_STAGE;
    const int kk = (kt + rot) & 7;
#pragma unroll
    for (int i = 0; i < 2; ++i) GLDS16A(xg[i] + kk * 64, st + i * 1024, AUX_X);
#pragma unroll
    for (int i = 0; i < 2; ++i) GLDS16A(wg[i] + (size_t)kk * 64 * 1536, st + 16384 + i * 1024, AUX_W);
  };

  f32x16 acc[2];
#pragma unroll
  for (int i = 0; i < 2; ++i)
#pragma unroll
    for (int e = 0; e < 16; ++e) acc[i][e] = 0.f;

  const int blk = (l >> 4) & 1, q4 = (l & 15) >> 2, p4 = l & 3;
  const int a_row_off = (wm * 64 + r) * 128;
  const int a_sw = (r >> 1) & 7;
  const int b_base = 16384 + (8 * hh + q4) * 256 + (p4 & 1) * 8 + (((wn * 4 + 2 * blk + (p4 >> 1)) ^ (q4 << 2)) * 16);

  STAMP(0, 0);
  issue(0);
  issue(1);
  issue(2);
  issue(3);
  bf16x8 fa[2][4][2], fb[2][4];
  auto ldfr = [&](int kt, bf16x8(&a)[4][2], bf16x8(&b)[4]) {
    const char* st = smem + (kt & 3) * K1_STAGE;
#pragma unroll
    for (int ks = 0; ks < 4; ++ks) {
#pragma unroll
      for (int i = 0; i < 2; ++i)
        a[ks][i] = *(const bf16x8*)(st + a_row_off + i * 32 * 128 + (((2 * ks + hh) ^ a_sw) * 16));
      s16x4 lo = tr_read(st + b_base + (16 * ks) * 256);
      s16x4 hi = tr_read(st + b_base + (16 * ks + 4) * 256);
      b[ks] = cat_frag(lo, hi);
    }
  };
  wait_vm<12>();
  RAWBAR();
  ldfr(0, fa[0], fb[0]);
#define K1_ILV 2
#pragma unroll
  for (int kt = 0; kt < 8; ++kt) {
    const char* stn = smem + ((kt + 1) & 3) * K1_STAGE;
    bf16x8(&an)[4][2] = fa[(kt + 1) & 1];
    bf16x8(&bn)[4] = fb[(kt + 1) & 1];
    s16x4 lo, hi;
    auto rdpair = [&](int j) {
      const int ks = j >> 1, i = j & 1;
      an[ks][i] = *(const bf16x8*)(stn + a_row_off + i * 32 * 128 + (((2 * ks + hh) ^ a_sw) * 16));
      if (i == 0) {
        lo = tr_read(stn + b_base + (16 * ks) * 256);
      } else {
        hi = tr_read(stn + b_base + (16 * ks + 4) * 256);
        bn[ks] = cat_frag(lo, hi);
      }
    };
    if (kt < 7) {
      if (kt <= 4) wait_vm<8>();
      else if (kt == 5) wait_vm<4>();
      else wait_vm<0>();
      RAWBAR();
    }
    __builtin_amdgcn_sched_barrier(0);
#pragma unroll
    for (int g = 0; g < 8; ++g) {
      const int ks = g >> 1, i = g & 1;
      if (MODE == 2)
        acc[i] = MFMA32(fa[kt & 1][ks][i], fb[kt & 1][ks], acc[i]);
      else
        acc[i] = MFMA32(fb[kt & 1][ks], fa[kt & 1][ks][i], acc[i]);
      __builtin_amdgcn_sched_barrier(0);
      if (kt < 7) {
        if (g < 2) {
          rdpair(2 * g);
          rdpair(2 * g + 1);
        } else if (g < 6) {
          rdpair(g + 2);
        }
      }
      if (i == 1 && kt + 4 <= 7) {
        char* st4 = ldsw + (kt & 3) * K1_STAGE;
        const int kk = (kt + 4 + rot) & 7;
        if (ks < 2)
          GLDS16A(xg[ks] + kk * 64, st4 + ks * 1024, AUX_X);
        else
          GLDS16A(wg[ks - 2] + (size_t)kk * 64 * 1536, st4 + 16384 + (ks - 2) * 1024, AUX_W);
      }
      __builtin_amdgcn_sched_barrier(0);
    }
  }
  STAMP(0, 9);
  const rsrc_t rs = ws_rsrc(ws);
  if (MODE == 2) {
#pragma unroll
    for (int i = 0; i < 2; ++i) {
      const int n = (n0 - 1024) + wn * 32 + r;
      const int tok0 = m0 + wm * 64 + i * 32;
      const unsigned dst = WS_VT + (unsigned)(n * SEQ + tok0 + 8 * hh) * 2;
#pragma unroll
      for (int bb = 0; bb < 2; ++bb) {
        u32x4 v;
        v[0] = pack2(acc[i][8 * bb + 0], acc[i][8 * bb + 1]);
        v[1] = pack2(acc[i][8 * bb + 2], acc[i][8 * bb + 3]);
        v[2] = pack2(acc[i][8 * bb + 4], acc[i][8 * bb + 5]);
        v[3] = pack2(acc[i][8 * bb + 6], acc[i][8 * bb + 7]);
        wst16<AUX_ST1>(rs, dst + 32 * bb, v);
      }
    }
  } else {
    const unsigned base = (MODE == 1 ? WS_K : WS_Q);
    const int nn0 = n0 & 511;
#pragma unroll
    for (int i = 0; i < 2; ++i) {
      const int tok = m0 + wm * 64 + i * 32 + r;
      const int nb = nn0 + wn * 32;
      const int head = nb >> 4;
#pragma unroll
      for (int g = 0; g < 4; ++g) {
        u32x2 v;
        const float sc = (MODE == 0) ? QSCALE : 1.f;
        v[0] = pack2(acc[i][4 * g + 0] * sc, acc[i][4 * g + 1] * sc);
        v[1] = pack2(acc[i][4 * g + 2] * sc, acc[i][4 * g + 3] * sc);
        wst8<AUX_ST1>(rs, base + (unsigned)(((head + (g >> 1)) * SEQ + tok) * 16 + 8 * (g & 1) + 4 * hh) * 2, v);
      }
    }
  }
  STAMP(0, 10);
}

__global__ __launch_bounds__(512) void k1_kernel(const u16* __restrict__ x, const u16* __restrict__ kin,
                                                 const u16* __restrict__ vin, const u16* __restrict__ shiftk,
                                                 const u16* __restrict__ wqkv, const u16* __restrict__ ssmk,
                                                 const u16* __restrict__ wo, const u16* __restrict__ ssmD,
                                                 const u16* __restrict__ gatew, const u16* __restrict__ outp,
                                                 char* __restrict__ ws) {
  __shared__ __attribute__((aligned(16))) char smem[4 * K1_STAGE];
  const int xcd = blockIdx.x & 7, bidx = blockIdx.x >> 3;
  if (bidx < 24) {
    const int tm = 2 * xcd + (bidx & 1), tn = bidx >> 1;
    if (tn >= 8)
      k1_gemm<2>(x, wqkv, ws, smem, tm, tn);
    else if (tn >= 4)
      k1_gemm<1>(x, wqkv, ws, smem, tm, tn);
    else
      k1_gemm<0>(x, wqkv, ws, smem, tm, tn);
    return;
  }
  const int l = threadIdx.x & 63, wv = threadIdx.x >> 6;
  const int t = threadIdx.x & 255, h2 = threadIdx.x >> 8;
  const int pb = xcd * 8 + (bidx - 24);
  STAMP(0, 0);
  u32x4 pf[4];
  unsigned tch = 0;
  {
    constexpr int G = TLB_TOUCH;
    constexpr int NWS = (13 << 20) >> G, NOUT = (2 << 20) >> G, NWO = ((1 << 19) >> G) ? ((1 << 19) >> G) : 1;
    constexpr int NTOT = NWS + 2 * NOUT + NWO + 2;
    const int tt = threadIdx.x;
    const char* tp = ws + ((size_t)tt << G);
    if (tt >= NWS) tp = (const char*)outp + ((size_t)(tt - NWS) << G);
    if (tt >= NWS + NOUT) tp = (const char*)ssmk + ((size_t)(tt - NWS - NOUT) << G);
    if (tt >= NWS + 2 * NOUT) tp = (const char*)wo + ((size_t)(tt - NWS - 2 * NOUT) << G);
    if (tt == NTOT - 2) tp = (const char*)ssmD;
    if (tt == NTOT - 1) tp = (const char*)gatew;
    if (tt < NTOT) tch = *(const unsigned*)tp;
  }
  {
    const int ch = t & 15, rg = t >> 4;
    u32x4 kr[7], vr[4], sk[4];
    const int tile = pb * 2 + h2;
    const int s0 = (tile >> 2) * 64, d0 = (tile & 3) * 128;
    {
      const int sb = s0 + rg * 4;
      const int dd = d0 + ch * 8;
#pragma unroll
      for (int i = 0; i < 7; ++i) {
        const int s = sb - 3 + i;
        u32x4 z = {0u, 0u, 0u, 0u};
        kr[i] = (s >= 0) ? __builtin_nontemporal_load((const u32x4*)(kin + (size_t)s * DM + dd)) : z;
      }
#pragma unroll
      for (int i = 0; i < 4; ++i) vr[i] = __builtin_nontemporal_load((const u32x4*)(vin + (size_t)(sb + i) * DM + dd));
#pragma unroll
      for (int i = 0; i < 4; ++i) sk[i] = *(const u32x4*)(shiftk + (size_t)dd * 4 + i * 8);
    }
    __builtin_amdgcn_sched_barrier(0);
    __builtin_amdgcn_sched_barrier(0);
    char* sm = smem + h2 * 17408;
#pragma unroll
    for (int c = 0; c < 4; ++c) {
      const f32x2 w0 = {bflo(sk[c][0]), bflo(sk[c][2])}, w1 = {bfhi(sk[c][0]), bfhi(sk[c][2])};
      const f32x2 w2 = {bflo(sk[c][1]), bflo(sk[c][3])}, w3 = {bfhi(sk[c][1]), bfhi(sk[c][3])};
      f32x2 kk[7];
#pragma unroll
      for (int i = 0; i < 7; ++i) kk[i] = (f32x2){bflo(kr[i][c]), bfhi(kr[i][c])};
      f32x2 r[4];
#pragma unroll
      for (int i = 0; i < 4; ++i) {
        f32x2 a = kk[i] * w3;
        a = __builtin_elementwise_fma(kk[i + 1], w2, a);
        a = __builtin_elementwise_fma(kk[i + 2], w1, a);
        a = __builtin_elementwise_fma(kk[i + 3], w0, a);
        const unsigned ap = pack2(a[0], a[1]);
        r[i] = (f32x2){bflo(ap), bfhi(ap)} * (f32x2){bflo(vr[i][c]), bfhi(vr[i][c])};
      }
      *(u32x2*)(sm + (ch * 8 + 2 * c) * 136 + rg * 8) = (u32x2){pack2(r[0][0], r[1][0]), pack2(r[2][0], r[3][0])};
      *(u32x2*)(sm + (ch * 8 + 2 * c + 1) * 136 + rg * 8) = (u32x2){pack2(r[0][1], r[1][1]), pack2(r[2][1], r[3][1])};
    }
    STAMP(0, 14);
    __builtin_amdgcn_sched_barrier(0);
#pragma unroll
    for (int i = 0; i < 4; ++i) pf[i] = *(const u32x4*)(ssmk + (size_t)pb * 16384 + (i * 512 + threadIdx.x) * 8);
    __builtin_amdgcn_sched_barrier(0);
    RAWBAR();
    STAMP(0, 15);
    const rsrc_t rs = ws_rsrc(ws);
#pragma unroll
    for (int i = 0; i < 4; ++i) {
      const int idx = t + 256 * i;
      const int dr = idx >> 3, c8 = idx & 7;
      const u32x2 a = *(const u32x2*)(sm + dr * 136 + c8 * 16);
      const u32x2 b = *(const u32x2*)(sm + dr * 136 + c8 * 16 + 8);
      u32x4 v = {a[0], a[1], b[0], b[1]};
      wst16<AUX_KVT>(rs, WS_KVT + (unsigned)((d0 + dr) * SEQ + s0 + c8 * 8) * 2, v);
    }
  }
  STAMP(0, 11);
#pragma unroll
  for (int i = 0; i < 4; ++i) asm volatile("" ::"v"(pf[i]));
  asm volatile("" ::"v"(tch));
  STAMP(0, 12);
}

#define CV_KVSTR 80
#define CV_HSTR 4160
#define CV_HBYTES (8 * CV_HSTR)
#define CV_REDSTR 36
#define CV_SMEM 40960

template <bool FIRST>
DI void attn_block(const char* __restrict__ ws, char* smem, int head, int qb) {
  const int l = threadIdx.x & 63, wv = threadIdx.x >> 6;
  const int r = l & 31, hh = l >> 5;
  const u16* Qh = (const u16*)(ws + WS_Q) + (size_t)head * SEQ * 16;
  const u16* Kh = (const u16*)(ws + WS_K) + (size_t)head * SEQ * 16;
  const u16* Vh = (const u16*)(ws + WS_VT) + (size_t)head * 16 * SEQ;
  u16* O = (u16*)(ws + WS_O);
  const int q0 = qb + 32 * wv;
  const int kt0 = FIRST ? (8 - wv) : 0;
  STAMP(1, 0);
  char* ks_ = smem;
  char* vs_ = smem + 16384;
  char* cs_ = smem + 32768;
  {
    const int key = l >> 1;
    const u16* ksrc = Kh + (size_t)key * 16 + (((l & 1) ^ ((key >> 3) & 1)) * 8);
    const int dd = l >> 2;
    const u16* vsrc = Vh + (size_t)dd * SEQ + (((l & 3) ^ ((dd >> 2) & 3)) * 8);
#pragma unroll
    for (int i = 0; i < 2; ++i) {
      const int u = wv + 8 * i;
      int key0 = qb - 256 + 32 * u;
      key0 = key0 < 0 ? 0 : key0;
      GLDS16A(ksrc + (size_t)key0 * 16, ks_ + u * 1024 + l * 16, AUX_LD2);
      GLDS16A(vsrc + key0, vs_ + u * 1024 + l * 16, AUX_LD2);
    }
  }
  const bf16x8 qf = __builtin_bit_cast(bf16x8, ld16<AUX_LD2 != 0>(Qh + (size_t)(q0 + r) * 16 + 8 * hh));
  if (threadIdx.x < 2) {
    const unsigned o = threadIdx.x == 0 ? 0x3f803f80u : 0u;
    *(u32x4*)(cs_ + threadIdx.x * 16) = (u32x4){o, o, o, o};
  }
  WAIT_V(0);
  RAWBAR();
  STAMP(1, 1);
  const char* krd = ks_ + (32 * wv + r) * 32 + ((hh ^ ((r >> 3) & 1)) * 16);
  f32x16 zero;
#pragma unroll
  for (int e = 0; e < 16; ++e) zero[e] = 0.f;
  const float NEG = -3.0e38f;
  auto mask = [&](int kt, f32x16& s) {
    if (kt == 0) {
#pragma unroll
      for (int e = 0; e < 16; ++e) s[e] = ((e & 3) + 8 * (e >> 2) + 4 * hh > r) ? s[e] : NEG;
    }
    if (kt == 8) {
#pragma unroll
      for (int e = 0; e < 16; ++e) s[e] = ((e & 3) + 8 * (e >> 2) + 4 * hh <= r) ? s[e] : NEG;
    }
  };
  float mx = NEG, mx2 = NEG;
  {
    bf16x8 kf[9];
#pragma unroll
    for (int kt = 0; kt < 9; ++kt) kf[kt] = *(const bf16x8*)(krd + kt * 1024);
    f32x16 s[3];
    s[0] = MFMA32(kf[0], qf, zero);
    s[1] = MFMA32(kf[1], qf, zero);
#pragma unroll
    for (int kt = 0; kt < 9; ++kt) {
      if (kt < 7) s[(kt + 2) % 3] = MFMA32(kf[kt + 2], qf, zero);
      f32x16& c = s[kt % 3];
      mask(kt, c);
      float m = NEG, m2 = NEG;
#pragma unroll
      for (int e = 0; e < 8; e += 2) {
        m = fmaxf(fmaxf(c[e], c[e + 1]), m);
        m2 = fmaxf(fmaxf(c[8 + e], c[9 + e]), m2);
      }
      if (FIRST) {
        m = (kt >= kt0) ? m : NEG;
        m2 = (kt >= kt0) ? m2 : NEG;
      }
      mx = fmaxf(mx, m);
      mx2 = fmaxf(mx2, m2);
    }
  }
  mx = fmaxf(mx, mx2);
  mx = fmaxf(mx, __shfl_xor(mx, 32));
  f32x16 negm;
#pragma unroll
  for (int e = 0; e < 16; ++e) negm[e] = -mx;
  STAMP(1, 2);
  const int vsw = (r >> 2) & 3;
  const bool isd = r < 16;
  const int cad = 32768 + ((r == 16 || r == 20) ? 0 : 16);
  int va0 = isd ? 16384 + wv * 1024 + r * 64 + ((hh ^ vsw) * 16) : cad;
  int va1 = isd ? 16384 + wv * 1024 + r * 64 + (((2 + hh) ^ vsw) * 16) : cad;
  const int vstep = isd ? 1024 : 0;
  f32x16 oacc = zero;
  {
    f32x16 s[2];
    bf16x8 kf = *(const bf16x8*)(krd);
    s[0] = MFMA32(kf, qf, negm);
#pragma unroll
    for (int kt = 0; kt < 9; ++kt) {
      const bf16x8 vf0 = *(const bf16x8*)(smem + va0);
      const bf16x8 vf1 = *(const bf16x8*)(smem + va1);
      va0 += vstep;
      va1 += vstep;
      if (kt < 8) {
        kf = *(const bf16x8*)(krd + (kt + 1) * 1024);
        s[(kt + 1) & 1] = MFMA32(kf, qf, negm);
      }
      f32x16& c = s[kt & 1];
      mask(kt, c);
      if (!FIRST || kt >= kt0) {
        u32x4 pp[2];
#pragma unroll
        for (int e = 0; e < 16; e += 2) {
          pp[e >> 3][(e >> 1) & 3] = pack2(__builtin_amdgcn_exp2f(c[e]), __builtin_amdgcn_exp2f(c[e + 1]));
        }
        oacc = MFMA32(vf0, __builtin_bit_cast(bf16x8, pp[0]), oacc);
        oacc = MFMA32(vf1, __builtin_bit_cast(bf16x8, pp[1]), oacc);
      }
    }
  }
  STAMP(1, 3);
  const float inv = 1.f / oacc[8];
  u32x2 v0 = {pack2(oacc[0] * inv, oacc[1] * inv), pack2(oacc[2] * inv, oacc[3] * inv)};
  u32x2 v1 = {pack2(oacc[4] * inv, oacc[5] * inv), pack2(oacc[6] * inv, oacc[7] * inv)};
  const rsrc_t rs = ws_rsrc(ws);
  const unsigned op = WS_O + (unsigned)((q0 + r) * DM + head * 16 + 4 * hh) * 2;
  wst8<AUX_ST2>(rs, op, v0);
  wst8<AUX_ST2>(rs, op + 16, v1);
  STAMP(1, 4);
}

DI void conv_block(const u16* __restrict__ ssmk, const u16* __restrict__ ssmD, char* __restrict__ ws, char* smem,
                   int d) {
  const int t = threadIdx.x & 255;
  const int l = t & 63, wv = t >> 6;
  const int r = l & 31, hh = l >> 5;
  char* hs = smem;
  char* kvs = smem + CV_HBYTES;
  const u16* kvT = (const u16*)(ws + WS_KVT) + (size_t)d * SEQ;
  const u16* hrow = ssmk + (size_t)d * SEQ;
  STAMP(1, 0);
  const u32x4 kvv = ld16<AUX_LD2 != 0>(kvT + 8 * t);
  const u32x4 whi = *(const u32x4*)(hrow + 8 * t);
  const u32x4 wlo0 = *(const u32x4*)(hrow + 8 * (t > 0 ? t - 1 : 0));
  const float Dd = bf2f(ssmD[d]);
  *(u32x4*)(kvs + ((t >> 2) + 32) * CV_KVSTR + (t & 3) * 16) = kvv;
  if (t < 128) {
    u32x4 z = {0u, 0u, 0u, 0u};
    *(u32x4*)(kvs + (t >> 2) * CV_KVSTR + (t & 3) * 16) = z;
  }
  {
    const int E = t;
    u32x4 wlo = wlo0;
    if (E == 0) wlo = (u32x4){0u, 0u, 0u, 0u};
    unsigned z[8];
#pragma unroll
    for (int i = 0; i < 4; ++i) {
      const unsigned a = whi[3 - i];
      z[i] = (a >> 16) | (a << 16);
      const unsigned b = wlo[3 - i];
      z[4 + i] = (b >> 16) | (b << 16);
    }
    const int M = 255 - E;
#pragma unroll
    for (int p = 0; p < 8; ++p) {
      const int o = 7 - p;
      u32x4 v;
      if ((o & 1) == 0) {
        v[0] = z[o / 2];
        v[1] = z[o / 2 + 1];
        v[2] = z[o / 2 + 2];
        v[3] = z[o / 2 + 3];
      } else {
        v[0] = (z[o / 2] >> 16) | (z[o / 2 + 1] << 16);
        v[1] = (z[o / 2 + 1] >> 16) | (z[o / 2 + 2] << 16);
        v[2] = (z[o / 2 + 2] >> 16) | (z[o / 2 + 3] << 16);
        v[3] = (z[o / 2 + 3] >> 16) | (z[o / 2 + 4] << 16);
      }
      *(u32x4*)(hs + p * CV_HSTR + 16 * M) = v;
    }
    if (t < 24) {
      u32x4 zz = {0u, 0u, 0u, 0u};
      *(u32x4*)(hs + (t & 7) * CV_HSTR + 16 * (256 + (t >> 3))) = zz;
    }
  }
  __syncthreads();
  STAMP(1, 1);
  f32x16 acc0, acc1, acc2;
#pragma unroll
  for (int e = 0; e < 16; ++e) {
    acc0[e] = 0.f;
    acc1[e] = 0.f;
    acc2[e] = 0.f;
  }
  const char* abase = hs + (r & 7) * CV_HSTR + 16 * (255 - (r >> 3) + hh) - 64 * wv;
  const char* bbase = kvs + (r + 32 - wv) * CV_KVSTR + 16 * hh;
  {
    bf16x8 fa[2][2], fb[2][2];
    auto ldc = [&](int step, bf16x8(&a)[2], bf16x8(&b)[2]) {
      const int i = step >> 1, ks = step & 1;
      a[0] = *(const bf16x8*)(abase - 256 * i + 32 * ks);
      a[1] = *(const bf16x8*)(abase - 256 * i - 2048 + 32 * ks);
      b[1] = *(const bf16x8*)(bbase + (32 - 4 * i) * CV_KVSTR + 32 * ks);
      b[0] = *(const bf16x8*)(bbase - (4 * i) * CV_KVSTR + 32 * ks);
    };
    ldc(0, fa[0], fb[0]);
#pragma unroll
    for (int step = 0; step < 16; ++step) {
      if (step < 15) ldc(step + 1, fa[(step + 1) & 1], fb[(step + 1) & 1]);
      __builtin_amdgcn_sched_barrier(0);
      acc1 = MFMA32(fa[step & 1][0], fb[step & 1][1], acc1);
      acc0 = MFMA32(fa[step & 1][0], fb[step & 1][0], acc0);
      acc2 = MFMA32(fa[step & 1][1], fb[step & 1][0], acc2);
      __builtin_amdgcn_sched_barrier(0);
    }
  }
  acc1 += acc2;
  STAMP(1, 2);
  __syncthreads();
  STAMP(1, 6);
  float* red = (float*)smem;
  {
    float* rw = red + wv * (64 * CV_REDSTR);
#pragma unroll
    for (int g = 0; g < 4; ++g) {
      f32x4 v0 = {acc0[4 * g], acc0[4 * g + 1], acc0[4 * g + 2], acc0[4 * g + 3]};
      f32x4 v1 = {acc1[4 * g], acc1[4 * g + 1], acc1[4 * g + 2], acc1[4 * g + 3]};
      *(f32x4*)(rw + r * CV_REDSTR + 8 * g + 4 * hh) = v0;
      *(f32x4*)(rw + (32 + r) * CV_REDSTR + 8 * g + 4 * hh) = v1;
    }
  }
  __syncthreads();
  STAMP(1, 7);
  {
    const int a = t >> 2, b0 = 8 * (t & 3);
    f32x4 s0 = {0.f, 0.f, 0.f, 0.f}, s1 = {0.f, 0.f, 0.f, 0.f};
#pragma unroll
    for (int w = 0; w < 4; ++w) {
      const float* rp = red + w * (64 * CV_REDSTR) + a * CV_REDSTR + b0;
      s0 += *(const f32x4*)(rp);
      s1 += *(const f32x4*)(rp + 4);
    }
    float yy[8] = {s0[0], s0[1], s0[2], s0[3], s1[0], s1[1], s1[2], s1[3]};
    u32x4 o;
#pragma unroll
    for (int i = 0; i < 4; ++i) {
      const float y0 = rbf(yy[2 * i]) + rbf(bflo(kvv[i]) * Dd);
      const float y1 = rbf(yy[2 * i + 1]) + rbf(bfhi(kvv[i]) * Dd);
      o[i] = pack2(y0, y1);
    }
    wst16<AUX_Y2T>(ws_rsrc(ws), WS_Y2T + (unsigned)(d * SEQ + 8 * t) * 2, o);
  }
  STAMP(1, 4);
}

__global__ __launch_bounds__(512, 4) void k2_kernel(const u16* __restrict__ ssmk, const u16* __restrict__ ssmD,
                                                    const u16* __restrict__ wo, const u16* __restrict__ x,
                                                    const u16* __restrict__ gatew, char* __restrict__ ws) {
  __shared__ __attribute__((aligned(16))) char smem[2 * CV_SMEM];
  const int xcd = blockIdx.x & 7, bidx = (blockIdx.x >> 3) ^ 32;
  if (bidx >= 32) {
#define PF_WO 2
    const int half = threadIdx.x >> 8;
    conv_block(ssmk, ssmD, ws, smem + half * CV_SMEM, xcd * 64 + (bidx - 32) * 2 + half);
    const u32x4 pf0 = *(const u32x4*)(wo + (size_t)(bidx - 32) * 8192 + threadIdx.x * 8);
    const u32x4 pf1 = *(const u32x4*)(wo + (size_t)(bidx - 32) * 8192 + 4096 + threadIdx.x * 8);
    {
      const int l = threadIdx.x & 63;
      const int row = (xcd * 32 + (bidx - 32)) * 8 + (threadIdx.x >> 6);
      const u32x4 gw = *(const u32x4*)(gatew + l * 8);
      const u32x4 xv = ld16<false>(x + (size_t)row * DM + l * 8);
      float sum = 0.f;
#pragma unroll
      for (int i = 0; i < 4; ++i) {
        const unsigned pr = pack2(bflo(xv[i]) * bflo(gw[i]), bfhi(xv[i]) * bfhi(gw[i]));
        sum += bflo(pr);
        sum += bfhi(pr);
      }
#pragma unroll
      for (int o = 32; o >= 1; o >>= 1) sum += __shfl_xor(sum, o);
      if (l == 0) ((float*)(ws + WS_G))[row] = rbf(1.f / (1.f + __expf(-rbf(sum))));
    }
    asm volatile("" ::"v"(pf0));
    asm volatile("" ::"v"(pf1));
  } else {
    const int head = 4 * xcd + (bidx & 3), win = bidx >> 2;
    if (win == 0)
      attn_block<true>(ws, smem, head, 0);
    else
      attn_block<false>(ws, smem, head, win * 256);
  }
}

#define K3_STAGE 32768
#define K3_CSTR 68
#define K3_YSTR 136

DI void k3_body(const u16* __restrict__ x, const u16* __restrict__ wo, const char* __restrict__ ws,
                u16* __restrict__ out, char* smem) {
  const int t = threadIdx.x;
  const int l = t & 63, wv = t >> 6;
  const int r = l & 31, hh = l >> 5;
  const int wm = wv & 1, wn = (wv >> 1) & 1, kh = wv >> 2;
  const int tm = (blockIdx.x & 7) * 4 + ((blockIdx.x >> 3) & 3), tn = blockIdx.x >> 5;
  const int m0 = tm * 64, n0 = tn * 64;
  const u16* o = (const u16*)(ws + WS_O);
  STAMP(2, 0);

  const u16* y2T = (const u16*)(ws + WS_Y2T);
  const float* gp = (const float*)(ws + WS_G);
  const int rr = t >> 3, ch = t & 7;
  const u32x4 yv = ld16<AUX_LD3 != 0>(y2T + (size_t)(n0 + rr) * SEQ + m0 + ch * 8);
  const u32x4 xq = ld16<false>(x + (size_t)(m0 + rr) * DM + n0 + ch * 8);
  const float g = gp[m0 + rr];
  __builtin_amdgcn_sched_barrier(0);
  {
    char* ldsw = smem + wv * 2048 + l * 16;
#pragma unroll
    for (int s4 = 0; s4 < 4; ++s4) {
      const int st = s4;
#pragma unroll
      for (int i = 0; i < 2; ++i) {
        const int ar = 8 * wv + 4 * i + (l >> 4);
        GLDS16A(o + (size_t)(m0 + ar) * DM + st * 128 + (((l & 15) ^ (ar & 15)) * 8), ldsw + st * K3_STAGE + i * 1024,
                AUX_LD3);
      }
#pragma unroll
      for (int i = 0; i < 2; ++i) {
        const int br = 16 * wv + 8 * i + (l >> 3);
        GLDS16(wo + (size_t)(st * 128 + br) * DM + n0 + (((l & 7) ^ (((br >> 1) & 1) << 2)) * 8),
               ldsw + st * K3_STAGE + 16384 + i * 1024);
      }
    }
  }

  f32x16 acc;
#pragma unroll
  for (int e = 0; e < 16; ++e) acc[e] = 0.f;
  const int blk = (l >> 4) & 1, q4 = (l & 15) >> 2, p4 = l & 3;
  const int arow = wm * 32 + r;
  const int a_off = arow * 256;
  const int a_sw = arow & 15;
  const int b_off = 16384 + (8 * hh + q4) * 128 + (((wn * 4 + 2 * blk + (p4 >> 1)) ^ (((q4 >> 1) & 1) << 2)) * 16) +
                    (p4 & 1) * 8;
  const int a_x = (8 * kh + hh) ^ a_sw;
  const int b_k = b_off + 64 * kh * 128;
#pragma unroll
  for (int st = 0; st < 4; ++st) {
    if (st == 0) WAIT_V(12);
    else if (st == 1) WAIT_V(8);
    else if (st == 2) WAIT_V(4);
    else WAIT_V(0);
    RAWBAR();
    const char* sb = smem + st * K3_STAGE;
    bf16x8 af[4], bfr[4];
#pragma unroll
    for (int ks = 0; ks < 4; ++ks) {
      af[ks] = *(const bf16x8*)(sb + a_off + ((a_x ^ (2 * ks)) * 16));
      s16x4 lo = tr_read(sb + b_k + (16 * ks) * 128);
      s16x4 hi = tr_read(sb + b_k + (16 * ks + 4) * 128);
      bfr[ks] = cat_frag(lo, hi);
    }
    __builtin_amdgcn_sched_barrier(0);
#pragma unroll
    for (int ks = 0; ks < 4; ++ks) acc = MFMA32(af[ks], bfr[ks], acc);
    __builtin_amdgcn_sched_barrier(0);
  }
  __syncthreads();
  STAMP(2, 5);
  float* Cs = (float*)smem;
  char* Ys = smem + 2 * 64 * K3_CSTR * 4;
#pragma unroll
  for (int e = 0; e < 16; ++e) {
    const int row = wm * 32 + (e & 3) + 8 * (e >> 2) + 4 * hh;
    Cs[kh * 64 * K3_CSTR + row * K3_CSTR + wn * 32 + r] = acc[e];
  }
  {
    u32x2 a = {yv[0], yv[1]}, b = {yv[2], yv[3]};
    *(u32x2*)(Ys + rr * K3_YSTR + ch * 16) = a;
    *(u32x2*)(Ys + rr * K3_YSTR + ch * 16 + 8) = b;
  }
  __syncthreads();
  {
    const int row = rr;
    const int s = m0 + row;
    const float omg = rbf(1.f - g);
    const f32x4 c0 = *(const f32x4*)(Cs + row * K3_CSTR + ch * 8) + *(const f32x4*)(Cs + (64 + row) * K3_CSTR + ch * 8);
    const f32x4 c1 =
        *(const f32x4*)(Cs + row * K3_CSTR + ch * 8 + 4) + *(const f32x4*)(Cs + (64 + row) * K3_CSTR + ch * 8 + 4);
    float bb[8] = {c0[0], c0[1], c0[2], c0[3], c1[0], c1[1], c1[2], c1[3]};
    float res[8];
#pragma unroll
    for (int j = 0; j < 8; ++j) {
      const float y2 = bf2f(*(const u16*)(Ys + (ch * 8 + j) * K3_YSTR + row * 2));
      const float qv = (j & 1) ? bfhi(xq[j >> 1]) : bflo(xq[j >> 1]);
      const float a = rbf(qv * y2);
      const float ga = rbf(g * a);
      const float gb = rbf(omg * rbf(bb[j]));
      res[j] = ga + gb;
    }
    u32x4 ov;
    ov[0] = pack2(res[0], res[1]);
    ov[1] = pack2(res[2], res[3]);
    ov[2] = pack2(res[4], res[5]);
    ov[3] = pack2(res[6], res[7]);
    *(u32x4*)(out + (size_t)s * DM + n0 + ch * 8) = ov;
  }
  STAMP(2, 6);
}

__global__ __launch_bounds__(512) void k3_kernel(const u16* __restrict__ x, const u16* __restrict__ wo,
                                                 const char* __restrict__ ws, u16* __restrict__ out) {
  __shared__ __attribute__((aligned(16))) char smem[4 * K3_STAGE];
  k3_body(x, wo, ws, out, smem);
}

extern "C" void kernel_launch(void* const* d_in, const int* in_sizes, int n_in, void* d_out, int out_size,
                              void* d_ws, size_t ws_size, hipStream_t stream) {
  const u16* A_q = (const u16*)d_in[0];
  const u16* A_k = (const u16*)d_in[1];
  const u16* A_v = (const u16*)d_in[2];
  const u16* A_shift = (const u16*)d_in[3];
  const u16* A_ssmk = (const u16*)d_in[4];
  const u16* A_ssmD = (const u16*)d_in[5];
  const u16* B_wqkv = (const u16*)d_in[7];
  const u16* B_wo = (const u16*)d_in[8];
  const u16* gate_w = (const u16*)d_in[9];
  char* ws = (char*)d_ws;
  k1_kernel<<<256, 512, 0, stream>>>(A_q, A_k, A_v, A_shift, B_wqkv, A_ssmk, B_wo, A_ssmD, gate_w, (const u16*)d_out, ws);
  k2_kernel<<<512, 512, 0, stream>>>(A_ssmk, A_ssmD, B_wo, A_q, gate_w, ws);
  k3_kernel<<<256, 512, 0, stream>>>(A_q, B_wo, ws, (u16*)d_out);
}
```

```cpp
#define ATT_MERGE
#include <hip/hip_runtime.h>
#include <hip/hip_bf16.h>
#include <stdint.h>

#define DI __device__ __forceinline__
typedef __attribute__((ext_vector_type(8))) __bf16 bf16x8;
typedef __attribute__((ext_vector_type(4))) __bf16 bf16x4;
typedef __attribute__((ext_vector_type(2))) __bf16 bf16x2;
typedef __attribute__((ext_vector_type(4))) short s16x4;
typedef __attribute__((ext_vector_type(16))) float f32x16;
typedef __attribute__((ext_vector_type(4))) float f32x4;
typedef __attribute__((ext_vector_type(2))) float f32x2;
typedef __attribute__((ext_vector_type(4))) unsigned u32x4;
typedef __attribute__((ext_vector_type(2))) unsigned u32x2;
typedef unsigned short u16;

#define SEQ 2048
#define DM 512

#define MFMA32(a, b, c) __builtin_amdgcn_mfma_f32_32x32x16_bf16((a), (b), (c), 0, 0, 0)

DI float bf2f(u16 v) { return __uint_as_float(((unsigned)v) << 16); }
DI float bflo(unsigned v) { return __uint_as_float(v << 16); }
DI float bfhi(unsigned v) { return __uint_as_float(v & 0xffff0000u); }
DI unsigned pack2(float a, float b) {
  f32x2 f = {a, b};
  bf16x2 r = __builtin_convertvector(f, bf16x2);
  return __builtin_bit_cast(unsigned, r);
}
DI float rbf(float a) {
  return (float)((__bf16)a);
}
DI s16x4 tr_read(const char* p) {
  typedef __attribute__((address_space(3))) s16x4* lp;
  return __builtin_amdgcn_ds_read_tr16_b64_v4i16((lp)(p));
}
DI bf16x8 cat_frag(s16x4 lo, s16x4 hi) {
  typedef __attribute__((ext_vector_type(8))) short s16x8;
  s16x8 v = __builtin_shufflevector(lo, hi, 0, 1, 2, 3, 4, 5, 6, 7);
  return __builtin_bit_cast(bf16x8, v);
}

#define STAMP(k, i) do {} while (0)
#define WS_Q 0
#define WS_K (2u << 20)
#define WS_VT (4u << 20)
#define WS_KVT (6u << 20)
#define WS_Y2T (8u << 20)
#define WS_O (10u << 20)
#define WS_G (12u << 20)

#define WT_AUX 0
typedef __amdgpu_buffer_rsrc_t rsrc_t;
DI rsrc_t ws_rsrc(const char* ws) { return __builtin_amdgcn_make_buffer_rsrc((void*)ws, (short)0, 16 << 20, 0x00020000); }
#define AUX_ST1 0
#define AUX_ST2 0
#define AUX_KVT 16
#define AUX_Y2T 16
#define NT_KV
#define PF_LATE 1
#define ATT_MAP
#define AUX_LD2 0
#define AUX_LD3 0
template <int AUX>
DI void wst16(rsrc_t rs, unsigned off, u32x4 v) { __builtin_amdgcn_raw_buffer_store_b128(v, rs, (int)off, 0, AUX); }
template <int AUX>
DI void wst8(rsrc_t rs, unsigned off, u32x2 v) { __builtin_amdgcn_raw_buffer_store_b64(v, rs, (int)off, 0, AUX); }
template <bool NT>
DI u32x4 ld16(const void* p) {
  if constexpr (NT) return __builtin_nontemporal_load((const u32x4*)p);
  else return *(const u32x4*)p;
}

#define GLDS16(g, l) __builtin_amdgcn_global_load_lds((const unsigned*)(g), (unsigned*)(l), 16, 0, 0)
#define GLDS16A(g, l, aux) __builtin_amdgcn_global_load_lds((const unsigned*)(g), (unsigned*)(l), 16, 0, aux)
#define AUX_X 2
#define AUX_W 0
#define WAIT_V(n) asm volatile("s_waitcnt vmcnt(" #n ")" ::: "memory")
#define RAWBAR() do { asm volatile("s_waitcnt lgkmcnt(0)" ::: "memory"); __builtin_amdgcn_s_barrier(); } while (0)
#define K1_STAGE 32768

template <int N>
DI void wait_vm() {
  if constexpr (N == 0) WAIT_V(0);
  else if constexpr (N == 4) WAIT_V(4);
  else if constexpr (N == 8) WAIT_V(8);
  else if constexpr (N == 12) WAIT_V(12);
  else if constexpr (N == 16) WAIT_V(16);
  else if constexpr (N == 24) WAIT_V(24);
}

#define QSCALE (0.25f * 1.44269504088896340736f)

template <int MODE>
DI void k1_gemm(const u16* __restrict__ x, const u16* __restrict__ w, char* __restrict__ ws, char* smem, int tm,
                int tn) {
  const int t = threadIdx.x;
  const int l = t & 63, wv = t >> 6;
  const int r = l & 31, hh = l >> 5;
  const int wm = wv & 1, wn = wv >> 1;
  const int m0 = tm * 128, n0 = tn * 128;

  const u16* xg[2];
  const u16* wg[2];
#pragma unroll
  for (int i = 0; i < 2; ++i) {
    const int ar = 16 * wv + 8 * i + (l >> 3);
    xg[i] = x + (size_t)(m0 + ar) * DM + (((l & 7) ^ ((ar >> 1) & 7)) * 8);
    const int br = 8 * wv + 4 * i + (l >> 4);
    wg[i] = w + (size_t)br * 1536 + n0 + (((l & 15) ^ ((br & 3) << 2)) * 8);
  }
  char* ldsw = smem + wv * 2048 + l * 16;

  const int rot = 0;
  auto issue = [&](int kt) {
    char* st = ldsw + (kt & 3) * K1_STAGE;
    const int kk = (kt + rot) & 7;
#pragma unroll
    for (int i = 0; i < 2; ++i) GLDS16A(xg[i] + kk * 64, st + i * 1024, AUX_X);
#pragma unroll
    for (int i = 0; i < 2; ++i) GLDS16A(wg[i] + (size_t)kk * 64 * 1536, st + 16384 + i * 1024, AUX_W);
  };

  f32x16 acc[2];
#pragma unroll
  for (int i = 0; i < 2; ++i)
#pragma unroll
    for (int e = 0; e < 16; ++e) acc[i][e] = 0.f;

  const int blk = (l >> 4) & 1, q4 = (l & 15) >> 2, p4 = l & 3;
  const int a_row_off = (wm * 64 + r) * 128;
  const int a_sw = (r >> 1) & 7;
  const int b_base = 16384 + (8 * hh + q4) * 256 + (p4 & 1) * 8 + (((wn * 4 + 2 * blk + (p4 >> 1)) ^ (q4 << 2)) * 16);

  STAMP(0, 0);
  issue(0);
  issue(1);
  issue(2);
  issue(3);
  bf16x8 fa[2][4][2], fb[2][4];
  auto ldfr = [&](int kt, bf16x8(&a)[4][2], bf16x8(&b)[4]) {
    const char* st = smem + (kt & 3) * K1_STAGE;
#pragma unroll
    for (int ks = 0; ks < 4; ++ks) {
#pragma unroll
      for (int i = 0; i < 2; ++i)
        a[ks][i] = *(const bf16x8*)(st + a_row_off + i * 32 * 128 + (((2 * ks + hh) ^ a_sw) * 16));
      s16x4 lo = tr_read(st + b_base + (16 * ks) * 256);
      s16x4 hi = tr_read(st + b_base + (16 * ks + 4) * 256);
      b[ks] = cat_frag(lo, hi);
    }
  };
  wait_vm<12>();
  RAWBAR();
  ldfr(0, fa[0], fb[0]);
#define K1_ILV 2
#pragma unroll
  for (int kt = 0; kt < 8; ++kt) {
    const char* stn = smem + ((kt + 1) & 3) * K1_STAGE;
    bf16x8(&an)[4][2] = fa[(kt + 1) & 1];
    bf16x8(&bn)[4] = fb[(kt + 1) & 1];
    s16x4 lo, hi;
    auto rdpair = [&](int j) {
      const int ks = j >> 1, i = j & 1;
      an[ks][i] = *(const bf16x8*)(stn + a_row_off + i * 32 * 128 + (((2 * ks + hh) ^ a_sw) * 16));
      if (i == 0) {
        lo = tr_read(stn + b_base + (16 * ks) * 256);
      } else {
        hi = tr_read(stn + b_base + (16 * ks + 4) * 256);
        bn[ks] = cat_frag(lo, hi);
      }
    };
    if (kt < 7) {
      if (kt <= 4) wait_vm<8>();
      else if (kt == 5) wait_vm<4>();
      else wait_vm<0>();
      RAWBAR();
    }
    __builtin_amdgcn_sched_barrier(0);
#pragma unroll
    for (int g = 0; g < 8; ++g) {
      const int ks = g >> 1, i = g & 1;
      if (MODE == 2)
        acc[i] = MFMA32(fa[kt & 1][ks][i], fb[kt & 1][ks], acc[i]);
      else
        acc[i] = MFMA32(fb[kt & 1][ks], fa[kt & 1][ks][i], acc[i]);
      __builtin_amdgcn_sched_barrier(0);
      if (kt < 7) {
        if (g < 2) {
          rdpair(2 * g);
          rdpair(2 * g + 1);
        } else if (g < 6) {
          rdpair(g + 2);
        }
      }
      if (i == 1 && kt + 4 <= 7) {
        char* st4 = ldsw + (kt & 3) * K1_STAGE;
        const int kk = (kt + 4 + rot) & 7;
        if (ks < 2)
          GLDS16A(xg[ks] + kk * 64, st4 + ks * 1024, AUX_X);
        else
          GLDS16A(wg[ks - 2] + (size_t)kk * 64 * 1536, st4 + 16384 + (ks - 2) * 1024, AUX_W);
      }
      __builtin_amdgcn_sched_barrier(0);
    }
  }
  STAMP(0, 9);
  const rsrc_t rs = ws_rsrc(ws);
  if (MODE == 2) {
#pragma unroll
    for (int i = 0; i < 2; ++i) {
      const int n = (n0 - 1024) + wn * 32 + r;
      const int tok0 = m0 + wm * 64 + i * 32;
      const unsigned dst = WS_VT + (unsigned)(n * SEQ + tok0 + 8 * hh) * 2;
#pragma unroll
      for (int bb = 0; bb < 2; ++bb) {
        u32x4 v;
        v[0] = pack2(acc[i][8 * bb + 0], acc[i][8 * bb + 1]);
        v[1] = pack2(acc[i][8 * bb + 2], acc[i][8 * bb + 3]);
        v[2] = pack2(acc[i][8 * bb + 4], acc[i][8 * bb + 5]);
        v[3] = pack2(acc[i][8 * bb + 6], acc[i][8 * bb + 7]);
        wst16<AUX_ST1>(rs, dst + 32 * bb, v);
      }
    }
  } else {
    const unsigned base = (MODE == 1 ? WS_K : WS_Q);
    const int nn0 = n0 & 511;
#pragma unroll
    for (int i = 0; i < 2; ++i) {
      const int tok = m0 + wm * 64 + i * 32 + r;
      const int nb = nn0 + wn * 32;
      const int head = nb >> 4;
#pragma unroll
      for (int g = 0; g < 4; ++g) {
        u32x2 v;
        const float sc = (MODE == 0) ? QSCALE : 1.f;
        v[0] = pack2(acc[i][4 * g + 0] * sc, acc[i][4 * g + 1] * sc);
        v[1] = pack2(acc[i][4 * g + 2] * sc, acc[i][4 * g + 3] * sc);
        wst8<AUX_ST1>(rs, base + (unsigned)(((head + (g >> 1)) * SEQ + tok) * 16 + 8 * (g & 1) + 4 * hh) * 2, v);
      }
    }
  }
  STAMP(0, 10);
}

__global__ __launch_bounds__(512) void k1_kernel(const u16* __restrict__ x, const u16* __restrict__ kin,
                                                 const u16* __restrict__ vin, const u16* __restrict__ shiftk,
                                                 const u16* __restrict__ wqkv, const u16* __restrict__ ssmk,
                                                 char* __restrict__ ws) {
  __shared__ __attribute__((aligned(16))) char smem[4 * K1_STAGE];
  const int xcd = blockIdx.x & 7, bidx = blockIdx.x >> 3;
  if (bidx < 24) {
    const int tm = 2 * xcd + (bidx & 1), tn = bidx >> 1;
    if (tn >= 8)
      k1_gemm<2>(x, wqkv, ws, smem, tm, tn);
    else if (tn >= 4)
      k1_gemm<1>(x, wqkv, ws, smem, tm, tn);
    else
      k1_gemm<0>(x, wqkv, ws, smem, tm, tn);
    return;
  }
  const int l = threadIdx.x & 63, wv = threadIdx.x >> 6;
  const int t = threadIdx.x & 255, h2 = threadIdx.x >> 8;
  const int pb = xcd * 8 + (bidx - 24);
  STAMP(0, 0);
  u32x4 pf[4];
  {
    const int ch = t & 15, rg = t >> 4;
    u32x4 kr[7], vr[4], sk[4];
    const int tile = pb * 2 + h2;
    const int s0 = (tile >> 2) * 64, d0 = (tile & 3) * 128;
    {
      const int sb = s0 + rg * 4;
      const int dd = d0 + ch * 8;
#pragma unroll
      for (int i = 0; i < 7; ++i) {
        const int s = sb - 3 + i;
        u32x4 z = {0u, 0u, 0u, 0u};
        kr[i] = (s >= 0) ? __builtin_nontemporal_load((const u32x4*)(kin + (size_t)s * DM + dd)) : z;
      }
#pragma unroll
      for (int i = 0; i < 4; ++i) vr[i] = __builtin_nontemporal_load((const u32x4*)(vin + (size_t)(sb + i) * DM + dd));
#pragma unroll
      for (int i = 0; i < 4; ++i) sk[i] = *(const u32x4*)(shiftk + (size_t)dd * 4 + i * 8);
    }
    __builtin_amdgcn_sched_barrier(0);
    __builtin_amdgcn_sched_barrier(0);
    char* sm = smem + h2 * 17408;
#pragma unroll
    for (int c = 0; c < 4; ++c) {
      const f32x2 w0 = {bflo(sk[c][0]), bflo(sk[c][2])}, w1 = {bfhi(sk[c][0]), bfhi(sk[c][2])};
      const f32x2 w2 = {bflo(sk[c][1]), bflo(sk[c][3])}, w3 = {bfhi(sk[c][1]), bfhi(sk[c][3])};
      f32x2 kk[7];
#pragma unroll
      for (int i = 0; i < 7; ++i) kk[i] = (f32x2){bflo(kr[i][c]), bfhi(kr[i][c])};
      f32x2 r[4];
#pragma unroll
      for (int i = 0; i < 4; ++i) {
        f32x2 a = kk[i] * w3;
        a = __builtin_elementwise_fma(kk[i + 1], w2, a);
        a = __builtin_elementwise_fma(kk[i + 2], w1, a);
        a = __builtin_elementwise_fma(kk[i + 3], w0, a);
        const unsigned ap = pack2(a[0], a[1]);
        r[i] = (f32x2){bflo(ap), bfhi(ap)} * (f32x2){bflo(vr[i][c]), bfhi(vr[i][c])};
      }
      *(u32x2*)(sm + (ch * 8 + 2 * c) * 136 + rg * 8) = (u32x2){pack2(r[0][0], r[1][0]), pack2(r[2][0], r[3][0])};
      *(u32x2*)(sm + (ch * 8 + 2 * c + 1) * 136 + rg * 8) = (u32x2){pack2(r[0][1], r[1][1]), pack2(r[2][1], r[3][1])};
    }
    STAMP(0, 14);
    __builtin_amdgcn_sched_barrier(0);
#pragma unroll
    for (int i = 0; i < 4; ++i) pf[i] = *(const u32x4*)(ssmk + (size_t)pb * 16384 + (i * 512 + threadIdx.x) * 8);
    __builtin_amdgcn_sched_barrier(0);
    RAWBAR();
    STAMP(0, 15);
    const rsrc_t rs = ws_rsrc(ws);
#pragma unroll
    for (int i = 0; i < 4; ++i) {
      const int idx = t + 256 * i;
      const int dr = idx >> 3, c8 = idx & 7;
      const u32x2 a = *(const u32x2*)(sm + dr * 136 + c8 * 16);
      const u32x2 b = *(const u32x2*)(sm + dr * 136 + c8 * 16 + 8);
      u32x4 v = {a[0], a[1], b[0], b[1]};
      wst16<AUX_KVT>(rs, WS_KVT + (unsigned)((d0 + dr) * SEQ + s0 + c8 * 8) * 2, v);
    }
  }
  STAMP(0, 11);
#pragma unroll
  for (int i = 0; i < 4; ++i) asm volatile("" ::"v"(pf[i]));
  STAMP(0, 12);
}

#define CV_KVSTR 80
#define CV_HSTR 4160
#define CV_HBYTES (8 * CV_HSTR)
#define CV_REDSTR 36
#define CV_SMEM 40960

template <bool FIRST>
DI void attn_block(const char* __restrict__ ws, char* smem, int head, int qb) {
  const int l = threadIdx.x & 63, wv = threadIdx.x >> 6;
  const int r = l & 31, hh = l >> 5;
  const u16* Qh = (const u16*)(ws + WS_Q) + (size_t)head * SEQ * 16;
  const u16* Kh = (const u16*)(ws + WS_K) + (size_t)head * SEQ * 16;
  const u16* Vh = (const u16*)(ws + WS_VT) + (size_t)head * 16 * SEQ;
  u16* O = (u16*)(ws + WS_O);
  const int q0 = qb + 32 * wv;
  const int kt0 = FIRST ? (8 - wv) : 0;
  STAMP(1, 0);
  char* ks_ = smem;
  char* vs_ = smem + 16384;
  char* cs_ = smem + 32768;
  {
    const int key = l >> 1;
    const u16* ksrc = Kh + (size_t)key * 16 + (((l & 1) ^ ((key >> 3) & 1)) * 8);
    const int dd = l >> 2;
    const u16* vsrc = Vh + (size_t)dd * SEQ + (((l & 3) ^ ((dd >> 2) & 3)) * 8);
#pragma unroll
    for (int i = 0; i < 2; ++i) {
      const int u = wv + 8 * i;
      int key0 = qb - 256 + 32 * u;
      key0 = key0 < 0 ? 0 : key0;
      GLDS16A(ksrc + (size_t)key0 * 16, ks_ + u * 1024 + l * 16, AUX_LD2);
      GLDS16A(vsrc + key0, vs_ + u * 1024 + l * 16, AUX_LD2);
    }
  }
  const bf16x8 qf = __builtin_bit_cast(bf16x8, ld16<AUX_LD2 != 0>(Qh + (size_t)(q0 + r) * 16 + 8 * hh));
  if (threadIdx.x < 2) {
    const unsigned o = threadIdx.x == 0 ? 0x3f803f80u : 0u;
    *(u32x4*)(cs_ + threadIdx.x * 16) = (u32x4){o, o, o, o};
  }
  WAIT_V(0);
  RAWBAR();
  STAMP(1, 1);
  const char* krd = ks_ + (32 * wv + r) * 32 + ((hh ^ ((r >> 3) & 1)) * 16);
  f32x16 zero;
#pragma unroll
  for (int e = 0; e < 16; ++e) zero[e] = 0.f;
  const float NEG = -3.0e38f;
  auto mask = [&](int kt, f32x16& s) {
    if (kt == 0) {
#pragma unroll
      for (int e = 0; e < 16; ++e) s[e] = ((e & 3) + 8 * (e >> 2) + 4 * hh > r) ? s[e] : NEG;
    }
    if (kt == 8) {
#pragma unroll
      for (int e = 0; e < 16; ++e) s[e] = ((e & 3) + 8 * (e >> 2) + 4 * hh <= r) ? s[e] : NEG;
    }
  };
  float mx = NEG, mx2 = NEG;
  if (!FIRST) {
    bf16x8 kf[9];
#pragma unroll
    for (int kt = 0; kt < 9; ++kt) kf[kt] = *(const bf16x8*)(krd + kt * 1024);
    f32x16 s[3];
    s[0] = MFMA32(kf[0], qf, zero);
    s[1] = MFMA32(kf[8], qf, zero);
    s[2] = MFMA32(kf[1], qf, zero);
#pragma unroll
    for (int e = 0; e < 16; ++e) s[0][e] = ((e & 3) + 8 * (e >> 2) + 4 * hh > r) ? s[0][e] : s[1][e];
    s[1] = MFMA32(kf[2], qf, zero);
#pragma unroll
    for (int j = 0; j < 8; ++j) {
      f32x16& c = s[(3 - (j % 3)) % 3];
      float m = NEG, m2 = NEG;
#pragma unroll
      for (int e = 0; e < 8; e += 2) {
        m = fmaxf(fmaxf(c[e], c[e + 1]), m);
        m2 = fmaxf(fmaxf(c[8 + e], c[9 + e]), m2);
      }
      mx = fmaxf(mx, m);
      mx2 = fmaxf(mx2, m2);
      if (j + 3 <= 7) s[(3 - (j % 3)) % 3] = MFMA32(kf[j + 3], qf, zero);
    }
  } else
  {
    bf16x8 kf[9];
#pragma unroll
    for (int kt = 0; kt < 9; ++kt) kf[kt] = *(const bf16x8*)(krd + kt * 1024);
    f32x16 s[3];
    s[0] = MFMA32(kf[0], qf, zero);
    s[1] = MFMA32(kf[1], qf, zero);
#pragma unroll
    for (int kt = 0; kt < 9; ++kt) {
      if (kt < 7) s[(kt + 2) % 3] = MFMA32(kf[kt + 2], qf, zero);
      f32x16& c = s[kt % 3];
      mask(kt, c);
      float m = NEG, m2 = NEG;
#pragma unroll
      for (int e = 0; e < 8; e += 2) {
        m = fmaxf(fmaxf(c[e], c[e + 1]), m);
        m2 = fmaxf(fmaxf(c[8 + e], c[9 + e]), m2);
      }
      if (FIRST) {
        m = (kt >= kt0) ? m : NEG;
        m2 = (kt >= kt0) ? m2 : NEG;
      }
      mx = fmaxf(mx, m);
      mx2 = fmaxf(mx2, m2);
    }
  }
  mx = fmaxf(mx, mx2);
  mx = fmaxf(mx, __shfl_xor(mx, 32));
  f32x16 negm;
#pragma unroll
  for (int e = 0; e < 16; ++e) negm[e] = -mx;
  STAMP(1, 2);
  const int vsw = (r >> 2) & 3;
  const bool isd = r < 16;
  const int cad = 32768 + ((r == 16 || r == 20) ? 0 : 16);
  int va0 = isd ? 16384 + wv * 1024 + r * 64 + ((hh ^ vsw) * 16) : cad;
  int va1 = isd ? 16384 + wv * 1024 + r * 64 + (((2 + hh) ^ vsw) * 16) : cad;
  const int vstep = isd ? 1024 : 0;
  f32x16 oacc = zero;
  if (!FIRST) {
    f32x16 s[2];
    bf16x8 kf = *(const bf16x8*)(krd);
    bf16x8 kg = *(const bf16x8*)(krd + 8 * 1024);
    s[0] = MFMA32(kf, qf, negm);
    s[1] = MFMA32(kg, qf, negm);
    {
      const bf16x8 vf0 = *(const bf16x8*)(smem + va0);
      const bf16x8 vf1 = *(const bf16x8*)(smem + va1);
      const bf16x8 vg0 = *(const bf16x8*)(smem + va0 + 8 * vstep);
      const bf16x8 vg1 = *(const bf16x8*)(smem + va1 + 8 * vstep);
      va0 += vstep;
      va1 += vstep;
      kf = *(const bf16x8*)(krd + 1024);
#pragma unroll
      for (int e = 0; e < 16; ++e) s[0][e] = ((e & 3) + 8 * (e >> 2) + 4 * hh > r) ? s[0][e] : s[1][e];
      s[1] = MFMA32(kf, qf, negm);
      u32x4 p0[2], p8[2];
#pragma unroll
      for (int e = 0; e < 16; e += 2) {
        const unsigned pk = pack2(__builtin_amdgcn_exp2f(s[0][e]), __builtin_amdgcn_exp2f(s[0][e + 1]));
        const int td = r - ((e & 3) + 8 * (e >> 2) + 4 * hh);
        const unsigned m8 = td >= 1 ? 0xffffffffu : (td == 0 ? 0x0000ffffu : 0u);
        p8[e >> 3][(e >> 1) & 3] = pk & m8;
        p0[e >> 3][(e >> 1) & 3] = pk & ~m8;
      }
      oacc = MFMA32(vf0, __builtin_bit_cast(bf16x8, p0[0]), oacc);
      oacc = MFMA32(vf1, __builtin_bit_cast(bf16x8, p0[1]), oacc);
      oacc = MFMA32(vg0, __builtin_bit_cast(bf16x8, p8[0]), oacc);
      oacc = MFMA32(vg1, __builtin_bit_cast(bf16x8, p8[1]), oacc);
    }
#pragma unroll
    for (int kt = 1; kt < 8; ++kt) {
      const bf16x8 vf0 = *(const bf16x8*)(smem + va0);
      const bf16x8 vf1 = *(const bf16x8*)(smem + va1);
      va0 += vstep;
      va1 += vstep;
      if (kt < 7) {
        kf = *(const bf16x8*)(krd + (kt + 1) * 1024);
        s[(kt + 1) & 1] = MFMA32(kf, qf, negm);
      }
      f32x16& c = s[kt & 1];
      u32x4 pp[2];
#pragma unroll
      for (int e = 0; e < 16; e += 2) {
        pp[e >> 3][(e >> 1) & 3] = pack2(__builtin_amdgcn_exp2f(c[e]), __builtin_amdgcn_exp2f(c[e + 1]));
      }
      oacc = MFMA32(vf0, __builtin_bit_cast(bf16x8, pp[0]), oacc);
      oacc = MFMA32(vf1, __builtin_bit_cast(bf16x8, pp[1]), oacc);
    }
  } else
  {
    f32x16 s[2];
    bf16x8 kf = *(const bf16x8*)(krd);
    s[0] = MFMA32(kf, qf, negm);
#pragma unroll
    for (int kt = 0; kt < 9; ++kt) {
      const bf16x8 vf0 = *(const bf16x8*)(smem + va0);
      const bf16x8 vf1 = *(const bf16x8*)(smem + va1);
      va0 += vstep;
      va1 += vstep;
      if (kt < 8) {
        kf = *(const bf16x8*)(krd + (kt + 1) * 1024);
        s[(kt + 1) & 1] = MFMA32(kf, qf, negm);
      }
      f32x16& c = s[kt & 1];
      mask(kt, c);
      if (!FIRST || kt >= kt0) {
        u32x4 pp[2];
#pragma unroll
        for (int e = 0; e < 16; e += 2) {
          pp[e >> 3][(e >> 1) & 3] = pack2(__builtin_amdgcn_exp2f(c[e]), __builtin_amdgcn_exp2f(c[e + 1]));
        }
        oacc = MFMA32(vf0, __builtin_bit_cast(bf16x8, pp[0]), oacc);
        oacc = MFMA32(vf1, __builtin_bit_cast(bf16x8, pp[1]), oacc);
      }
    }
  }
  STAMP(1, 3);
  const float inv = 1.f / oacc[8];
  u32x2 v0 = {pack2(oacc[0] * inv, oacc[1] * inv), pack2(oacc[2] * inv, oacc[3] * inv)};
  u32x2 v1 = {pack2(oacc[4] * inv, oacc[5] * inv), pack2(oacc[6] * inv, oacc[7] * inv)};
  const rsrc_t rs = ws_rsrc(ws);
  const unsigned op = WS_O + (unsigned)((q0 + r) * DM + head * 16 + 4 * hh) * 2;
  wst8<AUX_ST2>(rs, op, v0);
  wst8<AUX_ST2>(rs, op + 16, v1);
  STAMP(1, 4);
}

DI void conv_block(const u16* __restrict__ ssmk, const u16* __restrict__ ssmD, char* __restrict__ ws, char* smem,
                   int d) {
  const int t = threadIdx.x & 255;
  const int l = t & 63, wv = t >> 6;
  const int r = l & 31, hh = l >> 5;
  char* hs = smem;
  char* kvs = smem + CV_HBYTES;
  const u16* kvT = (const u16*)(ws + WS_KVT) + (size_t)d * SEQ;
  const u16* hrow = ssmk + (size_t)d * SEQ;
  STAMP(1, 0);
  const u32x4 kvv = ld16<AUX_LD2 != 0>(kvT + 8 * t);
  const u32x4 whi = *(const u32x4*)(hrow + 8 * t);
  const u32x4 wlo0 = *(const u32x4*)(hrow + 8 * (t > 0 ? t - 1 : 0));
  const float Dd = bf2f(ssmD[d]);
  *(u32x4*)(kvs + ((t >> 2) + 32) * CV_KVSTR + (t & 3) * 16) = kvv;
  if (t < 128) {
    u32x4 z = {0u, 0u, 0u, 0u};
    *(u32x4*)(kvs + (t >> 2) * CV_KVSTR + (t & 3) * 16) = z;
  }
  {
    const int E = t;
    u32x4 wlo = wlo0;
    if (E == 0) wlo = (u32x4){0u, 0u, 0u, 0u};
    unsigned z[8];
#pragma unroll
    for (int i = 0; i < 4; ++i) {
      const unsigned a = whi[3 - i];
      z[i] = (a >> 16) | (a << 16);
      const unsigned b = wlo[3 - i];
      z[4 + i] = (b >> 16) | (b << 16);
    }
    const int M = 255 - E;
#pragma unroll
    for (int p = 0; p < 8; ++p) {
      const int o = 7 - p;
      u32x4 v;
      if ((o & 1) == 0) {
        v[0] = z[o / 2];
        v[1] = z[o / 2 + 1];
        v[2] = z[o / 2 + 2];
        v[3] = z[o / 2 + 3];
      } else {
        v[0] = (z[o / 2] >> 16) | (z[o / 2 + 1] << 16);
        v[1] = (z[o / 2 + 1] >> 16) | (z[o / 2 + 2] << 16);
        v[2] = (z[o / 2 + 2] >> 16) | (z[o / 2 + 3] << 16);
        v[3] = (z[o / 2 + 3] >> 16) | (z[o / 2 + 4] << 16);
      }
      *(u32x4*)(hs + p * CV_HSTR + 16 * M) = v;
    }
    if (t < 24) {
      u32x4 zz = {0u, 0u, 0u, 0u};
      *(u32x4*)(hs + (t & 7) * CV_HSTR + 16 * (256 + (t >> 3))) = zz;
    }
  }
  __syncthreads();
  STAMP(1, 1);
  f32x16 acc0, acc1, acc2;
#pragma unroll
  for (int e = 0; e < 16; ++e) {
    acc0[e] = 0.f;
    acc1[e] = 0.f;
    acc2[e] = 0.f;
  }
  const char* abase = hs + (r & 7) * CV_HSTR + 16 * (255 - (r >> 3) + hh) - 64 * wv;
  const char* bbase = kvs + (r + 32 - wv) * CV_KVSTR + 16 * hh;
  {
    bf16x8 fa[2][2], fb[2][2];
    auto ldc = [&](int step, bf16x8(&a)[2], bf16x8(&b)[2]) {
      const int i = step >> 1, ks = step & 1;
      a[0] = *(const bf16x8*)(abase - 256 * i + 32 * ks);
      a[1] = *(const bf16x8*)(abase - 256 * i - 2048 + 32 * ks);
      b[1] = *(const bf16x8*)(bbase + (32 - 4 * i) * CV_KVSTR + 32 * ks);
      b[0] = *(const bf16x8*)(bbase - (4 * i) * CV_KVSTR + 32 * ks);
    };
    ldc(0, fa[0], fb[0]);
#pragma unroll
    for (int step = 0; step < 16; ++step) {
      if (step < 15) ldc(step + 1, fa[(step + 1) & 1], fb[(step + 1) & 1]);
      __builtin_amdgcn_sched_barrier(0);
      acc1 = MFMA32(fa[step & 1][0], fb[step & 1][1], acc1);
      acc0 = MFMA32(fa[step & 1][0], fb[step & 1][0], acc0);
      acc2 = MFMA32(fa[step & 1][1], fb[step & 1][0], acc2);
      __builtin_amdgcn_sched_barrier(0);
    }
  }
  acc1 += acc2;
  STAMP(1, 2);
  __syncthreads();
  STAMP(1, 6);
  float* red = (float*)smem;
  {
    float* rw = red + wv * (64 * CV_REDSTR);
#pragma unroll
    for (int g = 0; g < 4; ++g) {
      f32x4 v0 = {acc0[4 * g], acc0[4 * g + 1], acc0[4 * g + 2], acc0[4 * g + 3]};
      f32x4 v1 = {acc1[4 * g], acc1[4 * g + 1], acc1[4 * g + 2], acc1[4 * g + 3]};
      *(f32x4*)(rw + r * CV_REDSTR + 8 * g + 4 * hh) = v0;
      *(f32x4*)(rw + (32 + r) * CV_REDSTR + 8 * g + 4 * hh) = v1;
    }
  }
  __syncthreads();
  STAMP(1, 7);
  {
    const int a = t >> 2, b0 = 8 * (t & 3);
    f32x4 s0 = {0.f, 0.f, 0.f, 0.f}, s1 = {0.f, 0.f, 0.f, 0.f};
#pragma unroll
    for (int w = 0; w < 4; ++w) {
      const float* rp = red + w * (64 * CV_REDSTR) + a * CV_REDSTR + b0;
      s0 += *(const f32x4*)(rp);
      s1 += *(const f32x4*)(rp + 4);
    }
    float yy[8] = {s0[0], s0[1], s0[2], s0[3], s1[0], s1[1], s1[2], s1[3]};
    u32x4 o;
#pragma unroll
    for (int i = 0; i < 4; ++i) {
      const float y0 = rbf(yy[2 * i]) + rbf(bflo(kvv[i]) * Dd);
      const float y1 = rbf(yy[2 * i + 1]) + rbf(bfhi(kvv[i]) * Dd);
      o[i] = pack2(y0, y1);
    }
    wst16<AUX_Y2T>(ws_rsrc(ws), WS_Y2T + (unsigned)(d * SEQ + 8 * t) * 2, o);
  }
  STAMP(1, 4);
}

__global__ __launch_bounds__(512, 4) void k2_kernel(const u16* __restrict__ ssmk, const u16* __restrict__ ssmD,
                                                    const u16* __restrict__ wo, const u16* __restrict__ x,
                                                    const u16* __restrict__ gatew, char* __restrict__ ws) {
  __shared__ __attribute__((aligned(16))) char smem[2 * CV_SMEM];
  const int xcd = blockIdx.x & 7, bidx = (blockIdx.x >> 3) ^ 32;
  if (bidx >= 32) {
#define PF_WO 2
    const int half = threadIdx.x >> 8;
    conv_block(ssmk, ssmD, ws, smem + half * CV_SMEM, xcd * 64 + (bidx - 32) * 2 + half);
    const u32x4 pf0 = *(const u32x4*)(wo + (size_t)(bidx - 32) * 8192 + threadIdx.x * 8);
    const u32x4 pf1 = *(const u32x4*)(wo + (size_t)(bidx - 32) * 8192 + 4096 + threadIdx.x * 8);
    {
      const int l = threadIdx.x & 63;
      const int row = (xcd * 32 + (bidx - 32)) * 8 + (threadIdx.x >> 6);
      const u32x4 gw = *(const u32x4*)(gatew + l * 8);
      const u32x4 xv = ld16<false>(x + (size_t)row * DM + l * 8);
      float sum = 0.f;
#pragma unroll
      for (int i = 0; i < 4; ++i) {
        const unsigned pr = pack2(bflo(xv[i]) * bflo(gw[i]), bfhi(xv[i]) * bfhi(gw[i]));
        sum += bflo(pr);
        sum += bfhi(pr);
      }
#pragma unroll
      for (int o = 32; o >= 1; o >>= 1) sum += __shfl_xor(sum, o);
      if (l == 0) ((float*)(ws + WS_G))[row] = rbf(1.f / (1.f + __expf(-rbf(sum))));
    }
    asm volatile("" ::"v"(pf0));
    asm volatile("" ::"v"(pf1));
  } else {
    const int head = 4 * xcd + (bidx & 3), win = bidx >> 2;
    if (win == 0)
      attn_block<true>(ws, smem, head, 0);
    else
      attn_block<false>(ws, smem, head, win * 256);
  }
}

#define K3_STAGE 32768
#define K3_CSTR 68
#define K3_YSTR 136

DI void k3_body(const u16* __restrict__ x, const u16* __restrict__ wo, const char* __restrict__ ws,
                u16* __restrict__ out, char* smem) {
  const int t = threadIdx.x;
  const int l = t & 63, wv = t >> 6;
  const int r = l & 31, hh = l >> 5;
  const int wm = wv & 1, wn = (wv >> 1) & 1, kh = wv >> 2;
  const int tm = (blockIdx.x & 7) * 4 + ((blockIdx.x >> 3) & 3), tn = blockIdx.x >> 5;
  const int m0 = tm * 64, n0 = tn * 64;
  const u16* o = (const u16*)(ws + WS_O);
  STAMP(2, 0);

  const u16* y2T = (const u16*)(ws + WS_Y2T);
  const float* gp = (const float*)(ws + WS_G);
  const int rr = t >> 3, ch = t & 7;
  const u32x4 yv = ld16<AUX_LD3 != 0>(y2T + (size_t)(n0 + rr) * SEQ + m0 + ch * 8);
  const u32x4 xq = ld16<false>(x + (size_t)(m0 + rr) * DM + n0 + ch * 8);
  const float g = gp[m0 + rr];
  __builtin_amdgcn_sched_barrier(0);
  {
    char* ldsw = smem + wv * 2048 + l * 16;
#pragma unroll
    for (int s4 = 0; s4 < 4; ++s4) {
      const int st = s4;
#pragma unroll
      for (int i = 0; i < 2; ++i) {
        const int ar = 8 * wv + 4 * i + (l >> 4);
        GLDS16A(o + (size_t)(m0 + ar) * DM + st * 128 + (((l & 15) ^ (ar & 15)) * 8), ldsw + st * K3_STAGE + i * 1024,
                AUX_LD3);
      }
#pragma unroll
      for (int i = 0; i < 2; ++i) {
        const int br = 16 * wv + 8 * i + (l >> 3);
        GLDS16(wo + (size_t)(st * 128 + br) * DM + n0 + (((l & 7) ^ (((br >> 1) & 1) << 2)) * 8),
               ldsw + st * K3_STAGE + 16384 + i * 1024);
      }
    }
  }

  f32x16 acc;
#pragma unroll
  for (int e = 0; e < 16; ++e) acc[e] = 0.f;
  const int blk = (l >> 4) & 1, q4 = (l & 15) >> 2, p4 = l & 3;
  const int arow = wm * 32 + r;
  const int a_off = arow * 256;
  const int a_sw = arow & 15;
  const int b_off = 16384 + (8 * hh + q4) * 128 + (((wn * 4 + 2 * blk + (p4 >> 1)) ^ (((q4 >> 1) & 1) << 2)) * 16) +
                    (p4 & 1) * 8;
  const int a_x = (8 * kh + hh) ^ a_sw;
  const int b_k = b_off + 64 * kh * 128;
#pragma unroll
  for (int st = 0; st < 4; ++st) {
    if (st == 0) WAIT_V(12);
    else if (st == 1) WAIT_V(8);
    else if (st == 2) WAIT_V(4);
    else WAIT_V(0);
    RAWBAR();
    const char* sb = smem + st * K3_STAGE;
    bf16x8 af[4], bfr[4];
#pragma unroll
    for (int ks = 0; ks < 4; ++ks) {
      af[ks] = *(const bf16x8*)(sb + a_off + ((a_x ^ (2 * ks)) * 16));
      s16x4 lo = tr_read(sb + b_k + (16 * ks) * 128);
      s16x4 hi = tr_read(sb + b_k + (16 * ks + 4) * 128);
      bfr[ks] = cat_frag(lo, hi);
    }
    __builtin_amdgcn_sched_barrier(0);
#pragma unroll
    for (int ks = 0; ks < 4; ++ks) acc = MFMA32(af[ks], bfr[ks], acc);
    __builtin_amdgcn_sched_barrier(0);
  }
  __syncthreads();
  STAMP(2, 5);
  float* Cs = (float*)smem;
  char* Ys = smem + 2 * 64 * K3_CSTR * 4;
#pragma unroll
  for (int e = 0; e < 16; ++e) {
    const int row = wm * 32 + (e & 3) + 8 * (e >> 2) + 4 * hh;
    Cs[kh * 64 * K3_CSTR + row * K3_CSTR + wn * 32 + r] = acc[e];
  }
  {
    u32x2 a = {yv[0], yv[1]}, b = {yv[2], yv[3]};
    *(u32x2*)(Ys + rr * K3_YSTR + ch * 16) = a;
    *(u32x2*)(Ys + rr * K3_YSTR + ch * 16 + 8) = b;
  }
  __syncthreads();
  {
    const int row = rr;
    const int s = m0 + row;
    const float omg = rbf(1.f - g);
    const f32x4 c0 = *(const f32x4*)(Cs + row * K3_CSTR + ch * 8) + *(const f32x4*)(Cs + (64 + row) * K3_CSTR + ch * 8);
    const f32x4 c1 =
        *(const f32x4*)(Cs + row * K3_CSTR + ch * 8 + 4) + *(const f32x4*)(Cs + (64 + row) * K3_CSTR + ch * 8 + 4);
    float bb[8] = {c0[0], c0[1], c0[2], c0[3], c1[0], c1[1], c1[2], c1[3]};
    float res[8];
#pragma unroll
    for (int j = 0; j < 8; ++j) {
      const float y2 = bf2f(*(const u16*)(Ys + (ch * 8 + j) * K3_YSTR + row * 2));
      const float qv = (j & 1) ? bfhi(xq[j >> 1]) : bflo(xq[j >> 1]);
      const float a = rbf(qv * y2);
      const float ga = rbf(g * a);
      const float gb = rbf(omg * rbf(bb[j]));
      res[j] = ga + gb;
    }
    u32x4 ov;
    ov[0] = pack2(res[0], res[1]);
    ov[1] = pack2(res[2], res[3]);
    ov[2] = pack2(res[4], res[5]);
    ov[3] = pack2(res[6], res[7]);
    *(u32x4*)(out + (size_t)s * DM + n0 + ch * 8) = ov;
  }
  STAMP(2, 6);
}

__global__ __launch_bounds__(512) void k3_kernel(const u16* __restrict__ x, const u16* __restrict__ wo,
                                                 const char* __restrict__ ws, u16* __restrict__ out) {
  __shared__ __attribute__((aligned(16))) char smem[4 * K3_STAGE];
  k3_body(x, wo, ws, out, smem);
}

extern "C" void kernel_launch(void* const* d_in, const int* in_sizes, int n_in, void* d_out, int out_size,
                              void* d_ws, size_t ws_size, hipStream_t stream) {
  const u16* A_q = (const u16*)d_in[0];
  const u16* A_k = (const u16*)d_in[1];
  const u16* A_v = (const u16*)d_in[2];
  const u16* A_shift = (const u16*)d_in[3];
  const u16* A_ssmk = (const u16*)d_in[4];
  const u16* A_ssmD = (const u16*)d_in[5];
  const u16* B_wqkv = (const u16*)d_in[7];
  const u16* B_wo = (const u16*)d_in[8];
  const u16* gate_w = (const u16*)d_in[9];
  char* ws = (char*)d_ws;
  k1_kernel<<<256, 512, 0, stream>>>(A_q, A_k, A_v, A_shift, B_wqkv, A_ssmk, ws);
  k2_kernel<<<512, 512, 0, stream>>>(A_ssmk, A_ssmD, B_wo, A_q, gate_w, ws);
  k3_kernel<<<256, 512, 0, stream>>>(A_q, B_wo, ws, (u16*)d_out);
}
```

```cpp
#include <hip/hip_runtime.h>
#include <hip/hip_bf16.h>
#include <stdint.h>

#define DI __device__ __forceinline__
typedef __attribute__((ext_vector_type(8))) __bf16 bf16x8;
typedef __attribute__((ext_vector_type(4))) __bf16 bf16x4;
typedef __attribute__((ext_vector_type(2))) __bf16 bf16x2;
typedef __attribute__((ext_vector_type(4))) short s16x4;
typedef __attribute__((ext_vector_type(16))) float f32x16;
typedef __attribute__((ext_vector_type(4))) float f32x4;
typedef __attribute__((ext_vector_type(2))) float f32x2;
typedef __attribute__((ext_vector_type(4))) unsigned u32x4;
typedef __attribute__((ext_vector_type(2))) unsigned u32x2;
typedef unsigned short u16;

#define SEQ 2048
#define DM 512

#define MFMA32(a, b, c) __builtin_amdgcn_mfma_f32_32x32x16_bf16((a), (b), (c), 0, 0, 0)

DI float bf2f(u16 v) { return __uint_as_float(((unsigned)v) << 16); }
DI float bflo(unsigned v) { return __uint_as_float(v << 16); }
DI float bfhi(unsigned v) { return __uint_as_float(v & 0xffff0000u); }
DI unsigned pack2(float a, float b) {
  f32x2 f = {a, b};
  bf16x2 r = __builtin_convertvector(f, bf16x2);
  return __builtin_bit_cast(unsigned, r);
}
DI float rbf(float a) {
  return (float)((__bf16)a);
}
DI s16x4 tr_read(const char* p) {
  typedef __attribute__((address_space(3))) s16x4* lp;
  return __builtin_amdgcn_ds_read_tr16_b64_v4i16((lp)(p));
}
DI bf16x8 cat_frag(s16x4 lo, s16x4 hi) {
  typedef __attribute__((ext_vector_type(8))) short s16x8;
  s16x8 v = __builtin_shufflevector(lo, hi, 0, 1, 2, 3, 4, 5, 6, 7);
  return __builtin_bit_cast(bf16x8, v);
}

#define STAMP(k, i) do {} while (0)
#define WS_Q 0
#define WS_K (2u << 20)
#define WS_VT (4u << 20)
#define WS_KVT (6u << 20)
#define WS_Y2T (8u << 20)
#define WS_O (10u << 20)
#define WS_G (12u << 20)

#define WT_AUX 0
typedef __amdgpu_buffer_rsrc_t rsrc_t;
DI rsrc_t ws_rsrc(const char* ws) { return __builtin_amdgcn_make_buffer_rsrc((void*)ws, (short)0, 16 << 20, 0x00020000); }
#define AUX_ST1 0
#define AUX_ST2 0
#define AUX_KVT 16
#define AUX_Y2T 16
#define NT_KV
#define PF_LATE 1
#define ATT_MAP
#define ATT_MERGE
#define AUX_LD2 0
#define AUX_LD3 0
template <int AUX>
DI void wst16(rsrc_t rs, unsigned off, u32x4 v) { __builtin_amdgcn_raw_buffer_store_b128(v, rs, (int)off, 0, AUX); }
template <int AUX>
DI void wst8(rsrc_t rs, unsigned off, u32x2 v) { __builtin_amdgcn_raw_buffer_store_b64(v, rs, (int)off, 0, AUX); }
template <bool NT>
DI u32x4 ld16(const void* p) {
  if constexpr (NT) return __builtin_nontemporal_load((const u32x4*)p);
  else return *(const u32x4*)p;
}

#define GLDS16(g, l) __builtin_amdgcn_global_load_lds((const unsigned*)(g), (unsigned*)(l), 16, 0, 0)
#define GLDS16A(g, l, aux) __builtin_amdgcn_global_load_lds((const unsigned*)(g), (unsigned*)(l), 16, 0, aux)
#define AUX_X 2
#define AUX_W 0
#define WAIT_V(n) asm volatile("s_waitcnt vmcnt(" #n ")" ::: "memory")
#define RAWBAR() do { asm volatile("s_waitcnt lgkmcnt(0)" ::: "memory"); __builtin_amdgcn_s_barrier(); } while (0)
#define K1_STAGE 32768

template <int N>
DI void wait_vm() {
  if constexpr (N == 0) WAIT_V(0);
  else if constexpr (N == 4) WAIT_V(4);
  else if constexpr (N == 8) WAIT_V(8);
  else if constexpr (N == 10) WAIT_V(10);
  else if constexpr (N == 12) WAIT_V(12);
  else if constexpr (N == 14) WAIT_V(14);
  else if constexpr (N == 16) WAIT_V(16);
  else if constexpr (N == 24) WAIT_V(24);
}

#define QSCALE (0.25f * 1.44269504088896340736f)

template <int MODE>
DI void k1_gemm(const u16* __restrict__ x, const u16* __restrict__ w, char* __restrict__ ws, char* smem, int tm,
                int tn) {
  const int t = threadIdx.x;
  const int l = t & 63, wv = t >> 6;
  const int r = l & 31, hh = l >> 5;
  const int wm = wv & 1, wn = wv >> 1;
  const int m0 = tm * 128, n0 = tn * 128;

  const u16* xg[2];
  const u16* wg[2];
#pragma unroll
  for (int i = 0; i < 2; ++i) {
    const int ar = 16 * wv + 8 * i + (l >> 3);
    xg[i] = x + (size_t)(m0 + ar) * DM + (((l & 7) ^ ((ar >> 1) & 7)) * 8);
    const int br = 8 * wv + 4 * i + (l >> 4);
    wg[i] = w + (size_t)br * 1536 + n0 + (((l & 15) ^ ((br & 3) << 2)) * 8);
  }
  char* ldsw = smem + wv * 2048 + l * 16;

  const int rot = 0;
  auto issue = [&](int kt) {
    char* st = ldsw + (kt & 3) * K1_STAGE;
    const int kk = (kt + rot) & 7;
#pragma unroll
    for (int i = 0; i < 2; ++i) GLDS16A(xg[i] + kk * 64, st + i * 1024, AUX_X);
#pragma unroll
    for (int i = 0; i < 2; ++i) GLDS16A(wg[i] + (size_t)kk * 64 * 1536, st + 16384 + i * 1024, AUX_W);
  };

  f32x16 acc[2];
#pragma unroll
  for (int i = 0; i < 2; ++i)
#pragma unroll
    for (int e = 0; e < 16; ++e) acc[i][e] = 0.f;

  const int blk = (l >> 4) & 1, q4 = (l & 15) >> 2, p4 = l & 3;
  const int a_row_off = (wm * 64 + r) * 128;
  const int a_sw = (r >> 1) & 7;
  const int b_base = 16384 + (8 * hh + q4) * 256 + (p4 & 1) * 8 + (((wn * 4 + 2 * blk + (p4 >> 1)) ^ (q4 << 2)) * 16);

  STAMP(0, 0);
  issue(0);
  issue(1);
  issue(2);
  issue(3);
#define PFN 0
  bf16x8 fa[2][4][2], fb[2][4];
  auto ldfr = [&](int kt, bf16x8(&a)[4][2], bf16x8(&b)[4]) {
    const char* st = smem + (kt & 3) * K1_STAGE;
#pragma unroll
    for (int ks = 0; ks < 4; ++ks) {
#pragma unroll
      for (int i = 0; i < 2; ++i)
        a[ks][i] = *(const bf16x8*)(st + a_row_off + i * 32 * 128 + (((2 * ks + hh) ^ a_sw) * 16));
      s16x4 lo = tr_read(st + b_base + (16 * ks) * 256);
      s16x4 hi = tr_read(st + b_base + (16 * ks + 4) * 256);
      b[ks] = cat_frag(lo, hi);
    }
  };
  wait_vm<12 + PFN>();
  RAWBAR();
  ldfr(0, fa[0], fb[0]);
#define K1_ILV 2
#pragma unroll
  for (int kt = 0; kt < 8; ++kt) {
    const char* stn = smem + ((kt + 1) & 3) * K1_STAGE;
    bf16x8(&an)[4][2] = fa[(kt + 1) & 1];
    bf16x8(&bn)[4] = fb[(kt + 1) & 1];
    s16x4 lo, hi;
    auto rdpair = [&](int j) {
      const int ks = j >> 1, i = j & 1;
      an[ks][i] = *(const bf16x8*)(stn + a_row_off + i * 32 * 128 + (((2 * ks + hh) ^ a_sw) * 16));
      if (i == 0) {
        lo = tr_read(stn + b_base + (16 * ks) * 256);
      } else {
        hi = tr_read(stn + b_base + (16 * ks + 4) * 256);
        bn[ks] = cat_frag(lo, hi);
      }
    };
    if (kt < 7) {
      if (kt <= 2) wait_vm<8 + PFN>();
      else if (kt <= 4) wait_vm<8>();
      else if (kt == 5) wait_vm<4>();
      else wait_vm<0>();
      RAWBAR();
    }
    __builtin_amdgcn_sched_barrier(0);
#pragma unroll
    for (int g = 0; g < 8; ++g) {
      const int ks = g >> 1, i = g & 1;
      if (MODE == 2)
        acc[i] = MFMA32(fa[kt & 1][ks][i], fb[kt & 1][ks], acc[i]);
      else
        acc[i] = MFMA32(fb[kt & 1][ks], fa[kt & 1][ks][i], acc[i]);
      __builtin_amdgcn_sched_barrier(0);
      if (kt < 7) {
        if (g < 2) {
          rdpair(2 * g);
          rdpair(2 * g + 1);
        } else if (g < 6) {
          rdpair(g + 2);
        }
      }
      if (i == 1 && kt + 4 <= 7) {
        char* st4 = ldsw + (kt & 3) * K1_STAGE;
        const int kk = (kt + 4 + rot) & 7;
        if (ks < 2)
          GLDS16A(xg[ks] + kk * 64, st4 + ks * 1024, AUX_X);
        else
          GLDS16A(wg[ks - 2] + (size_t)kk * 64 * 1536, st4 + 16384 + (ks - 2) * 1024, AUX_W);
      }
      __builtin_amdgcn_sched_barrier(0);
    }
  }
  STAMP(0, 9);
  const rsrc_t rs = ws_rsrc(ws);
  if (MODE == 2) {
#pragma unroll
    for (int i = 0; i < 2; ++i) {
      const int n = (n0 - 1024) + wn * 32 + r;
      const int tok0 = m0 + wm * 64 + i * 32;
      const unsigned dst = WS_VT + (unsigned)(n * SEQ + tok0 + 8 * hh) * 2;
#pragma unroll
      for (int bb = 0; bb < 2; ++bb) {
        u32x4 v;
        v[0] = pack2(acc[i][8 * bb + 0], acc[i][8 * bb + 1]);
        v[1] = pack2(acc[i][8 * bb + 2], acc[i][8 * bb + 3]);
        v[2] = pack2(acc[i][8 * bb + 4], acc[i][8 * bb + 5]);
        v[3] = pack2(acc[i][8 * bb + 6], acc[i][8 * bb + 7]);
        wst16<AUX_ST1>(rs, dst + 32 * bb, v);
      }
    }
  } else {
    const unsigned base = (MODE == 1 ? WS_K : WS_Q);
    const int nn0 = n0 & 511;
#pragma unroll
    for (int i = 0; i < 2; ++i) {
      const int tok = m0 + wm * 64 + i * 32 + r;
      const int nb = nn0 + wn * 32;
      const int head = nb >> 4;
#pragma unroll
      for (int g = 0; g < 4; ++g) {
        u32x2 v;
        const float sc = (MODE == 0) ? QSCALE : 1.f;
        v[0] = pack2(acc[i][4 * g + 0] * sc, acc[i][4 * g + 1] * sc);
        v[1] = pack2(acc[i][4 * g + 2] * sc, acc[i][4 * g + 3] * sc);
        wst8<AUX_ST1>(rs, base + (unsigned)(((head + (g >> 1)) * SEQ + tok) * 16 + 8 * (g & 1) + 4 * hh) * 2, v);
      }
    }
  }
  STAMP(0, 10);
}

__global__ __launch_bounds__(512) void k1_kernel(const u16* __restrict__ x, const u16* __restrict__ kin,
                                                 const u16* __restrict__ vin, const u16* __restrict__ shiftk,
                                                 const u16* __restrict__ wqkv, const u16* __restrict__ ssmk,
                                                 char* __restrict__ ws) {
  __shared__ __attribute__((aligned(16))) char smem[4 * K1_STAGE];
  const int xcd = blockIdx.x & 7, bidx = blockIdx.x >> 3;
  if (bidx < 24) {
    const int tm = 2 * xcd + (bidx & 1), tn = bidx >> 1;
    if (tn >= 8)
      k1_gemm<2>(x, wqkv, ws, smem, tm, tn);
    else if (tn >= 4)
      k1_gemm<1>(x, wqkv, ws, smem, tm, tn);
    else
      k1_gemm<0>(x, wqkv, ws, smem, tm, tn);
    return;
  }
  const int l = threadIdx.x & 63, wv = threadIdx.x >> 6;
  const int t = threadIdx.x & 255, h2 = threadIdx.x >> 8;
  const int pb = xcd * 8 + (bidx - 24);
  STAMP(0, 0);
  u32x4 pf[4];
  {
    const int ch = t & 15, rg = t >> 4;
    u32x4 kr[7], vr[4], sk[4];
    const int tile = pb * 2 + h2;
    const int s0 = (tile >> 2) * 64, d0 = (tile & 3) * 128;
    {
      const int sb = s0 + rg * 4;
      const int dd = d0 + ch * 8;
#pragma unroll
      for (int i = 0; i < 7; ++i) {
        const int s = sb - 3 + i;
        u32x4 z = {0u, 0u, 0u, 0u};
        kr[i] = (s >= 0) ? __builtin_nontemporal_load((const u32x4*)(kin + (size_t)s * DM + dd)) : z;
      }
#pragma unroll
      for (int i = 0; i < 4; ++i) vr[i] = __builtin_nontemporal_load((const u32x4*)(vin + (size_t)(sb + i) * DM + dd));
#pragma unroll
      for (int i = 0; i < 4; ++i) sk[i] = *(const u32x4*)(shiftk + (size_t)dd * 4 + i * 8);
    }
    __builtin_amdgcn_sched_barrier(0);
    __builtin_amdgcn_sched_barrier(0);
    char* sm = smem + h2 * 17408;
#pragma unroll
    for (int c = 0; c < 4; ++c) {
      const f32x2 w0 = {bflo(sk[c][0]), bflo(sk[c][2])}, w1 = {bfhi(sk[c][0]), bfhi(sk[c][2])};
      const f32x2 w2 = {bflo(sk[c][1]), bflo(sk[c][3])}, w3 = {bfhi(sk[c][1]), bfhi(sk[c][3])};
      f32x2 kk[7];
#pragma unroll
      for (int i = 0; i < 7; ++i) kk[i] = (f32x2){bflo(kr[i][c]), bfhi(kr[i][c])};
      f32x2 r[4];
#pragma unroll
      for (int i = 0; i < 4; ++i) {
        f32x2 a = kk[i] * w3;
        a = __builtin_elementwise_fma(kk[i + 1], w2, a);
        a = __builtin_elementwise_fma(kk[i + 2], w1, a);
        a = __builtin_elementwise_fma(kk[i + 3], w0, a);
        const unsigned ap = pack2(a[0], a[1]);
        r[i] = (f32x2){bflo(ap), bfhi(ap)} * (f32x2){bflo(vr[i][c]), bfhi(vr[i][c])};
      }
      *(u32x2*)(sm + (ch * 8 + 2 * c) * 136 + rg * 8) = (u32x2){pack2(r[0][0], r[1][0]), pack2(r[2][0], r[3][0])};
      *(u32x2*)(sm + (ch * 8 + 2 * c + 1) * 136 + rg * 8) = (u32x2){pack2(r[0][1], r[1][1]), pack2(r[2][1], r[3][1])};
    }
    STAMP(0, 14);
    __builtin_amdgcn_sched_barrier(0);
#pragma unroll
    for (int i = 0; i < 4; ++i) pf[i] = *(const u32x4*)(ssmk + (size_t)pb * 16384 + (i * 512 + threadIdx.x) * 8);
    __builtin_amdgcn_sched_barrier(0);
    RAWBAR();
    STAMP(0, 15);
    const rsrc_t rs = ws_rsrc(ws);
#pragma unroll
    for (int i = 0; i < 4; ++i) {
      const int idx = t + 256 * i;
      const int dr = idx >> 3, c8 = idx & 7;
      const u32x2 a = *(const u32x2*)(sm + dr * 136 + c8 * 16);
      const u32x2 b = *(const u32x2*)(sm + dr * 136 + c8 * 16 + 8);
      u32x4 v = {a[0], a[1], b[0], b[1]};
      wst16<AUX_KVT>(rs, WS_KVT + (unsigned)((d0 + dr) * SEQ + s0 + c8 * 8) * 2, v);
    }
  }
  STAMP(0, 11);
#pragma unroll
  for (int i = 0; i < 4; ++i) asm volatile("" ::"v"(pf[i]));
  STAMP(0, 12);
}

#define CV_KVSTR 80
#define CV_HSTR 4160
#define CV_HBYTES (8 * CV_HSTR)
#define CV_REDSTR 36
#define CV_SMEM 40960

template <bool FIRST>
DI void attn_block(const char* __restrict__ ws, char* smem, int head, int qb) {
  const int l = threadIdx.x & 63, wv = threadIdx.x >> 6;
  const int r = l & 31, hh = l >> 5;
  const u16* Qh = (const u16*)(ws + WS_Q) + (size_t)head * SEQ * 16;
  const u16* Kh = (const u16*)(ws + WS_K) + (size_t)head * SEQ * 16;
  const u16* Vh = (const u16*)(ws + WS_VT) + (size_t)head * 16 * SEQ;
  u16* O = (u16*)(ws + WS_O);
  const int q0 = qb + 32 * wv;
  const int kt0 = FIRST ? (8 - wv) : 0;
  STAMP(1, 0);
  char* ks_ = smem;
  char* vs_ = smem + 16384;
  char* cs_ = smem + 32768;
  {
    const int key = l >> 1;
    const u16* ksrc = Kh + (size_t)key * 16 + (((l & 1) ^ ((key >> 3) & 1)) * 8);
    const int dd = l >> 2;
    const u16* vsrc = Vh + (size_t)dd * SEQ + (((l & 3) ^ ((dd >> 2) & 3)) * 8);
#pragma unroll
    for (int i = 0; i < 2; ++i) {
      const int u = wv + 8 * i;
      int key0 = qb - 256 + 32 * u;
      key0 = key0 < 0 ? 0 : key0;
      GLDS16A(ksrc + (size_t)key0 * 16, ks_ + u * 1024 + l * 16, AUX_LD2);
      GLDS16A(vsrc + key0, vs_ + u * 1024 + l * 16, AUX_LD2);
    }
  }
  const bf16x8 qf = __builtin_bit_cast(bf16x8, ld16<AUX_LD2 != 0>(Qh + (size_t)(q0 + r) * 16 + 8 * hh));
  if (threadIdx.x < 2) {
    const unsigned o = threadIdx.x == 0 ? 0x3f803f80u : 0u;
    *(u32x4*)(cs_ + threadIdx.x * 16) = (u32x4){o, o, o, o};
  }
  WAIT_V(0);
  RAWBAR();
  STAMP(1, 1);
  const char* krd = ks_ + (32 * wv + r) * 32 + ((hh ^ ((r >> 3) & 1)) * 16);
  f32x16 zero;
#pragma unroll
  for (int e = 0; e < 16; ++e) zero[e] = 0.f;
  const float NEG = -3.0e38f;
  auto mask = [&](int kt, f32x16& s) {
    if (kt == 0) {
#pragma unroll
      for (int e = 0; e < 16; ++e) s[e] = ((e & 3) + 8 * (e >> 2) + 4 * hh > r) ? s[e] : NEG;
    }
    if (kt == 8) {
#pragma unroll
      for (int e = 0; e < 16; ++e) s[e] = ((e & 3) + 8 * (e >> 2) + 4 * hh <= r) ? s[e] : NEG;
    }
  };
  float mx = NEG, mx2 = NEG;
  if (!FIRST) {
    bf16x8 kf[9];
#pragma unroll
    for (int kt = 0; kt < 9; ++kt) kf[kt] = *(const bf16x8*)(krd + kt * 1024);
    f32x16 s[3];
    s[0] = MFMA32(kf[0], qf, zero);
    s[1] = MFMA32(kf[8], qf, zero);
    s[2] = MFMA32(kf[1], qf, zero);
#pragma unroll
    for (int e = 0; e < 16; ++e) s[0][e] = ((e & 3) + 8 * (e >> 2) + 4 * hh > r) ? s[0][e] : s[1][e];
    s[1] = MFMA32(kf[2], qf, zero);
#pragma unroll
    for (int j = 0; j < 8; ++j) {
      f32x16& c = s[(3 - (j % 3)) % 3];
      float m = NEG, m2 = NEG;
#pragma unroll
      for (int e = 0; e < 8; e += 2) {
        m = fmaxf(fmaxf(c[e], c[e + 1]), m);
        m2 = fmaxf(fmaxf(c[8 + e], c[9 + e]), m2);
      }
      mx = fmaxf(mx, m);
      mx2 = fmaxf(mx2, m2);
      if (j + 3 <= 7) s[(3 - (j % 3)) % 3] = MFMA32(kf[j + 3], qf, zero);
    }
  } else
  {
    bf16x8 kf[9];
#pragma unroll
    for (int kt = 0; kt < 9; ++kt) kf[kt] = *(const bf16x8*)(krd + kt * 1024);
    f32x16 s[3];
    s[0] = MFMA32(kf[0], qf, zero);
    s[1] = MFMA32(kf[1], qf, zero);
#pragma unroll
    for (int kt = 0; kt < 9; ++kt) {
      if (kt < 7) s[(kt + 2) % 3] = MFMA32(kf[kt + 2], qf, zero);
      f32x16& c = s[kt % 3];
      mask(kt, c);
      float m = NEG, m2 = NEG;
#pragma unroll
      for (int e = 0; e < 8; e += 2) {
        m = fmaxf(fmaxf(c[e], c[e + 1]), m);
        m2 = fmaxf(fmaxf(c[8 + e], c[9 + e]), m2);
      }
      if (FIRST) {
        m = (kt >= kt0) ? m : NEG;
        m2 = (kt >= kt0) ? m2 : NEG;
      }
      mx = fmaxf(mx, m);
      mx2 = fmaxf(mx2, m2);
    }
  }
  mx = fmaxf(mx, mx2);
  mx = fmaxf(mx, __shfl_xor(mx, 32));
  f32x16 negm;
#pragma unroll
  for (int e = 0; e < 16; ++e) negm[e] = -mx;
  STAMP(1, 2);
  const int vsw = (r >> 2) & 3;
  const bool isd = r < 16;
  const int cad = 32768 + ((r == 16 || r == 20) ? 0 : 16);
  int va0 = isd ? 16384 + wv * 1024 + r * 64 + ((hh ^ vsw) * 16) : cad;
  int va1 = isd ? 16384 + wv * 1024 + r * 64 + (((2 + hh) ^ vsw) * 16) : cad;
  const int vstep = isd ? 1024 : 0;
  f32x16 oacc = zero;
  if (!FIRST) {
    f32x16 s[2];
    bf16x8 kf = *(const bf16x8*)(krd);
    bf16x8 kg = *(const bf16x8*)(krd + 8 * 1024);
    s[0] = MFMA32(kf, qf, negm);
    s[1] = MFMA32(kg, qf, negm);
    {
      const bf16x8 vf0 = *(const bf16x8*)(smem + va0);
      const bf16x8 vf1 = *(const bf16x8*)(smem + va1);
      const bf16x8 vg0 = *(const bf16x8*)(smem + va0 + 8 * vstep);
      const bf16x8 vg1 = *(const bf16x8*)(smem + va1 + 8 * vstep);
      va0 += vstep;
      va1 += vstep;
      kf = *(const bf16x8*)(krd + 1024);
#pragma unroll
      for (int e = 0; e < 16; ++e) s[0][e] = ((e & 3) + 8 * (e >> 2) + 4 * hh > r) ? s[0][e] : s[1][e];
      s[1] = MFMA32(kf, qf, negm);
      u32x4 p0[2], p8[2];
#pragma unroll
      for (int e = 0; e < 16; e += 2) {
        const unsigned pk = pack2(__builtin_amdgcn_exp2f(s[0][e]), __builtin_amdgcn_exp2f(s[0][e + 1]));
        const int td = r - ((e & 3) + 8 * (e >> 2) + 4 * hh);
        const unsigned m8 = td >= 1 ? 0xffffffffu : (td == 0 ? 0x0000ffffu : 0u);
        p8[e >> 3][(e >> 1) & 3] = pk & m8;
        p0[e >> 3][(e >> 1) & 3] = pk & ~m8;
      }
      oacc = MFMA32(vf0, __builtin_bit_cast(bf16x8, p0[0]), oacc);
      oacc = MFMA32(vf1, __builtin_bit_cast(bf16x8, p0[1]), oacc);
      oacc = MFMA32(vg0, __builtin_bit_cast(bf16x8, p8[0]), oacc);
      oacc = MFMA32(vg1, __builtin_bit_cast(bf16x8, p8[1]), oacc);
    }
#pragma unroll
    for (int kt = 1; kt < 8; ++kt) {
      const bf16x8 vf0 = *(const bf16x8*)(smem + va0);
      const bf16x8 vf1 = *(const bf16x8*)(smem + va1);
      va0 += vstep;
      va1 += vstep;
      if (kt < 7) {
        kf = *(const bf16x8*)(krd + (kt + 1) * 1024);
        s[(kt + 1) & 1] = MFMA32(kf, qf, negm);
      }
      f32x16& c = s[kt & 1];
      u32x4 pp[2];
#pragma unroll
      for (int e = 0; e < 16; e += 2) {
        pp[e >> 3][(e >> 1) & 3] = pack2(__builtin_amdgcn_exp2f(c[e]), __builtin_amdgcn_exp2f(c[e + 1]));
      }
      oacc = MFMA32(vf0, __builtin_bit_cast(bf16x8, pp[0]), oacc);
      oacc = MFMA32(vf1, __builtin_bit_cast(bf16x8, pp[1]), oacc);
    }
  } else
  {
    f32x16 s[2];
    bf16x8 kf = *(const bf16x8*)(krd);
    s[0] = MFMA32(kf, qf, negm);
#pragma unroll
    for (int kt = 0; kt < 9; ++kt) {
      const bf16x8 vf0 = *(const bf16x8*)(smem + va0);
      const bf16x8 vf1 = *(const bf16x8*)(smem + va1);
      va0 += vstep;
      va1 += vstep;
      if (kt < 8) {
        kf = *(const bf16x8*)(krd + (kt + 1) * 1024);
        s[(kt + 1) & 1] = MFMA32(kf, qf, negm);
      }
      f32x16& c = s[kt & 1];
      mask(kt, c);
      if (!FIRST || kt >= kt0) {
        u32x4 pp[2];
#pragma unroll
        for (int e = 0; e < 16; e += 2) {
          pp[e >> 3][(e >> 1) & 3] = pack2(__builtin_amdgcn_exp2f(c[e]), __builtin_amdgcn_exp2f(c[e + 1]));
        }
        oacc = MFMA32(vf0, __builtin_bit_cast(bf16x8, pp[0]), oacc);
        oacc = MFMA32(vf1, __builtin_bit_cast(bf16x8, pp[1]), oacc);
      }
    }
  }
  STAMP(1, 3);
  const float inv = 1.f / oacc[8];
  u32x2 v0 = {pack2(oacc[0] * inv, oacc[1] * inv), pack2(oacc[2] * inv, oacc[3] * inv)};
  u32x2 v1 = {pack2(oacc[4] * inv, oacc[5] * inv), pack2(oacc[6] * inv, oacc[7] * inv)};
  const rsrc_t rs = ws_rsrc(ws);
  const unsigned op = WS_O + (unsigned)((q0 + r) * DM + head * 16 + 4 * hh) * 2;
  wst8<AUX_ST2>(rs, op, v0);
  wst8<AUX_ST2>(rs, op + 16, v1);
  STAMP(1, 4);
}

DI void conv_block(const u16* __restrict__ ssmk, const u16* __restrict__ ssmD, char* __restrict__ ws, char* smem,
                   int d) {
  const int t = threadIdx.x & 255;
  const int l = t & 63, wv = t >> 6;
  const int r = l & 31, hh = l >> 5;
  char* hs = smem;
  char* kvs = smem + CV_HBYTES;
  const u16* kvT = (const u16*)(ws + WS_KVT) + (size_t)d * SEQ;
  const u16* hrow = ssmk + (size_t)d * SEQ;
  STAMP(1, 0);
  const u32x4 kvv = ld16<AUX_LD2 != 0>(kvT + 8 * t);
  const u32x4 whi = *(const u32x4*)(hrow + 8 * t);
  const u32x4 wlo0 = *(const u32x4*)(hrow + 8 * (t > 0 ? t - 1 : 0));
  const float Dd = bf2f(ssmD[d]);
  *(u32x4*)(kvs + ((t >> 2) + 32) * CV_KVSTR + (t & 3) * 16) = kvv;
  if (t < 128) {
    u32x4 z = {0u, 0u, 0u, 0u};
    *(u32x4*)(kvs + (t >> 2) * CV_KVSTR + (t & 3) * 16) = z;
  }
  {
    const int E = t;
    u32x4 wlo = wlo0;
    if (E == 0) wlo = (u32x4){0u, 0u, 0u, 0u};
    unsigned z[8];
#pragma unroll
    for (int i = 0; i < 4; ++i) {
      const unsigned a = whi[3 - i];
      z[i] = (a >> 16) | (a << 16);
      const unsigned b = wlo[3 - i];
      z[4 + i] = (b >> 16) | (b << 16);
    }
    const int M = 255 - E;
#pragma unroll
    for (int p = 0; p < 8; ++p) {
      const int o = 7 - p;
      u32x4 v;
      if ((o & 1) == 0) {
        v[0] = z[o / 2];
        v[1] = z[o / 2 + 1];
        v[2] = z[o / 2 + 2];
        v[3] = z[o / 2 + 3];
      } else {
        v[0] = (z[o / 2] >> 16) | (z[o / 2 + 1] << 16);
        v[1] = (z[o / 2 + 1] >> 16) | (z[o / 2 + 2] << 16);
        v[2] = (z[o / 2 + 2] >> 16) | (z[o / 2 + 3] << 16);
        v[3] = (z[o / 2 + 3] >> 16) | (z[o / 2 + 4] << 16);
      }
      *(u32x4*)(hs + p * CV_HSTR + 16 * M) = v;
    }
    if (t < 24) {
      u32x4 zz = {0u, 0u, 0u, 0u};
      *(u32x4*)(hs + (t & 7) * CV_HSTR + 16 * (256 + (t >> 3))) = zz;
    }
  }
  __syncthreads();
  STAMP(1, 1);
  f32x16 acc0, acc1, acc2;
#pragma unroll
  for (int e = 0; e < 16; ++e) {
    acc0[e] = 0.f;
    acc1[e] = 0.f;
    acc2[e] = 0.f;
  }
  const char* abase = hs + (r & 7) * CV_HSTR + 16 * (255 - (r >> 3) + hh) - 64 * wv;
  const char* bbase = kvs + (r + 32 - wv) * CV_KVSTR + 16 * hh;
  {
    bf16x8 fa[2][2], fb[2][2];
    auto ldc = [&](int step, bf16x8(&a)[2], bf16x8(&b)[2]) {
      const int i = step >> 1, ks = step & 1;
      a[0] = *(const bf16x8*)(abase - 256 * i + 32 * ks);
      a[1] = *(const bf16x8*)(abase - 256 * i - 2048 + 32 * ks);
      b[1] = *(const bf16x8*)(bbase + (32 - 4 * i) * CV_KVSTR + 32 * ks);
      b[0] = *(const bf16x8*)(bbase - (4 * i) * CV_KVSTR + 32 * ks);
    };
    ldc(0, fa[0], fb[0]);
#pragma unroll
    for (int step = 0; step < 16; ++step) {
      if (step < 15) ldc(step + 1, fa[(step + 1) & 1], fb[(step + 1) & 1]);
      __builtin_amdgcn_sched_barrier(0);
      acc1 = MFMA32(fa[step & 1][0], fb[step & 1][1], acc1);
      acc0 = MFMA32(fa[step & 1][0], fb[step & 1][0], acc0);
      acc2 = MFMA32(fa[step & 1][1], fb[step & 1][0], acc2);
      __builtin_amdgcn_sched_barrier(0);
    }
  }
  acc1 += acc2;
  STAMP(1, 2);
  __syncthreads();
  STAMP(1, 6);
  float* red = (float*)smem;
  {
    float* rw = red + wv * (64 * CV_REDSTR);
#pragma unroll
    for (int g = 0; g < 4; ++g) {
      f32x4 v0 = {acc0[4 * g], acc0[4 * g + 1], acc0[4 * g + 2], acc0[4 * g + 3]};
      f32x4 v1 = {acc1[4 * g], acc1[4 * g + 1], acc1[4 * g + 2], acc1[4 * g + 3]};
      *(f32x4*)(rw + r * CV_REDSTR + 8 * g + 4 * hh) = v0;
      *(f32x4*)(rw + (32 + r) * CV_REDSTR + 8 * g + 4 * hh) = v1;
    }
  }
  __syncthreads();
  STAMP(1, 7);
  {
    const int a = t >> 2, b0 = 8 * (t & 3);
    f32x4 s0 = {0.f, 0.f, 0.f, 0.f}, s1 = {0.f, 0.f, 0.f, 0.f};
#pragma unroll
    for (int w = 0; w < 4; ++w) {
      const float* rp = red + w * (64 * CV_REDSTR) + a * CV_REDSTR + b0;
      s0 += *(const f32x4*)(rp);
      s1 += *(const f32x4*)(rp + 4);
    }
    float yy[8] = {s0[0], s0[1], s0[2], s0[3], s1[0], s1[1], s1[2], s1[3]};
    u32x4 o;
#pragma unroll
    for (int i = 0; i < 4; ++i) {
      const float y0 = rbf(yy[2 * i]) + rbf(bflo(kvv[i]) * Dd);
      const float y1 = rbf(yy[2 * i + 1]) + rbf(bfhi(kvv[i]) * Dd);
      o[i] = pack2(y0, y1);
    }
    wst16<AUX_Y2T>(ws_rsrc(ws), WS_Y2T + (unsigned)(d * SEQ + 8 * t) * 2, o);
  }
  STAMP(1, 4);
}

__global__ __launch_bounds__(512, 4) void k2_kernel(const u16* __restrict__ ssmk, const u16* __restrict__ ssmD,
                                                    const u16* __restrict__ wo, const u16* __restrict__ x,
                                                    const u16* __restrict__ gatew, char* __restrict__ ws) {
  __shared__ __attribute__((aligned(16))) char smem[2 * CV_SMEM];
  const int xcd = blockIdx.x & 7, bidx = (blockIdx.x >> 3) ^ 32;
  if (bidx >= 32) {
#define PF_WO 2
    const int half = threadIdx.x >> 8;
    conv_block(ssmk, ssmD, ws, smem + half * CV_SMEM, xcd * 64 + (bidx - 32) * 2 + half);
    const u32x4 pf0 = *(const u32x4*)(wo + (size_t)(bidx - 32) * 8192 + threadIdx.x * 8);
    const u32x4 pf1 = *(const u32x4*)(wo + (size_t)(bidx - 32) * 8192 + 4096 + threadIdx.x * 8);
    {
      const int l = threadIdx.x & 63;
      const int row = (xcd * 32 + (bidx - 32)) * 8 + (threadIdx.x >> 6);
      const u32x4 gw = *(const u32x4*)(gatew + l * 8);
      const u32x4 xv = ld16<false>(x + (size_t)row * DM + l * 8);
      float sum = 0.f;
#pragma unroll
      for (int i = 0; i < 4; ++i) {
        const unsigned pr = pack2(bflo(xv[i]) * bflo(gw[i]), bfhi(xv[i]) * bfhi(gw[i]));
        sum += bflo(pr);
        sum += bfhi(pr);
      }
#pragma unroll
      for (int o = 32; o >= 1; o >>= 1) sum += __shfl_xor(sum, o);
      if (l == 0) ((float*)(ws + WS_G))[row] = rbf(1.f / (1.f + __expf(-rbf(sum))));
    }
    asm volatile("" ::"v"(pf0));
    asm volatile("" ::"v"(pf1));
  } else {
    const int head = 4 * xcd + (bidx & 3), win = bidx >> 2;
    if (win == 0)
      attn_block<true>(ws, smem, head, 0);
    else
      attn_block<false>(ws, smem, head, win * 256);
  }
}

#define K3_STAGE 32768
#define K3_CSTR 68
#define K3_YSTR 136

DI void k3_body(const u16* __restrict__ x, const u16* __restrict__ wo, const char* __restrict__ ws,
                u16* __restrict__ out, char* smem) {
  const int t = threadIdx.x;
  const int l = t & 63, wv = t >> 6;
  const int r = l & 31, hh = l >> 5;
  const int wm = wv & 1, wn = (wv >> 1) & 1, kh = wv >> 2;
  const int tm = (blockIdx.x & 7) * 4 + ((blockIdx.x >> 3) & 3), tn = blockIdx.x >> 5;
  const int m0 = tm * 64, n0 = tn * 64;
  const u16* o = (const u16*)(ws + WS_O);
  STAMP(2, 0);

  const u16* y2T = (const u16*)(ws + WS_Y2T);
  const float* gp = (const float*)(ws + WS_G);
  const int rr = t >> 3, ch = t & 7;
  const u32x4 yv = ld16<AUX_LD3 != 0>(y2T + (size_t)(n0 + rr) * SEQ + m0 + ch * 8);
  const u32x4 xq = ld16<false>(x + (size_t)(m0 + rr) * DM + n0 + ch * 8);
  const float g = gp[m0 + rr];
  __builtin_amdgcn_sched_barrier(0);
  {
    char* ldsw = smem + wv * 2048 + l * 16;
#pragma unroll
    for (int s4 = 0; s4 < 4; ++s4) {
      const int st = s4;
#pragma unroll
      for (int i = 0; i < 2; ++i) {
        const int ar = 8 * wv + 4 * i + (l >> 4);
        GLDS16A(o + (size_t)(m0 + ar) * DM + st * 128 + (((l & 15) ^ (ar & 15)) * 8), ldsw + st * K3_STAGE + i * 1024,
                AUX_LD3);
      }
#pragma unroll
      for (int i = 0; i < 2; ++i) {
        const int br = 16 * wv + 8 * i + (l >> 3);
        GLDS16(wo + (size_t)(st * 128 + br) * DM + n0 + (((l & 7) ^ (((br >> 1) & 1) << 2)) * 8),
               ldsw + st * K3_STAGE + 16384 + i * 1024);
      }
    }
  }

  f32x16 acc;
#pragma unroll
  for (int e = 0; e < 16; ++e) acc[e] = 0.f;
  const int blk = (l >> 4) & 1, q4 = (l & 15) >> 2, p4 = l & 3;
  const int arow = wm * 32 + r;
  const int a_off = arow * 256;
  const int a_sw = arow & 15;
  const int b_off = 16384 + (8 * hh + q4) * 128 + (((wn * 4 + 2 * blk + (p4 >> 1)) ^ (((q4 >> 1) & 1) << 2)) * 16) +
                    (p4 & 1) * 8;
  const int a_x = (8 * kh + hh) ^ a_sw;
  const int b_k = b_off + 64 * kh * 128;
#pragma unroll
  for (int st = 0; st < 4; ++st) {
    if (st == 0) WAIT_V(12);
    else if (st == 1) WAIT_V(8);
    else if (st == 2) WAIT_V(4);
    else WAIT_V(0);
    RAWBAR();
    const char* sb = smem + st * K3_STAGE;
    bf16x8 af[4], bfr[4];
#pragma unroll
    for (int ks = 0; ks < 4; ++ks) {
      af[ks] = *(const bf16x8*)(sb + a_off + ((a_x ^ (2 * ks)) * 16));
      s16x4 lo = tr_read(sb + b_k + (16 * ks) * 128);
      s16x4 hi = tr_read(sb + b_k + (16 * ks + 4) * 128);
      bfr[ks] = cat_frag(lo, hi);
    }
    __builtin_amdgcn_sched_barrier(0);
#pragma unroll
    for (int ks = 0; ks < 4; ++ks) acc = MFMA32(af[ks], bfr[ks], acc);
    __builtin_amdgcn_sched_barrier(0);
  }
  __syncthreads();
  STAMP(2, 5);
  float* Cs = (float*)smem;
  char* Ys = smem + 2 * 64 * K3_CSTR * 4;
#pragma unroll
  for (int e = 0; e < 16; ++e) {
    const int row = wm * 32 + (e & 3) + 8 * (e >> 2) + 4 * hh;
    Cs[kh * 64 * K3_CSTR + row * K3_CSTR + wn * 32 + r] = acc[e];
  }
  {
    u32x2 a = {yv[0], yv[1]}, b = {yv[2], yv[3]};
    *(u32x2*)(Ys + rr * K3_YSTR + ch * 16) = a;
    *(u32x2*)(Ys + rr * K3_YSTR + ch * 16 + 8) = b;
  }
  __syncthreads();
  {
    const int row = rr;
    const int s = m0 + row;
    const float omg = rbf(1.f - g);
    const f32x4 c0 = *(const f32x4*)(Cs + row * K3_CSTR + ch * 8) + *(const f32x4*)(Cs + (64 + row) * K3_CSTR + ch * 8);
    const f32x4 c1 =
        *(const f32x4*)(Cs + row * K3_CSTR + ch * 8 + 4) + *(const f32x4*)(Cs + (64 + row) * K3_CSTR + ch * 8 + 4);
    float bb[8] = {c0[0], c0[1], c0[2], c0[3], c1[0], c1[1], c1[2], c1[3]};
    float res[8];
#pragma unroll
    for (int j = 0; j < 8; ++j) {
      const float y2 = bf2f(*(const u16*)(Ys + (ch * 8 + j) * K3_YSTR + row * 2));
      const float qv = (j & 1) ? bfhi(xq[j >> 1]) : bflo(xq[j >> 1]);
      const float a = rbf(qv * y2);
      const float ga = rbf(g * a);
      const float gb = rbf(omg * rbf(bb[j]));
      res[j] = ga + gb;
    }
    u32x4 ov;
    ov[0] = pack2(res[0], res[1]);
    ov[1] = pack2(res[2], res[3]);
    ov[2] = pack2(res[4], res[5]);
    ov[3] = pack2(res[6], res[7]);
    *(u32x4*)(out + (size_t)s * DM + n0 + ch * 8) = ov;
  }
  STAMP(2, 6);
}

__global__ __launch_bounds__(512) void k3_kernel(const u16* __restrict__ x, const u16* __restrict__ wo,
                                                 const char* __restrict__ ws, u16* __restrict__ out) {
  __shared__ __attribute__((aligned(16))) char smem[4 * K3_STAGE];
  k3_body(x, wo, ws, out, smem);
}

extern "C" void kernel_launch(void* const* d_in, const int* in_sizes, int n_in, void* d_out, int out_size,
                              void* d_ws, size_t ws_size, hipStream_t stream) {
  const u16* A_q = (const u16*)d_in[0];
  const u16* A_k = (const u16*)d_in[1];
  const u16* A_v = (const u16*)d_in[2];
  const u16* A_shift = (const u16*)d_in[3];
  const u16* A_ssmk = (const u16*)d_in[4];
  const u16* A_ssmD = (const u16*)d_in[5];
  const u16* B_wqkv = (const u16*)d_in[7];
  const u16* B_wo = (const u16*)d_in[8];
  const u16* gate_w = (const u16*)d_in[9];
  char* ws = (char*)d_ws;
  k1_kernel<<<256, 512, 0, stream>>>(A_q, A_k, A_v, A_shift, B_wqkv, A_ssmk, ws);
  k2_kernel<<<512, 512, 0, stream>>>(A_ssmk, A_ssmD, B_wo, A_q, gate_w, ws);
  k3_kernel<<<256, 512, 0, stream>>>(A_q, B_wo, ws, (u16*)d_out);
}
```

```cpp
#include <hip/hip_runtime.h>
#include <hip/hip_bf16.h>
#include <stdint.h>

#define DI __device__ __forceinline__
typedef __attribute__((ext_vector_type(8))) __bf16 bf16x8;
typedef __attribute__((ext_vector_type(4))) __bf16 bf16x4;
typedef __attribute__((ext_vector_type(2))) __bf16 bf16x2;
typedef __attribute__((ext_vector_type(4))) short s16x4;
typedef __attribute__((ext_vector_type(16))) float f32x16;
typedef __attribute__((ext_vector_type(4))) float f32x4;
typedef __attribute__((ext_vector_type(2))) float f32x2;
typedef __attribute__((ext_vector_type(4))) unsigned u32x4;
typedef __attribute__((ext_vector_type(2))) unsigned u32x2;
typedef unsigned short u16;

#define SEQ 2048
#define DM 512

#define MFMA32(a, b, c) __builtin_amdgcn_mfma_f32_32x32x16_bf16((a), (b), (c), 0, 0, 0)

DI float bf2f(u16 v) { return __uint_as_float(((unsigned)v) << 16); }
DI float bflo(unsigned v) { return __uint_as_float(v << 16); }
DI float bfhi(unsigned v) { return __uint_as_float(v & 0xffff0000u); }
DI unsigned pack2(float a, float b) {
  f32x2 f = {a, b};
  bf16x2 r = __builtin_convertvector(f, bf16x2);
  return __builtin_bit_cast(unsigned, r);
}
DI float rbf(float a) {
  return (float)((__bf16)a);
}
DI s16x4 tr_read(const char* p) {
  typedef __attribute__((address_space(3))) s16x4* lp;
  return __builtin_amdgcn_ds_read_tr16_b64_v4i16((lp)(p));
}
DI bf16x8 cat_frag(s16x4 lo, s16x4 hi) {
  typedef __attribute__((ext_vector_type(8))) short s16x8;
  s16x8 v = __builtin_shufflevector(lo, hi, 0, 1, 2, 3, 4, 5, 6, 7);
  return __builtin_bit_cast(bf16x8, v);
}

#define STAMP(k, i) do {} while (0)
#define WS_Q 0
#define WS_K (2u << 20)
#define WS_VT (4u << 20)
#define WS_KVT (6u << 20)
#define WS_Y2T (8u << 20)
#define WS_O (10u << 20)
#define WS_G (12u << 20)

typedef __amdgpu_buffer_rsrc_t rsrc_t;
DI rsrc_t ws_rsrc(const char* ws) { return __builtin_amdgcn_make_buffer_rsrc((void*)ws, (short)0, 16 << 20, 0x00020000); }
#define AUX_WT 16
#define AUX_ST1 16
#define AUX_ST2 16
#define AUX_KVT 16
#define AUX_Y2T 16
#define AUX_LD2 2
#define AUX_LD3 0
template <int AUX>
DI void wst16(rsrc_t rs, unsigned off, u32x4 v) { __builtin_amdgcn_raw_buffer_store_b128(v, rs, (int)off, 0, AUX); }
template <int AUX>
DI void wst8(rsrc_t rs, unsigned off, u32x2 v) { __builtin_amdgcn_raw_buffer_store_b64(v, rs, (int)off, 0, AUX); }
template <bool NT>
DI u32x4 ld16(const void* p) {
  if constexpr (NT) return __builtin_nontemporal_load((const u32x4*)p);
  else return *(const u32x4*)p;
}

#define GLDS16(g, l) __builtin_amdgcn_global_load_lds((const unsigned*)(g), (unsigned*)(l), 16, 0, 0)
#define GLDS16A(g, l, aux) __builtin_amdgcn_global_load_lds((const unsigned*)(g), (unsigned*)(l), 16, 0, aux)
#define AUX_X 2
#define AUX_W 0
#define WAIT_V(n) asm volatile("s_waitcnt vmcnt(" #n ")" ::: "memory")
#define RAWBAR() do { asm volatile("s_waitcnt lgkmcnt(0)" ::: "memory"); __builtin_amdgcn_s_barrier(); } while (0)
#define K1_STAGE 32768
#define K1_NST 4

template <int N>
DI void wait_vm() {
  if constexpr (N == 0) WAIT_V(0);
  else if constexpr (N == 4) WAIT_V(4);
  else if constexpr (N == 8) WAIT_V(8);
  else if constexpr (N == 10) WAIT_V(10);
  else if constexpr (N == 12) WAIT_V(12);
  else if constexpr (N == 14) WAIT_V(14);
  else if constexpr (N == 16) WAIT_V(16);
  else if constexpr (N == 24) WAIT_V(24);
}

#define QSCALE (0.25f * 1.44269504088896340736f)

template <int MODE>
DI void k1_gemm(const u16* __restrict__ x, const u16* __restrict__ w, char* __restrict__ ws, char* smem, int tm,
                int tn) {
  const int t = threadIdx.x;
  const int l = t & 63, wv = t >> 6;
  const int r = l & 31, hh = l >> 5;
  const int wm = wv & 1, wn = wv >> 1;
  const int m0 = tm * 128, n0 = tn * 128;

  const u16* xg[2];
  const u16* wg[2];
#pragma unroll
  for (int i = 0; i < 2; ++i) {
    const int ar = 16 * wv + 8 * i + (l >> 3);
    xg[i] = x + (size_t)(m0 + ar) * DM + (((l & 7) ^ ((ar >> 1) & 7)) * 8);
    const int br = 8 * wv + 4 * i + (l >> 4);
    wg[i] = w + (size_t)br * 1536 + n0 + (((l & 15) ^ ((br & 3) << 2)) * 8);
  }
  char* ldsw = smem + wv * 2048 + l * 16;

  auto issue1 = [&](int kt, int j) {
    char* st = ldsw + (kt % K1_NST) * K1_STAGE;
    if (j < 2)
      GLDS16A(xg[j] + kt * 64, st + j * 1024, AUX_X);
    else
      GLDS16A(wg[j - 2] + (size_t)kt * 64 * 1536, st + 16384 + (j - 2) * 1024, AUX_W);
  };

  f32x16 acc[2];
#pragma unroll
  for (int i = 0; i < 2; ++i)
#pragma unroll
    for (int e = 0; e < 16; ++e) acc[i][e] = 0.f;

  const int blk = (l >> 4) & 1, q4 = (l & 15) >> 2, p4 = l & 3;
  const int a_row_off = (wm * 64 + r) * 128;
  const int a_sw = (r >> 1) & 7;
  const int b_base = 16384 + (8 * hh + q4) * 256 + (p4 & 1) * 8 + (((wn * 4 + 2 * blk + (p4 >> 1)) ^ (q4 << 2)) * 16);

  STAMP(0, 0);
  constexpr int NA = 3, NB = 1;
#pragma unroll
  for (int kt = 0; kt < NA; ++kt)
#pragma unroll
    for (int j = 0; j < 4; ++j) issue1(kt, j);
  bf16x8 fa[2][4][2], fb[2][4];
  wait_vm<4 * (NA - 1)>();
  RAWBAR();
#pragma unroll
  for (int ks = 0; ks < 4; ++ks) {
#pragma unroll
    for (int i = 0; i < 2; ++i)
      fa[0][ks][i] = *(const bf16x8*)(smem + a_row_off + i * 32 * 128 + (((2 * ks + hh) ^ a_sw) * 16));
    s16x4 lo = tr_read(smem + b_base + (16 * ks) * 256);
    s16x4 hi = tr_read(smem + b_base + (16 * ks + 4) * 256);
    fb[0][ks] = cat_frag(lo, hi);
  }
  __builtin_amdgcn_sched_barrier(0);
#pragma unroll
  for (int kt = NA; kt < NA + NB; ++kt)
#pragma unroll
    for (int j = 0; j < 4; ++j) issue1(kt, j);
  __builtin_amdgcn_sched_barrier(0);
#pragma unroll
  for (int kt = 0; kt < 8; ++kt) {
    const char* stn = smem + ((kt + 1) % K1_NST) * K1_STAGE;
    bf16x8(&an)[4][2] = fa[(kt + 1) & 1];
    bf16x8(&bn)[4] = fb[(kt + 1) & 1];
    s16x4 lo, hi;
    auto rdpair = [&](int j) {
      const int ks = j >> 1, i = j & 1;
      an[ks][i] = *(const bf16x8*)(stn + a_row_off + i * 32 * 128 + (((2 * ks + hh) ^ a_sw) * 16));
      if (i == 0) {
        lo = tr_read(stn + b_base + (16 * ks) * 256);
      } else {
        hi = tr_read(stn + b_base + (16 * ks + 4) * 256);
        bn[ks] = cat_frag(lo, hi);
      }
    };
    const int i0 = (kt == 0) ? (NA + NB) : ((kt + 4 < 8) ? kt + 4 : 8);
    const int i1 = (kt + 5 < 8) ? kt + 5 : 8;
    if (kt < 7) {
      const int allow = i0 - (kt + 2);
      if (allow == 3) wait_vm<12>();
      else if (allow == 2) wait_vm<8>();
      else if (allow == 1) wait_vm<4>();
      else wait_vm<0>();
      RAWBAR();
    }
    __builtin_amdgcn_sched_barrier(0);
#pragma unroll
    for (int g = 0; g < 8; ++g) {
      const int ks = g >> 1, i = g & 1;
      if (MODE == 2)
        acc[i] = MFMA32(fa[kt & 1][ks][i], fb[kt & 1][ks], acc[i]);
      else
        acc[i] = MFMA32(fb[kt & 1][ks], fa[kt & 1][ks][i], acc[i]);
      __builtin_amdgcn_sched_barrier(0);
      if (kt < 7) {
        if (g < 2) {
          rdpair(2 * g);
          rdpair(2 * g + 1);
        } else if (g < 6) {
          rdpair(g + 2);
        }
      }
      if (i1 - i0 == 2) {
        issue1(i0 + (g >> 2), g & 3);
      } else if (i1 - i0 == 1) {
        if (i == 1) issue1(i0, ks);
      }
      __builtin_amdgcn_sched_barrier(0);
    }
  }

  STAMP(0, 9);
  const rsrc_t rs = ws_rsrc(ws);
  const int nbw = (n0 & 511) + wn * 32;
  if (MODE == 2) {
#pragma unroll
    for (int i = 0; i < 2; ++i) {
      const int n = nbw + r;
      const int tok0 = m0 + wm * 64 + i * 32;
      const unsigned dst = WS_VT + (unsigned)(((n >> 4) * 64 + (tok0 >> 5)) * 512 + (n & 15) * 16 + 8 * hh) * 2;
#pragma unroll
      for (int bb = 0; bb < 2; ++bb) {
        u32x4 v;
        v[0] = pack2(acc[i][8 * bb + 0], acc[i][8 * bb + 1]);
        v[1] = pack2(acc[i][8 * bb + 2], acc[i][8 * bb + 3]);
        v[2] = pack2(acc[i][8 * bb + 4], acc[i][8 * bb + 5]);
        v[3] = pack2(acc[i][8 * bb + 6], acc[i][8 * bb + 7]);
        wst16<AUX_ST1>(rs, dst + 512 * bb, v);
      }
    }
  } else {
    const unsigned base = (MODE == 1 ? WS_K : WS_Q);
    const int nn0 = n0 & 511;
#pragma unroll
    for (int i = 0; i < 2; ++i) {
      const int tok = m0 + wm * 64 + i * 32 + r;
      const int nb = nbw;
      const int head = nb >> 4;
#pragma unroll
      for (int g2 = 0; g2 < 2; ++g2) {
        const float sc = (MODE == 0) ? QSCALE : 1.f;
        const unsigned a0 = pack2(acc[i][8 * g2 + 0] * sc, acc[i][8 * g2 + 1] * sc);
        const unsigned a1 = pack2(acc[i][8 * g2 + 2] * sc, acc[i][8 * g2 + 3] * sc);
        const unsigned b0 = pack2(acc[i][8 * g2 + 4] * sc, acc[i][8 * g2 + 5] * sc);
        const unsigned b1 = pack2(acc[i][8 * g2 + 6] * sc, acc[i][8 * g2 + 7] * sc);
        const auto sx = __builtin_amdgcn_permlane32_swap(a0, b0, false, false);
        const auto sy = __builtin_amdgcn_permlane32_swap(a1, b1, false, false);
        const u32x4 v = {sx[0], sy[0], sx[1], sy[1]};
        wst16<AUX_ST1>(rs, base + (unsigned)(((head + g2) * SEQ + tok) * 16 + 8 * hh) * 2, v);
      }
    }
  }
  STAMP(0, 10);
}

__global__ __launch_bounds__(512) void k1_kernel(const u16* __restrict__ x, const u16* __restrict__ kin,
                                                 const u16* __restrict__ vin, const u16* __restrict__ shiftk,
                                                 const u16* __restrict__ wqkv, const u16* __restrict__ ssmk,
                                                 const u16* __restrict__ gatew, char* __restrict__ ws) {
  __shared__ __attribute__((aligned(16))) char smem[K1_NST * K1_STAGE];
  const int xcd = blockIdx.x & 7, bidx = blockIdx.x >> 3;
  if (bidx < 24) {
    const int tm = 2 * xcd + (bidx & 1), tn = bidx >> 1;
    if (tn >= 8)
      k1_gemm<2>(x, wqkv, ws, smem, tm, tn);
    else if (tn >= 4)
      k1_gemm<1>(x, wqkv, ws, smem, tm, tn);
    else
      k1_gemm<0>(x, wqkv, ws, smem, tm, tn);
    return;
  }
  const int l = threadIdx.x & 63, wv = threadIdx.x >> 6;
  const int t = threadIdx.x & 255, h2 = threadIdx.x >> 8;
  const int pb = xcd * 8 + (bidx - 24);
  STAMP(0, 0);
  u32x4 pf[4];
  u32x4 gw, xv[4];
  {
    const int ch = t & 15, rg = t >> 4;
    u32x4 kr[7], vr[4], sk[4];
    const int tile = pb * 2 + h2;
    const int s0 = (tile >> 2) * 64, d0 = (tile & 3) * 128;
    {
      const int sb = s0 + rg * 4;
      const int dd = d0 + ch * 8;
#pragma unroll
      for (int i = 0; i < 7; ++i) {
        const int s = sb - 3 + i;
        kr[i] = __builtin_nontemporal_load((const u32x4*)(kin + (size_t)(s < 0 ? 0 : s) * DM + dd));
      }
#pragma unroll
      for (int i = 0; i < 4; ++i) vr[i] = __builtin_nontemporal_load((const u32x4*)(vin + (size_t)(sb + i) * DM + dd));
#pragma unroll
      for (int i = 0; i < 4; ++i) sk[i] = *(const u32x4*)(shiftk + (size_t)dd * 4 + i * 8);
    }
    __builtin_amdgcn_sched_barrier(0);
    gw = *(const u32x4*)(gatew + l * 8);
#pragma unroll
    for (int i = 0; i < 4; ++i) xv[i] = *(const u32x4*)(x + (size_t)(pb * 32 + wv * 4 + i) * DM + l * 8);
    __builtin_amdgcn_sched_barrier(0);
    if (s0 + rg * 4 == 0) {
#pragma unroll
      for (int i = 0; i < 3; ++i) kr[i] = (u32x4){0u, 0u, 0u, 0u};
    }
    char* sm = smem + h2 * 17408;
#pragma unroll
    for (int c = 0; c < 4; ++c) {
      const f32x2 w0 = {bflo(sk[c][0]), bflo(sk[c][2])}, w1 = {bfhi(sk[c][0]), bfhi(sk[c][2])};
      const f32x2 w2 = {bflo(sk[c][1]), bflo(sk[c][3])}, w3 = {bfhi(sk[c][1]), bfhi(sk[c][3])};
      f32x2 kk[7];
#pragma unroll
      for (int i = 0; i < 7; ++i) kk[i] = (f32x2){bflo(kr[i][c]), bfhi(kr[i][c])};
      f32x2 r[4];
#pragma unroll
      for (int i = 0; i < 4; ++i) {
        f32x2 a = kk[i] * w3;
        a = __builtin_elementwise_fma(kk[i + 1], w2, a);
        a = __builtin_elementwise_fma(kk[i + 2], w1, a);
        a = __builtin_elementwise_fma(kk[i + 3], w0, a);
        const unsigned ap = pack2(a[0], a[1]);
        r[i] = (f32x2){bflo(ap), bfhi(ap)} * (f32x2){bflo(vr[i][c]), bfhi(vr[i][c])};
      }
      *(u32x2*)(sm + (ch * 8 + 2 * c) * 136 + rg * 8) = (u32x2){pack2(r[0][0], r[1][0]), pack2(r[2][0], r[3][0])};
      *(u32x2*)(sm + (ch * 8 + 2 * c + 1) * 136 + rg * 8) = (u32x2){pack2(r[0][1], r[1][1]), pack2(r[2][1], r[3][1])};
    }
    STAMP(0, 14);
    __builtin_amdgcn_sched_barrier(0);
#pragma unroll
    for (int i = 0; i < 4; ++i) pf[i] = *(const u32x4*)(ssmk + (size_t)pb * 16384 + (i * 512 + threadIdx.x) * 8);
    __builtin_amdgcn_sched_barrier(0);
    RAWBAR();
    STAMP(0, 15);
    const rsrc_t rs = ws_rsrc(ws);
#pragma unroll
    for (int i = 0; i < 4; ++i) {
      const int idx = t + 256 * i;
      const int dr = idx >> 3, c8 = idx & 7;
      const u32x2 a = *(const u32x2*)(sm + dr * 136 + c8 * 16);
      const u32x2 b = *(const u32x2*)(sm + dr * 136 + c8 * 16 + 8);
      u32x4 v = {a[0], a[1], b[0], b[1]};
      wst16<AUX_KVT>(rs, WS_KVT + (unsigned)((d0 + dr) * SEQ + s0 + c8 * 8) * 2, v);
    }
  }
  STAMP(0, 11);
  {
    float sum[4];
#pragma unroll
    for (int j = 0; j < 4; ++j) {
      sum[j] = 0.f;
#pragma unroll
      for (int i = 0; i < 4; ++i) {
        const unsigned pr = pack2(bflo(xv[j][i]) * bflo(gw[i]), bfhi(xv[j][i]) * bfhi(gw[i]));
        sum[j] += bflo(pr);
        sum[j] += bfhi(pr);
      }
    }
#pragma unroll
    for (int o = 32; o >= 1; o >>= 1)
#pragma unroll
      for (int j = 0; j < 4; ++j) sum[j] += __shfl_xor(sum[j], o);
    if (l == 0) {
      u32x4 gv;
#pragma unroll
      for (int j = 0; j < 4; ++j) gv[j] = __float_as_uint(rbf(1.f / (1.f + __expf(-rbf(sum[j])))));
      wst16<AUX_WT>(ws_rsrc(ws), WS_G + (unsigned)(pb * 32 + wv * 4) * 4, gv);
    }
  }
#pragma unroll
  for (int i = 0; i < 4; ++i) asm volatile("" ::"v"(pf[i]));
  STAMP(0, 12);
}

#define CV_KVSTR 80
#define CV_HSTR 4160
#define CV_HBYTES (8 * CV_HSTR)
#define CV_REDSTR 36
#define CV_SMEM 40960

template <bool FIRST>
DI void attn_block(const char* __restrict__ ws, char* smem, int head, int qb) {
  const int l = threadIdx.x & 63, wv = threadIdx.x >> 6;
  const int r = l & 31, hh = l >> 5;
  const u16* Qh = (const u16*)(ws + WS_Q) + (size_t)head * SEQ * 16;
  const u16* Kh = (const u16*)(ws + WS_K) + (size_t)head * SEQ * 16;
  const u16* Vh = (const u16*)(ws + WS_VT) + (size_t)head * 16 * SEQ;
  u16* O = (u16*)(ws + WS_O);
  const int q0 = qb + 32 * wv;
  const int kt0 = FIRST ? (8 - wv) : 0;
  STAMP(1, 0);
  char* ks_ = smem;
  char* vs_ = smem + 16384;
  char* cs_ = smem + 32768;
  {
    const int key = l >> 1;
    const u16* ksrc = Kh + (size_t)key * 16 + (((l & 1) ^ ((key >> 3) & 1)) * 8);
    const int dd = l >> 2;
    const int vc = (l & 3) ^ ((dd >> 2) & 3);
    const u16* vsrc = Vh + (vc >> 1) * 256 + dd * 16 + (vc & 1) * 8;
#pragma unroll
    for (int i = 0; i < 2; ++i) {
      const int u = wv + 8 * i;
      int key0 = qb - 256 + 32 * u;
      key0 = key0 < 0 ? 0 : key0;
      GLDS16A(ksrc + (size_t)key0 * 16, ks_ + u * 1024 + l * 16, AUX_LD2);
      GLDS16A(vsrc + (size_t)key0 * 16, vs_ + u * 1024 + l * 16, AUX_LD2);
    }
  }
  const bf16x8 qf = __builtin_bit_cast(bf16x8, ld16<AUX_LD2 != 0>(Qh + (size_t)(q0 + r) * 16 + 8 * hh));
  if (threadIdx.x < 2) {
    const unsigned o = threadIdx.x == 0 ? 0x3f803f80u : 0u;
    *(u32x4*)(cs_ + threadIdx.x * 16) = (u32x4){o, o, o, o};
  }
  WAIT_V(0);
  RAWBAR();
  STAMP(1, 1);
  const char* krd = ks_ + (32 * wv + r) * 32 + ((hh ^ ((r >> 3) & 1)) * 16);
  f32x16 zero;
#pragma unroll
  for (int e = 0; e < 16; ++e) zero[e] = 0.f;
  const float NEG = -3.0e38f;
  auto mask = [&](int kt, f32x16& s) {
    if (kt == 0) {
#pragma unroll
      for (int e = 0; e < 16; ++e) s[e] = ((e & 3) + 8 * (e >> 2) + 4 * hh > r) ? s[e] : NEG;
    }
    if (kt == 8) {
#pragma unroll
      for (int e = 0; e < 16; ++e) s[e] = ((e & 3) + 8 * (e >> 2) + 4 * hh <= r) ? s[e] : NEG;
    }
  };
  float mx = NEG, mx2 = NEG;
  if (!FIRST) {
    bf16x8 kf[9];
#pragma unroll
    for (int kt = 0; kt < 9; ++kt) kf[kt] = *(const bf16x8*)(krd + kt * 1024);
    f32x16 s[3];
    s[0] = MFMA32(kf[0], qf, zero);
    s[1] = MFMA32(kf[8], qf, zero);
    s[2] = MFMA32(kf[1], qf, zero);
#pragma unroll
    for (int e = 0; e < 16; ++e) s[0][e] = ((e & 3) + 8 * (e >> 2) + 4 * hh > r) ? s[0][e] : s[1][e];
    s[1] = MFMA32(kf[2], qf, zero);
#pragma unroll
    for (int j = 0; j < 8; ++j) {
      f32x16& c = s[(3 - (j % 3)) % 3];
      float m = NEG, m2 = NEG;
#pragma unroll
      for (int e = 0; e < 8; e += 2) {
        m = fmaxf(fmaxf(c[e], c[e + 1]), m);
        m2 = fmaxf(fmaxf(c[8 + e], c[9 + e]), m2);
      }
      mx = fmaxf(mx, m);
      mx2 = fmaxf(mx2, m2);
      if (j + 3 <= 7) s[(3 - (j % 3)) % 3] = MFMA32(kf[j + 3], qf, zero);
    }
  } else
  {
    bf16x8 kf[9];
#pragma unroll
    for (int kt = 0; kt < 9; ++kt) kf[kt] = *(const bf16x8*)(krd + kt * 1024);
    f32x16 s[3];
    s[0] = MFMA32(kf[0], qf, zero);
    s[1] = MFMA32(kf[1], qf, zero);
#pragma unroll
    for (int kt = 0; kt < 9; ++kt) {
      if (kt < 7) s[(kt + 2) % 3] = MFMA32(kf[kt + 2], qf, zero);
      f32x16& c = s[kt % 3];
      mask(kt, c);
      float m = NEG, m2 = NEG;
#pragma unroll
      for (int e = 0; e < 8; e += 2) {
        m = fmaxf(fmaxf(c[e], c[e + 1]), m);
        m2 = fmaxf(fmaxf(c[8 + e], c[9 + e]), m2);
      }
      if (FIRST) {
        m = (kt >= kt0) ? m : NEG;
        m2 = (kt >= kt0) ? m2 : NEG;
      }
      mx = fmaxf(mx, m);
      mx2 = fmaxf(mx2, m2);
    }
  }
  mx = fmaxf(mx, mx2);
  mx = fmaxf(mx, __shfl_xor(mx, 32));
  f32x16 negm;
#pragma unroll
  for (int e = 0; e < 16; ++e) negm[e] = -mx;
  STAMP(1, 2);
  const int vsw = (r >> 2) & 3;
  const bool isd = r < 16;
  const int cad = 32768 + ((r == 16 || r == 20) ? 0 : 16);
  int va0 = isd ? 16384 + wv * 1024 + r * 64 + ((hh ^ vsw) * 16) : cad;
  int va1 = isd ? 16384 + wv * 1024 + r * 64 + (((2 + hh) ^ vsw) * 16) : cad;
  const int vstep = isd ? 1024 : 0;
  f32x16 oacc = zero;
  if (!FIRST) {
    f32x16 s[2];
    bf16x8 kf = *(const bf16x8*)(krd);
    bf16x8 kg = *(const bf16x8*)(krd + 8 * 1024);
    s[0] = MFMA32(kf, qf, negm);
    s[1] = MFMA32(kg, qf, negm);
    {
      const bf16x8 vf0 = *(const bf16x8*)(smem + va0);
      const bf16x8 vf1 = *(const bf16x8*)(smem + va1);
      const bf16x8 vg0 = *(const bf16x8*)(smem + va0 + 8 * vstep);
      const bf16x8 vg1 = *(const bf16x8*)(smem + va1 + 8 * vstep);
      va0 += vstep;
      va1 += vstep;
      kf = *(const bf16x8*)(krd + 1024);
#pragma unroll
      for (int e = 0; e < 16; ++e) s[0][e] = ((e & 3) + 8 * (e >> 2) + 4 * hh > r) ? s[0][e] : s[1][e];
      s[1] = MFMA32(kf, qf, negm);
      u32x4 p0[2], p8[2];
#pragma unroll
      for (int e = 0; e < 16; e += 2) {
        const unsigned pk = pack2(__builtin_amdgcn_exp2f(s[0][e]), __builtin_amdgcn_exp2f(s[0][e + 1]));
        const int td = r - ((e & 3) + 8 * (e >> 2) + 4 * hh);
        const unsigned m8 = td >= 1 ? 0xffffffffu : (td == 0 ? 0x0000ffffu : 0u);
        p8[e >> 3][(e >> 1) & 3] = pk & m8;
        p0[e >> 3][(e >> 1) & 3] = pk & ~m8;
      }
      oacc = MFMA32(vf0, __builtin_bit_cast(bf16x8, p0[0]), oacc);
      oacc = MFMA32(vf1, __builtin_bit_cast(bf16x8, p0[1]), oacc);
      oacc = MFMA32(vg0, __builtin_bit_cast(bf16x8, p8[0]), oacc);
      oacc = MFMA32(vg1, __builtin_bit_cast(bf16x8, p8[1]), oacc);
    }
#pragma unroll
    for (int kt = 1; kt < 8; ++kt) {
      const bf16x8 vf0 = *(const bf16x8*)(smem + va0);
      const bf16x8 vf1 = *(const bf16x8*)(smem + va1);
      va0 += vstep;
      va1 += vstep;
      if (kt < 7) {
        kf = *(const bf16x8*)(krd + (kt + 1) * 1024);
        s[(kt + 1) & 1] = MFMA32(kf, qf, negm);
      }
      f32x16& c = s[kt & 1];
      u32x4 pp[2];
#pragma unroll
      for (int e = 0; e < 16; e += 2) {
        pp[e >> 3][(e >> 1) & 3] = pack2(__builtin_amdgcn_exp2f(c[e]), __builtin_amdgcn_exp2f(c[e + 1]));
      }
      oacc = MFMA32(vf0, __builtin_bit_cast(bf16x8, pp[0]), oacc);
      oacc = MFMA32(vf1, __builtin_bit_cast(bf16x8, pp[1]), oacc);
    }
  } else
  {
    f32x16 s[2];
    bf16x8 kf = *(const bf16x8*)(krd);
    s[0] = MFMA32(kf, qf, negm);
#pragma unroll
    for (int kt = 0; kt < 9; ++kt) {
      const bf16x8 vf0 = *(const bf16x8*)(smem + va0);
      const bf16x8 vf1 = *(const bf16x8*)(smem + va1);
      va0 += vstep;
      va1 += vstep;
      if (kt < 8) {
        kf = *(const bf16x8*)(krd + (kt + 1) * 1024);
        s[(kt + 1) & 1] = MFMA32(kf, qf, negm);
      }
      f32x16& c = s[kt & 1];
      mask(kt, c);
      if (!FIRST || kt >= kt0) {
        u32x4 pp[2];
#pragma unroll
        for (int e = 0; e < 16; e += 2) {
          pp[e >> 3][(e >> 1) & 3] = pack2(__builtin_amdgcn_exp2f(c[e]), __builtin_amdgcn_exp2f(c[e + 1]));
        }
        oacc = MFMA32(vf0, __builtin_bit_cast(bf16x8, pp[0]), oacc);
        oacc = MFMA32(vf1, __builtin_bit_cast(bf16x8, pp[1]), oacc);
      }
    }
  }
  STAMP(1, 3);
  const float inv = 1.f / oacc[8];
  u32x2 v0 = {pack2(oacc[0] * inv, oacc[1] * inv), pack2(oacc[2] * inv, oacc[3] * inv)};
  u32x2 v1 = {pack2(oacc[4] * inv, oacc[5] * inv), pack2(oacc[6] * inv, oacc[7] * inv)};
  const rsrc_t rs = ws_rsrc(ws);
  {
    const auto sx = __builtin_amdgcn_permlane32_swap(v0[0], v1[0], false, false);
    const auto sy = __builtin_amdgcn_permlane32_swap(v0[1], v1[1], false, false);
    const u32x4 ov = {sx[0], sy[0], sx[1], sy[1]};
    wst16<AUX_ST2>(rs, WS_O + (unsigned)((((q0 >> 5) * 32 + head) * 32 + r) * 16 + 8 * hh) * 2, ov);
  }
  STAMP(1, 4);
}

DI void conv_block(const u16* __restrict__ ssmk, const u16* __restrict__ ssmD, char* __restrict__ ws, char* smem,
                   int d) {
  const int t = threadIdx.x & 255;
  const int l = t & 63, wv = t >> 6;
  const int r = l & 31, hh = l >> 5;
  char* hs = smem;
  char* kvs = smem + CV_HBYTES;
  const u16* kvT = (const u16*)(ws + WS_KVT) + (size_t)d * SEQ;
  const u16* hrow = ssmk + (size_t)d * SEQ;
  STAMP(1, 0);
  const u32x4 kvv = ld16<AUX_LD2 != 0>(kvT + 8 * t);
  const u32x4 whi = *(const u32x4*)(hrow + 8 * t);
  const u32x4 wlo0 = *(const u32x4*)(hrow + 8 * (t > 0 ? t - 1 : 0));
  const float Dd = bf2f(ssmD[d]);
  *(u32x4*)(kvs + ((t >> 2) + 32) * CV_KVSTR + (t & 3) * 16) = kvv;
  if (t < 128) {
    u32x4 z = {0u, 0u, 0u, 0u};
    *(u32x4*)(kvs + (t >> 2) * CV_KVSTR + (t & 3) * 16) = z;
  }
  {
    const int E = t;
    u32x4 wlo = wlo0;
    if (E == 0) wlo = (u32x4){0u, 0u, 0u, 0u};
    unsigned z[8];
#pragma unroll
    for (int i = 0; i < 4; ++i) {
      const unsigned a = whi[3 - i];
      z[i] = (a >> 16) | (a << 16);
      const unsigned b = wlo[3 - i];
      z[4 + i] = (b >> 16) | (b << 16);
    }
    const int M = 255 - E;
#pragma unroll
    for (int p = 0; p < 8; ++p) {
      const int o = 7 - p;
      u32x4 v;
      if ((o & 1) == 0) {
        v[0] = z[o / 2];
        v[1] = z[o / 2 + 1];
        v[2] = z[o / 2 + 2];
        v[3] = z[o / 2 + 3];
      } else {
        v[0] = (z[o / 2] >> 16) | (z[o / 2 + 1] << 16);
        v[1] = (z[o / 2 + 1] >> 16) | (z[o / 2 + 2] << 16);
        v[2] = (z[o / 2 + 2] >> 16) | (z[o / 2 + 3] << 16);
        v[3] = (z[o / 2 + 3] >> 16) | (z[o / 2 + 4] << 16);
      }
      *(u32x4*)(hs + p * CV_HSTR + 16 * M) = v;
    }
    if (t < 24) {
      u32x4 zz = {0u, 0u, 0u, 0u};
      *(u32x4*)(hs + (t & 7) * CV_HSTR + 16 * (256 + (t >> 3))) = zz;
    }
  }
  __syncthreads();
  STAMP(1, 1);
  f32x16 acc0, acc1, acc2;
#pragma unroll
  for (int e = 0; e < 16; ++e) {
    acc0[e] = 0.f;
    acc1[e] = 0.f;
    acc2[e] = 0.f;
  }
  const char* abase = hs + (r & 7) * CV_HSTR + 16 * (255 - (r >> 3) + hh) - 64 * wv;
  const char* bbase = kvs + (r + 32 - wv) * CV_KVSTR + 16 * hh;
  {
    bf16x8 fa[2][2], fb[2][2];
    auto ldc = [&](int step, bf16x8(&a)[2], bf16x8(&b)[2]) {
      const int i = step >> 1, ks = step & 1;
      a[0] = *(const bf16x8*)(abase - 256 * i + 32 * ks);
      a[1] = *(const bf16x8*)(abase - 256 * i - 2048 + 32 * ks);
      b[1] = *(const bf16x8*)(bbase + (32 - 4 * i) * CV_KVSTR + 32 * ks);
      b[0] = *(const bf16x8*)(bbase - (4 * i) * CV_KVSTR + 32 * ks);
    };
    ldc(0, fa[0], fb[0]);
#pragma unroll
    for (int step = 0; step < 16; ++step) {
      if (step < 15) ldc(step + 1, fa[(step + 1) & 1], fb[(step + 1) & 1]);
      __builtin_amdgcn_sched_barrier(0);
      acc1 = MFMA32(fa[step & 1][0], fb[step & 1][1], acc1);
      acc0 = MFMA32(fa[step & 1][0], fb[step & 1][0], acc0);
      acc2 = MFMA32(fa[step & 1][1], fb[step & 1][0], acc2);
      __builtin_amdgcn_sched_barrier(0);
    }
  }
  acc1 += acc2;
  STAMP(1, 2);
  __syncthreads();
  STAMP(1, 6);
  float* red = (float*)smem;
  {
    float* rw = red + wv * (64 * CV_REDSTR);
#pragma unroll
    for (int g = 0; g < 4; ++g) {
      f32x4 v0 = {acc0[4 * g], acc0[4 * g + 1], acc0[4 * g + 2], acc0[4 * g + 3]};
      f32x4 v1 = {acc1[4 * g], acc1[4 * g + 1], acc1[4 * g + 2], acc1[4 * g + 3]};
      *(f32x4*)(rw + r * CV_REDSTR + 8 * g + 4 * hh) = v0;
      *(f32x4*)(rw + (32 + r) * CV_REDSTR + 8 * g + 4 * hh) = v1;
    }
  }
  __syncthreads();
  STAMP(1, 7);
  {
    const int a = t >> 2, b0 = 8 * (t & 3);
    f32x4 s0 = {0.f, 0.f, 0.f, 0.f}, s1 = {0.f, 0.f, 0.f, 0.f};
#pragma unroll
    for (int w = 0; w < 4; ++w) {
      const float* rp = red + w * (64 * CV_REDSTR) + a * CV_REDSTR + b0;
      s0 += *(const f32x4*)(rp);
      s1 += *(const f32x4*)(rp + 4);
    }
    float yy[8] = {s0[0], s0[1], s0[2], s0[3], s1[0], s1[1], s1[2], s1[3]};
    u32x4 o;
#pragma unroll
    for (int i = 0; i < 4; ++i) {
      const float y0 = rbf(yy[2 * i]) + rbf(bflo(kvv[i]) * Dd);
      const float y1 = rbf(yy[2 * i + 1]) + rbf(bfhi(kvv[i]) * Dd);
      o[i] = pack2(y0, y1);
    }
    wst16<AUX_Y2T>(ws_rsrc(ws), WS_Y2T + (unsigned)(d * SEQ + 8 * t) * 2, o);
  }
  STAMP(1, 4);
}

__global__ __launch_bounds__(512, 4) void k2_kernel(const u16* __restrict__ ssmk, const u16* __restrict__ ssmD,
                                                    const u16* __restrict__ wo, char* __restrict__ ws) {
  __shared__ __attribute__((aligned(16))) char smem[2 * CV_SMEM];
  const int xcd = blockIdx.x & 7, bidx = (blockIdx.x >> 3) ^ 32;
  if (bidx >= 32) {
    const int half = threadIdx.x >> 8;
    conv_block(ssmk, ssmD, ws, smem + half * CV_SMEM, xcd * 64 + (bidx - 32) * 2 + half);
    const u32x4 pf0 = *(const u32x4*)(wo + (size_t)(bidx - 32) * 8192 + threadIdx.x * 8);
    const u32x4 pf1 = *(const u32x4*)(wo + (size_t)(bidx - 32) * 8192 + 4096 + threadIdx.x * 8);
    asm volatile("" ::"v"(pf0));
    asm volatile("" ::"v"(pf1));
  } else {
    const int head = 4 * xcd + (bidx & 3), win = bidx >> 2;
    if (win == 0)
      attn_block<true>(ws, smem, head, 0);
    else
      attn_block<false>(ws, smem, head, win * 256);
  }
}

#define K3_STAGE 32768
#define K3_CSTR 68
#define K3_YSTR 136

DI void k3_body(const u16* __restrict__ x, const u16* __restrict__ wo, const char* __restrict__ ws,
                u16* __restrict__ out, char* smem) {
  const int t = threadIdx.x;
  const int l = t & 63, wv = t >> 6;
  const int r = l & 31, hh = l >> 5;
  const int wm = wv & 1, wn = (wv >> 1) & 1, kh = wv >> 2;
  const int tm = (blockIdx.x & 7) * 4 + ((blockIdx.x >> 3) & 3), tn = blockIdx.x >> 5;
  const int m0 = tm * 64, n0 = tn * 64;
  const u16* o = (const u16*)(ws + WS_O);
  STAMP(2, 0);

  const u16* y2T = (const u16*)(ws + WS_Y2T);
  const float* gp = (const float*)(ws + WS_G);
  const int rr = t >> 3, ch = t & 7;
  const u32x4 yv = ld16<AUX_LD3 != 0>(y2T + (size_t)(n0 + rr) * SEQ + m0 + ch * 8);
  const u32x4 xq = ld16<false>(x + (size_t)(m0 + rr) * DM + n0 + ch * 8);
  const float g = gp[m0 + rr];
  __builtin_amdgcn_sched_barrier(0);
  {
    char* ldsw = smem + wv * 2048 + l * 16;
#pragma unroll
    for (int s4 = 0; s4 < 4; ++s4) {
      const int st = s4;
#pragma unroll
      for (int i = 0; i < 2; ++i) {
        const int ar = 8 * wv + 4 * i + (l >> 4);
        GLDS16A(o + (size_t)((((m0 >> 5) + i) * 32 + st * 8 + wv) * 512 + (l >> 1) * 16 + (((l & 1) ^ ((l >> 4) & 1)) * 8)),
                ldsw + st * K3_STAGE + i * 1024, AUX_LD3);
      }
#pragma unroll
      for (int i = 0; i < 2; ++i) {
        const int br = 16 * wv + 8 * i + (l >> 3);
        GLDS16(wo + (size_t)(st * 128 + br) * DM + n0 + (((l & 7) ^ (((br >> 1) & 1) << 2)) * 8),
               ldsw + st * K3_STAGE + 16384 + i * 1024);
      }
    }
  }

  f32x16 acc;
#pragma unroll
  for (int e = 0; e < 16; ++e) acc[e] = 0.f;
  const int blk = (l >> 4) & 1, q4 = (l & 15) >> 2, p4 = l & 3;
  const int arow = wm * 32 + r;
  const int a_off = arow * 256;
  const int a_sw = arow & 15;
  const int b_off = 16384 + (8 * hh + q4) * 128 + (((wn * 4 + 2 * blk + (p4 >> 1)) ^ (((q4 >> 1) & 1) << 2)) * 16) +
                    (p4 & 1) * 8;
  const int a_x = (8 * kh + hh) ^ a_sw;
  const int b_k = b_off + 64 * kh * 128;
#pragma unroll
  for (int st = 0; st < 4; ++st) {
    if (st == 0) WAIT_V(12);
    else if (st == 1) WAIT_V(8);
    else if (st == 2) WAIT_V(4);
    else WAIT_V(0);
    RAWBAR();
    const char* sb = smem + st * K3_STAGE;
    bf16x8 af[4], bfr[4];
#pragma unroll
    for (int ks = 0; ks < 4; ++ks) {
      af[ks] = *(const bf16x8*)(sb + (4 * kh + ks) * 2048 + wm * 1024 + r * 32 + ((hh ^ ((r >> 3) & 1)) * 16));
      s16x4 lo = tr_read(sb + b_k + (16 * ks) * 128);
      s16x4 hi = tr_read(sb + b_k + (16 * ks + 4) * 128);
      bfr[ks] = cat_frag(lo, hi);
    }
    __builtin_amdgcn_sched_barrier(0);
#pragma unroll
    for (int ks = 0; ks < 4; ++ks) acc = MFMA32(af[ks], bfr[ks], acc);
    __builtin_amdgcn_sched_barrier(0);
  }
  __syncthreads();
  STAMP(2, 5);
  float* Cs = (float*)smem;
  char* Ys = smem + 2 * 64 * K3_CSTR * 4;
#pragma unroll
  for (int e = 0; e < 16; ++e) {
    const int row = wm * 32 + (e & 3) + 8 * (e >> 2) + 4 * hh;
    Cs[kh * 64 * K3_CSTR + row * K3_CSTR + wn * 32 + r] = acc[e];
  }
  {
    u32x2 a = {yv[0], yv[1]}, b = {yv[2], yv[3]};
    *(u32x2*)(Ys + rr * K3_YSTR + ch * 16) = a;
    *(u32x2*)(Ys + rr * K3_YSTR + ch * 16 + 8) = b;
  }
  __syncthreads();
  {
    const int row = rr;
    const int s = m0 + row;
    const float omg = rbf(1.f - g);
    const f32x4 c0 = *(const f32x4*)(Cs + row * K3_CSTR + ch * 8) + *(const f32x4*)(Cs + (64 + row) * K3_CSTR + ch * 8);
    const f32x4 c1 =
        *(const f32x4*)(Cs + row * K3_CSTR + ch * 8 + 4) + *(const f32x4*)(Cs + (64 + row) * K3_CSTR + ch * 8 + 4);
    float bb[8] = {c0[0], c0[1], c0[2], c0[3], c1[0], c1[1], c1[2], c1[3]};
    float res[8];
#pragma unroll
    for (int j = 0; j < 8; ++j) {
      const float y2 = bf2f(*(const u16*)(Ys + (ch * 8 + j) * K3_YSTR + row * 2));
      const float qv = (j & 1) ? bfhi(xq[j >> 1]) : bflo(xq[j >> 1]);
      const float a = rbf(qv * y2);
      const float ga = rbf(g * a);
      const float gb = rbf(omg * rbf(bb[j]));
      res[j] = ga + gb;
    }
    u32x4 ov;
    ov[0] = pack2(res[0], res[1]);
    ov[1] = pack2(res[2], res[3]);
    ov[2] = pack2(res[4], res[5]);
    ov[3] = pack2(res[6], res[7]);
    const rsrc_t ro = __builtin_amdgcn_make_buffer_rsrc((void*)out, (short)0, SEQ * DM * 2, 0x00020000);
    wst16<AUX_WT>(ro, (unsigned)(s * DM + n0 + ch * 8) * 2, ov);
  }
  STAMP(2, 6);
}

__global__ __launch_bounds__(512) void k3_kernel(const u16* __restrict__ x, const u16* __restrict__ wo,
                                                 const char* __restrict__ ws, u16* __restrict__ out) {
  __shared__ __attribute__((aligned(16))) char smem[4 * K3_STAGE];
  k3_body(x, wo, ws, out, smem);
}

extern "C" void kernel_launch(void* const* d_in, const int* in_sizes, int n_in, void* d_out, int out_size,
                              void* d_ws, size_t ws_size, hipStream_t stream) {
  const u16* A_q = (const u16*)d_in[0];
  const u16* A_k = (const u16*)d_in[1];
  const u16* A_v = (const u16*)d_in[2];
  const u16* A_shift = (const u16*)d_in[3];
  const u16* A_ssmk = (const u16*)d_in[4];
  const u16* A_ssmD = (const u16*)d_in[5];
  const u16* B_wqkv = (const u16*)d_in[7];
  const u16* B_wo = (const u16*)d_in[8];
  const u16* gate_w = (const u16*)d_in[9];
  char* ws = (char*)d_ws;
  k1_kernel<<<256, 512, 0, stream>>>(A_q, A_k, A_v, A_shift, B_wqkv, A_ssmk, gate_w, ws);
  k2_kernel<<<512, 512, 0, stream>>>(A_ssmk, A_ssmD, B_wo, ws);
  k3_kernel<<<256, 512, 0, stream>>>(A_q, B_wo, ws, (u16*)d_out);
}
```
